```python
import torch

def _softmax(x, axis=-1):
    return torch.softmax(x, dim=axis)
def _gelu(x):
    return torch.nn.functional.gelu(x, approximate="tanh")
def _silu(x):
    return torch.nn.functional.silu(x)
def _sigmoid(x):
    return torch.sigmoid(x)
def _layernorm(x, axis=-1, eps=1e-5):
    m = x.mean(dim=axis, keepdim=True)
    v = x.var(dim=axis, keepdim=True, unbiased=False)
    return (x - m) / torch.sqrt(v + eps)
def _scatter_add(tgt, idx, src, axis=0):
    return tgt.clone().index_add_(axis, idx, src)
def _segment_sum(src, seg, n):
    out = torch.zeros((n,) + src.shape[1:], dtype=src.dtype)
    return out.index_add_(0, seg, src)
def _pad(x, pw):
    flat = tuple(v for pair in reversed(pw) for v in pair)
    return torch.nn.functional.pad(x, flat)

kernel_name = 'sequential_preLN__softmax_full_mha__glu_gated_swiglu__prompt'

def setup_inputs():

    _w = (lambda a: (lambda t: t.to(torch.bfloat16) if t.is_floating_point() else t)(torch.as_tensor(a)))
    def _A_setup():
        B, S, D = 1, 2048, 640
        return {
            "x": _w((lambda *s: torch.randn(*s, dtype=torch.bfloat16) * 0.05)(B, S, D)),
            "wqkv": _w((lambda *s: torch.randn(*s, dtype=torch.bfloat16) * 0.05)(D, 3 * D)),
            "wo": _w((lambda *s: torch.randn(*s, dtype=torch.bfloat16) * 0.05)(D, D)),
        }
    _A_in = _A_setup()

    def _B_setup():
        B, S, D, F = 1, 2048, 640, 1728
        return {
            "x": _w((lambda *s: torch.randn(*s, dtype=torch.bfloat16) * 0.05)(B, S, D)),
            "w1": _w((lambda *s: torch.randn(*s, dtype=torch.bfloat16) * 0.4)(D, F) * (2.0 / D) ** 0.5),
            "w3": _w((lambda *s: torch.randn(*s, dtype=torch.bfloat16) * 0.4)(D, F) * (2.0 / D) ** 0.5),
            "w2": _w((lambda *s: torch.randn(*s, dtype=torch.bfloat16) * 0.4)(F, D) * (2.0 / F) ** 0.5),
        }
    _B_in = _B_setup()

    return {
        "A_x": _A_in["x"],
        "A_wqkv": _A_in["wqkv"],
        "A_wo": _A_in["wo"],
        "B_x": _B_in["x"],
        "B_w1": _B_in["w1"],
        "B_w3": _B_in["w3"],
        "B_w2": _B_in["w2"],
    }

def reference(A_x, A_wqkv, A_wo, B_x, B_w1, B_w3, B_w2):
    def _A_ref(x, wqkv, wo):
        B, S, D = x.shape
        H = 32; Dh = D // H
        qkv = (lambda x, *d: x.permute(*d))((x @ wqkv).reshape(B, S, 3, H, Dh), 2, 0, 3, 1, 4)
        q, k, v = qkv[0], qkv[1], qkv[2]
        scale = Dh ** -0.5
        scores = torch.einsum("bhqd,bhkd->bhqk", q, k) * scale
        p = _softmax(scores, axis=-1)
        o = torch.einsum("bhqk,bhkd->bhqd", p, v)
        return (lambda x, *d: x.permute(*d))(o, 0, 2, 1, 3).reshape(B, S, D) @ wo

    def _B_ref(x, w1, w3, w2):
        gate = torch.einsum("bsd,df->bsf", x, w1)
        up = torch.einsum("bsd,df->bsf", x, w3)
        h = _silu(gate) * up
        out = torch.einsum("bsf,fd->bsd", h, w2)
        return out

    x = A_x
    h = x + _A_ref(_layernorm(x, axis=-1), A_wqkv, A_wo)
    h2 = h + _B_ref(_layernorm(h, axis=-1), B_w1, B_w3, B_w2)
    return h2
```

```cpp
#include <hip/hip_runtime.h>
#include <hip/hip_bf16.h>
#include <stdint.h>

namespace blk {

constexpr int S = 2048, D = 640, H = 32, F = 1728;
constexpr float LN_EPS = 1e-5f;
constexpr float QSCALE = 0.22360679774997896f * 1.4426950408889634f;

#define LAS __attribute__((address_space(3)))
#define GAS __attribute__((address_space(1)))
#define DI __device__ __forceinline__
typedef short s16x8 __attribute__((ext_vector_type(8)));
typedef short s16x4 __attribute__((ext_vector_type(4)));
typedef float f32x16 __attribute__((ext_vector_type(16)));
typedef float f32x4 __attribute__((ext_vector_type(4)));
typedef float f32x2 __attribute__((ext_vector_type(2)));
typedef unsigned u32x4 __attribute__((ext_vector_type(4)));
typedef unsigned u32x2 __attribute__((ext_vector_type(2)));
typedef __bf16 bf16x2 __attribute__((ext_vector_type(2)));
typedef LAS char* lptr;
typedef LAS const char* lcptr;
typedef __amdgpu_buffer_rsrc_t rsrc_t;

constexpr unsigned WS_E = 0;
constexpr unsigned WS_FLAG = 4096;
constexpr unsigned WS_Q = 1u << 20;
constexpr unsigned WS_KV = 4u << 20;
constexpr unsigned WS_O = 12u << 20;
constexpr unsigned WS_H = 16u << 20;
constexpr unsigned WS_HST = 20u << 20;
constexpr unsigned WS_CSP = 21u << 20;
constexpr unsigned WS_HM = 24u << 20;
constexpr unsigned WS_W13P = 32u << 20;
constexpr unsigned WS_W2P = 40u << 20;
constexpr unsigned WS_WOP = 44u << 20;
constexpr unsigned WS_END = 48u << 20;
enum { FL_KV = 0, FL_PK = 1, FL_O = 2, FL_H = 3, FL_M = 4 };
constexpr int LDS_BYTES = 163840;
constexpr unsigned SPIN_CAP = 1u << 16;

#define STAMP(ws, kid, pt) do { } while (0)
#define ABL 0
#define SBAR() __builtin_amdgcn_sched_barrier(0)
#define PIN(x) asm volatile("" : "+v"(x))
#define MFMA32(a, b, c) __builtin_amdgcn_mfma_f32_32x32x16_bf16(a, b, c, 0, 0, 0)
#define MFMA32K8(a, b, c) __builtin_amdgcn_mfma_f32_32x32x8bf16_1k(a, b, c, 0, 0, 0)
#define MFMA16(a, b, c) __builtin_amdgcn_mfma_f32_16x16x32_bf16(a, b, c, 0, 0, 0)
#define WAIT_BAR(N) asm volatile("s_waitcnt vmcnt(%0) lgkmcnt(0)\n\ts_barrier" :: "i"(N) : "memory")
#define WAIT_VM(N) asm volatile("s_waitcnt vmcnt(%0)" :: "i"(N) : "memory")
#define LDS_BAR() asm volatile("s_waitcnt lgkmcnt(0)\n\ts_barrier" ::: "memory")

#define SCON true
template <bool SC> DI void dma16(const void* sbase, unsigned voff, unsigned ldsaddr) {
    if (SC && SCON) asm volatile("s_mov_b32 m0, %2\n\ts_nop 0\n\tglobal_load_lds_dwordx4 %0, %1 sc1" :: "v"(voff), "s"(sbase), "s"(ldsaddr) : "memory");
    else asm volatile("s_mov_b32 m0, %2\n\ts_nop 0\n\tglobal_load_lds_dwordx4 %0, %1" :: "v"(voff), "s"(sbase), "s"(ldsaddr) : "memory");
}
template <bool SC> DI void dma16m(const void* sbase, unsigned voff, unsigned ldsaddr, unsigned long long mask) {
    if (SC && SCON) asm volatile("s_mov_b32 m0, %2\n\ts_mov_b64 exec, %3\n\tglobal_load_lds_dwordx4 %0, %1 sc1\n\ts_mov_b64 exec, -1" :: "v"(voff), "s"(sbase), "s"(ldsaddr), "s"(mask) : "memory");
    else asm volatile("s_mov_b32 m0, %2\n\ts_mov_b64 exec, %3\n\tglobal_load_lds_dwordx4 %0, %1\n\ts_mov_b64 exec, -1" :: "v"(voff), "s"(sbase), "s"(ldsaddr), "s"(mask) : "memory");
}
constexpr int imin(int a, int b) { return a < b ? a : b; }
constexpr int imax(int a, int b) { return a > b ? a : b; }
constexpr int fly(int s, int R, int NS) { return imax(0, imin(R - 3, NS - 2 - s)); }
DI unsigned cvtpk(float lo, float hi) { f32x2 v = {lo, hi}; return __builtin_bit_cast(unsigned, __builtin_convertvector(v, bf16x2)); }
DI float bflo(unsigned w) { return __uint_as_float(w << 16); }
DI float bfhi(unsigned w) { return __uint_as_float(w & 0xffff0000u); }
DI s16x8 ld128(lcptr p) { return *(const LAS s16x8*)p; }
DI s16x4 ld64(lcptr p) { return *(const LAS s16x4*)p; }
DI s16x4 ldtr(lcptr p) { return __builtin_bit_cast(s16x4, __builtin_amdgcn_ds_read_tr16_b64_v4i16((LAS s16x4*)p)); }
DI s16x8 cat(s16x4 a, s16x4 b) { return __builtin_shufflevector(a, b, 0, 1, 2, 3, 4, 5, 6, 7); }
DI f32x4 sel4(unsigned m, f32x4 a, f32x4 b) { const u32x4 x = __builtin_bit_cast(u32x4, a), y = __builtin_bit_cast(u32x4, b); return __builtin_bit_cast(f32x4, (x & m) | (y & ~m)); }
DI float swap32_add(float v) { auto r = __builtin_amdgcn_permlane32_swap(__float_as_uint(v), __float_as_uint(v), false, false); return __uint_as_float(r[0]) + __uint_as_float(r[1]); }
DI float swap32_max(float v) { auto r = __builtin_amdgcn_permlane32_swap(__float_as_uint(v), __float_as_uint(v), false, false); return fmaxf(__uint_as_float(r[0]), __uint_as_float(r[1])); }
DI float upper_half(float v) { auto r = __builtin_amdgcn_permlane32_swap(__float_as_uint(v), __float_as_uint(v), false, false); return __uint_as_float(r[1]); }
DI void st128(rsrc_t rs, unsigned off, u32x4 v) { __builtin_amdgcn_raw_buffer_store_b128(v, rs, off, 0, 16); }
DI void st64(rsrc_t rs, unsigned off, u32x2 v) { __builtin_amdgcn_raw_buffer_store_b64(v, rs, off, 0, 16); }
DI void st32(rsrc_t rs, unsigned off, unsigned v) { __builtin_amdgcn_raw_buffer_store_b32(v, rs, off, 0, 16); }
DI u32x4 lg128(rsrc_t rs, unsigned off) { return __builtin_amdgcn_raw_buffer_load_b128(rs, off, 0, 16); }
DI u32x2 lg64(rsrc_t rs, unsigned off) { return __builtin_amdgcn_raw_buffer_load_b64(rs, off, 0, 16); }
DI unsigned lg32(rsrc_t rs, unsigned off) { return __builtin_amdgcn_raw_buffer_load_b32(rs, off, 0, 16); }

struct Ctx { const char *x, *wqkv, *wo, *w1, *w3, *w2; char *out, *ws; rsrc_t rs; lptr lds; unsigned lds0, tag; int b, lane, wid; };

DI unsigned flag_off(int kind, int wg) { return WS_FLAG + (unsigned)(kind * 256 + wg) * 128u; }
DI void publish(const Ctx& c, int kind) {
    asm volatile("s_barrier" ::: "memory");
    if (threadIdx.x == 0) st32(c.rs, flag_off(kind, c.b), c.tag);
}
template <int N> DI void wait_flags(const Ctx& c, const unsigned (&off)[N]) {
    if (c.wid == 0) {
        for (unsigned spins = 0; spins < SPIN_CAP; ++spins) {
            unsigned v[N];
#pragma unroll
            for (int i = 0; i < N; ++i) v[i] = lg32(c.rs, off[i]);
            bool ok = true;
#pragma unroll
            for (int i = 0; i < N; ++i) ok &= v[i] == c.tag;
            if (__all(ok)) break;
            __builtin_amdgcn_s_sleep(1);
        }
    }
    asm volatile("s_waitcnt vmcnt(0) lgkmcnt(0)\n\ts_barrier" ::: "memory");
}
DI int wg_p1(int pb, int nb) { return ((pb >> 3) | ((nb >> 2) << 1)) + 8 * ((pb & 7) + 8 * (nb & 3)); }
DI int wg_p2(int hh, int qb) { return (hh >> 2) + 8 * ((hh & 3) + 4 * qb); }
DI int wg_p3(int pp, int cc) { return (((pp >> 3) << 1) | (cc >> 2)) + 8 * ((pp & 7) + 8 * (cc & 3)); }
DI int wg_p4(int qb, int fc) { return ((qb >> 3) | ((fc >> 2) << 1)) + 8 * ((qb & 7) + 8 * (fc & 3)); }

constexpr int P1_R = 5, P1_SLOT = 32768, P1_NS = 10;
constexpr int P1_STG = 264;
constexpr int P1_ST = 40960;
constexpr int P1_CS = P1_ST + 1024;

template <int UP>
DI void p1_loop(const Ctx& c, unsigned xoff, unsigned woff, int tt, f32x16& acc0, f32x16& acc1, f32x16& accx) {
    const int lane = c.lane, wid = c.wid, r = lane & 31, h = lane >> 5, q = (lane >> 2) & 3, p = lane & 3, cg = (lane >> 4) & 1;
    constexpr unsigned long long WMASK = ~((1ull << 15) | (1ull << 27) | (1ull << 39) | (1ull << 51));
    const unsigned xdst = c.lds0 + wid * 2048, wdst = c.lds0 + 16384 + wid * 2048;
    const int xrow = 32 * tt + r;
    const unsigned xa = xrow * 128 + ((h ^ ((xrow >> 1) & 7)) * 16);
    const unsigned wk = 16384 + (8 * h + q) * 256 + (p & 1) * 8;
    const unsigned wa0 = wk + (((4 * (2 * UP) + 2 * cg + (p >> 1)) ^ (4 * q)) * 16);
    const unsigned wa1 = wk + (((4 * (2 * UP + 1) + 2 * cg + (p >> 1)) ^ (4 * q)) * 16);
    const unsigned waz = wk + (((4 * tt + 2 * cg + (p >> 1)) ^ (4 * q)) * 16);
    const s16x8 ones = {0x3f80, 0x3f80, 0x3f80, 0x3f80, 0x3f80, 0x3f80, 0x3f80, 0x3f80};
#define P1_ISSUE(st, slot) do { if (ABL & 1) break; dma16<false>(c.x + (st) * 128, xoff, xdst + (slot)); dma16<false>(c.x + (st) * 128 + 8 * 1280, xoff ^ 64, xdst + (slot) + 1024); \
        const char* w_ = c.wqkv + (size_t)(st) * (64 * 3840); dma16m<false>(w_, woff, wdst + (slot), WMASK); dma16m<false>(w_ + 4 * 3840, woff, wdst + (slot) + 1024, WMASK); } while (0)
#define P1_READ(X, W0, W1, WZ, slot, ks) do { if ((ABL & 2) && ((slot) != 0 || (ks) > 1)) break; lcptr b_ = (lcptr)c.lds + (slot); \
        X = ld128(b_ + (xa ^ ((ks) * 32))); \
        W0 = cat(ldtr(b_ + wa0 + (ks) * 4096), ldtr(b_ + wa0 + (ks) * 4096 + 1024)); \
        W1 = cat(ldtr(b_ + wa1 + (ks) * 4096), ldtr(b_ + wa1 + (ks) * 4096 + 1024)); \
        if (UP) WZ = cat(ldtr(b_ + waz + (ks) * 4096), ldtr(b_ + waz + (ks) * 4096 + 1024)); } while (0)
#define P1_MMA(X, W0, W1, WZ) do { if (ABL & 4) { PIN(X); PIN(W0); PIN(W1); if (UP) PIN(WZ); break; } acc0 = MFMA32(W0, X, acc0); acc1 = MFMA32(W1, X, acc1); \
        if (UP) accx = MFMA32(WZ, ones, accx); else accx = MFMA32(X, X, accx); } while (0)
#pragma unroll
    for (int s = 0; s < P1_R - 1; ++s) P1_ISSUE(s, s * P1_SLOT);
    WAIT_BAR(4 * (P1_R - 2));
    STAMP(c.ws, 2, 1);
    s16x8 x0, w00, w01, z0 = {}, x1, w10, w11, z1 = {};
    P1_READ(x0, w00, w01, z0, 0, 0);
#pragma unroll
    for (int s = 0; s < P1_NS; ++s) {
        const int cur = (s % P1_R) * P1_SLOT, nxt = ((s + 1) % P1_R) * P1_SLOT;
        WAIT_BAR(4 * fly(s, P1_R, P1_NS));
        SBAR();
        if (s + P1_R - 1 < P1_NS) P1_ISSUE(s + P1_R - 1, ((s + P1_R - 1) % P1_R) * P1_SLOT);
        P1_READ(x1, w10, w11, z1, cur, 1); SBAR(); P1_MMA(x0, w00, w01, z0); SBAR();
        P1_READ(x0, w00, w01, z0, cur, 2); SBAR(); P1_MMA(x1, w10, w11, z1); SBAR();
        P1_READ(x1, w10, w11, z1, cur, 3); SBAR(); P1_MMA(x0, w00, w01, z0); SBAR();
        if (s + 1 < P1_NS) P1_READ(x0, w00, w01, z0, nxt, 0);
        SBAR(); P1_MMA(x1, w10, w11, z1); SBAR();
    }
#undef P1_ISSUE
#undef P1_READ
#undef P1_MMA
}

DI void phase1(const Ctx& c) {
    const int lane = c.lane, wid = c.wid, tid = threadIdx.x, g = c.b & 7, idx = c.b >> 3;
    const int pb = 8 * (g & 1) + (idx & 7), nb = 4 * (g >> 1) + (idx >> 3);
    const int tt = wid & 3, up = wid >> 2, r = lane & 31, h = lane >> 5;
    lptr lds = c.lds;
    if (tid < P1_R * 64) { const int sl = tid >> 6, k = tid & 63;
        *(LAS u32x4*)(lds + sl * P1_SLOT + 16384 + k * 256 + ((15 ^ (4 * (k & 3))) * 16)) = (u32x4){0x3f803f80u, 0x3f803f80u, 0x3f803f80u, 0x3f803f80u}; }
    const int xr = 16 * wid + (lane >> 3), xc = (lane & 7) ^ ((xr >> 1) & 7);
    const unsigned xoff = (pb * 128 + xr) * 1280 + xc * 16;
    const int wkk = 8 * wid + (lane >> 4), wp = (lane & 15) ^ (4 * (wkk & 3)), wpc = wp < 15 ? wp : 14;
    const unsigned woff = wkk * 3840 + nb * 240 + wpc * 16;
    f32x16 acc0 = {}, acc1 = {}, accx = {};
    STAMP(c.ws, 2, 0);
    if (up == 0) p1_loop<0>(c, xoff, woff, tt, acc0, acc1, accx);
    else p1_loop<1>(c, xoff, woff, tt, acc0, acc1, accx);
    STAMP(c.ws, 2, 2);
    LDS_BAR();
    LAS float* st = (LAS float*)(lds + P1_ST); LAS float* cs = (LAS float*)(lds + P1_CS);
    if (up == 0) {
        const int tr = (r & 3) + 4 * (r >> 3); float v = 0.f;
#pragma unroll
        for (int i = 0; i < 16; ++i) v = (tr == i) ? accx[i] : v;
        if (h == ((r >> 2) & 1)) st[(tt * 32 + r) * 2 + 1] = v;
    } else {
        if (h == 0) st[(tt * 32 + r) * 2] = acc1[12];
        if (r == 0) {
#pragma unroll
            for (int gq = 0; gq < 4; ++gq) *(LAS f32x4*)(cs + tt * 32 + 8 * gq + 4 * h) = (f32x4){accx[4 * gq], accx[4 * gq + 1], accx[4 * gq + 2], accx[4 * gq + 3]};
        }
    }
    LDS_BAR();
    STAMP(c.ws, 2, 3);
    { const f32x2 sv = *(const LAS f32x2*)(st + (tt * 32 + r) * 2);
      const float mean = sv.x * (1.0f / D), var = fmaxf(sv.y * (1.0f / D) - mean * mean, 0.f), rstd = __builtin_amdgcn_rsqf(var + LN_EPS), rm = rstd * mean;
      lptr stg = lds + (32 * tt + r) * P1_STG;
#pragma unroll
      for (int u = 0; u < 2; ++u)
#pragma unroll
        for (int gq = 0; gq < 4; ++gq) {
            const int col = (2 * up + u) * 32 + 8 * gq + 4 * h;
            const f32x4 c4 = *(const LAS f32x4*)(cs + col);
            const float sc = (nb * 120 + col < D) ? QSCALE : 1.0f, a = rstd * sc, bb = rm * sc;
            float v[4];
#pragma unroll
            for (int e = 0; e < 4; ++e) v[e] = a * (u ? acc1[4 * gq + e] : acc0[4 * gq + e]) - bb * c4[e];
            *(LAS u32x2*)(stg + col * 2) = (u32x2){cvtpk(v[0], v[1]), cvtpk(v[2], v[3])};
        }
    }
    LDS_BAR();
    STAMP(c.ws, 2, 4);
#pragma unroll
    for (int j = 0; j < 3; ++j) {
        const int slot = up + 2 * j, gidx = 6 * nb + slot, type = gidx >> 5, head = gidx & 31, tile = 4 * pb + tt;
        if (type < 2) {
            lcptr row = (lcptr)lds + (32 * tt + r) * P1_STG + slot * 40;
            const u32x2 a0 = *(const LAS u32x2*)(row + 16 * h), a1 = *(const LAS u32x2*)(row + 16 * h + 8), a2 = *(const LAS u32x2*)(row + 32);
            const unsigned dst = type ? WS_KV + (unsigned)(head * 64 + tile) * 3072 : WS_Q + (unsigned)(head * 64 + tile) * 1536;
            st128(c.rs, dst + lane * 16, (u32x4){a0.x, a0.y, a1.x, a1.y});
            st64(c.rs, dst + 1024 + lane * 8, h ? (u32x2){type ? 0x00003f80u : 0u, 0u} : a2);
        } else {
            const int q = (lane >> 2) & 3, p = lane & 3, cg = (lane >> 4) & 1;
            lcptr vb = (lcptr)lds + (32 * tt + 4 * h + q) * P1_STG + slot * 40 + (cg * 4 + p < 5 ? (cg * 4 + p) * 8 : 0);
            const unsigned dst = WS_KV + (unsigned)(head * 64 + tile) * 3072 + 1536;
#pragma unroll
            for (int s = 0; s < 2; ++s) {
                const u32x2 a = __builtin_bit_cast(u32x2, ldtr(vb + (16 * s) * P1_STG)), e = __builtin_bit_cast(u32x2, ldtr(vb + (16 * s + 8) * P1_STG));
                u32x4 v = {a.x, a.y, e.x, e.y};
                if (r == 20) v = (u32x4){0x3f803f80u, 0x3f803f80u, 0x3f803f80u, 0x3f803f80u};
                if (r > 20) v = (u32x4){0u, 0u, 0u, 0u};
                if (r < 24) st128(c.rs, dst + s * 768 + h * 384 + r * 16, v);
            }
        }
    }
}

constexpr int PK_OFF = 65536;
DI int pk_job(int b, int i) { const int j = b + 256 * i; return j < 910 ? j : j - 256; }
DI void repack_issue(const Ctx& c) {
    const int row = 8 * c.wid + (c.lane >> 3), pc = c.lane & 7;
#pragma unroll
    for (int i = 0; i < 4; ++i) {
        const int j = pk_job(c.b, i);
        const char* src; unsigned off;
        if (j < 540) { const int jj = j < 270 ? j : j - 270, kb = jj / 27, cb = jj % 27; src = j < 270 ? c.w1 : c.w3; off = (64 * kb + row) * 3456 + cb * 128 + pc * 16; }
        else if (j < 810) { const int jj = j - 540, kb = jj / 10, cb = jj % 10; src = c.w2; off = (64 * kb + row) * 1280 + cb * 128 + pc * 16; }
        else { const int jj = j - 810, kb = jj / 10, cb = jj % 10, kp = 64 * kb + row, G = kp >> 3, e = kp & 7;
            const int k = G < 64 ? 20 * (G >> 1) + 8 * (G & 1) + e : 40 * (G - 64) + 16 + (e < 4 ? e : e + 16);
            src = c.wo; off = k * 1280 + cb * 128 + pc * 16; }
        dma16<false>(src, off, c.lds0 + PK_OFF + i * 8192 + c.wid * 1024);
    }
}
DI void repack_store(const Ctx& c) {
    const int lane = c.lane, w = c.wid, g = lane >> 4, i16 = lane & 15, q = i16 >> 2, p = i16 & 3;
#pragma unroll
    for (int i = 0; i < 4; ++i) {
        const int j = pk_job(c.b, i);
        lcptr blk = (lcptr)c.lds + PK_OFF + i * 8192 + (w >> 1) * 32 + p * 8;
        if (j < 540) {
            const int mat = j >= 270, jj = mat ? j - 270 : j, kb = jj / 27, cb = jj % 27, ks = 2 * (w & 1) + (g >> 1), hh = g & 1;
            lcptr a = blk + (16 * ks + 8 * hh + q) * 128;
            const u32x2 v0 = __builtin_bit_cast(u32x2, ldtr(a)), v1 = __builtin_bit_cast(u32x2, ldtr(a + 512));
            st128(c.rs, WS_W13P + (unsigned)((4 * cb + (w >> 1)) * 40 + 4 * kb + ks) * 1024 + (mat * 16 + i16 + 32 * hh) * 16, (u32x4){v0.x, v0.y, v1.x, v1.y});
            float s = ((bflo(v0.x) + bfhi(v0.x)) + (bflo(v0.y) + bfhi(v0.y))) + ((bflo(v1.x) + bfhi(v1.x)) + (bflo(v1.y) + bfhi(v1.y)));
            s += __shfl_xor(s, 16); s += __shfl_xor(s, 32);
            if (lane < 16) st32(c.rs, WS_CSP + (unsigned)((mat * 20 + 2 * kb + (w & 1)) * F + 64 * cb + 16 * (w >> 1) + i16) * 4, __float_as_uint(s));
        } else {
            const bool isw2 = j < 810; const int jj = isw2 ? j - 540 : j - 810, kb = jj / 10, cb = jj % 10, ks = w & 1;
            lcptr a = blk + (32 * ks + 8 * g + q) * 128;
            const u32x2 v0 = __builtin_bit_cast(u32x2, ldtr(a)), v1 = __builtin_bit_cast(u32x2, ldtr(a + 512));
            const unsigned dst = isw2 ? WS_W2P + (unsigned)((4 * cb + (w >> 1)) * 54 + 2 * kb + ks) * 1024 : WS_WOP + (unsigned)((4 * cb + (w >> 1)) * 20 + 2 * kb + ks) * 1024;
            st128(c.rs, dst + lane * 16, (u32x4){v0.x, v0.y, v1.x, v1.y});
        }
    }
}

constexpr int P2_R = 4, P2_SLOT = 12288, P2_NS = 16;
constexpr int P2_Q = P2_R * P2_SLOT;
constexpr int P2_EX = P2_Q + 8 * 1536;

DI void attn_safe(const Ctx& c, unsigned kv, s16x8 q0, s16x4 q1z, f32x16& o) {
    const int lane = c.lane, r = lane & 31, h = lane >> 5, vr = r < 24 ? r : r - 16;
    float m = -INFINITY; o = (f32x16){};
    for (int t = 0; t < 64; ++t) {
        const unsigned p = kv + t * 3072;
        const u32x4 k0 = lg128(c.rs, p + lane * 16); const u32x2 k1 = lg64(c.rs, p + 1024 + lane * 8);
        const u32x4 v0 = lg128(c.rs, p + 1536 + h * 384 + vr * 16), v1 = lg128(c.rs, p + 1536 + 768 + h * 384 + vr * 16);
        f32x16 s = MFMA32(__builtin_bit_cast(s16x8, k0), q0, (f32x16){}); s = MFMA32K8(__builtin_bit_cast(s16x4, k1), q1z, s);
        float tm = s[0];
#pragma unroll
        for (int i = 1; i < 16; ++i) tm = fmaxf(tm, s[i]);
        tm = swap32_max(tm);
        const float mn = fmaxf(m, tm), al = __builtin_amdgcn_exp2f(m - mn); m = mn;
#pragma unroll
        for (int i = 0; i < 16; ++i) { o[i] *= al; s[i] = __builtin_amdgcn_exp2f(s[i] - mn); }
        const u32x4 p0 = {cvtpk(s[0], s[1]), cvtpk(s[2], s[3]), cvtpk(s[4], s[5]), cvtpk(s[6], s[7])};
        const u32x4 p1 = {cvtpk(s[8], s[9]), cvtpk(s[10], s[11]), cvtpk(s[12], s[13]), cvtpk(s[14], s[15])};
        o = MFMA32(__builtin_bit_cast(s16x8, v0), __builtin_bit_cast(s16x8, p0), o); o = MFMA32(__builtin_bit_cast(s16x8, v1), __builtin_bit_cast(s16x8, p1), o);
    }
}

DI void phase2(const Ctx& c) {
    const int lane = c.lane, wid = c.wid, g = c.b & 7, idx = c.b >> 3, hh = 4 * g + (idx & 3), qb = idx >> 2;
    const int qg = wid & 3, kh = wid >> 2, r = lane & 31, h = lane >> 5;
    lptr lds = c.lds;
    const char* kvsrc = c.ws + WS_KV + (size_t)hh * 64 * 3072 + wid * 1536;
    const unsigned dst0 = c.lds0 + wid * 1536, voff = lane * 16;
#define P2_ISSUE(st, slot) do { const char* s_ = kvsrc + (size_t)(st) * 12288; dma16<true>(s_, voff, dst0 + (slot)); dma16m<true>(s_ + 1024, voff, dst0 + (slot) + 1024, 0xffffffffull); } while (0)
    { const char* qsrc = c.ws + WS_Q + ((size_t)hh * 64 + 8 * qb + wid) * 1536;
      dma16<true>(qsrc, voff, c.lds0 + P2_Q + wid * 1536); dma16m<true>(qsrc + 1024, voff, c.lds0 + P2_Q + wid * 1536 + 1024, 0xffffffffull); }
#pragma unroll
    for (int s = 0; s < P2_R - 1; ++s) P2_ISSUE(s, s * P2_SLOT);
    const int vr = r < 24 ? r : r - 16;
    const unsigned ka = 2 * kh * 3072 + lane * 16, kb = 2 * kh * 3072 + 1024 + lane * 8, va = 2 * kh * 3072 + 1536 + h * 384 + vr * 16;
#define LDK0(slot, t) ld128((lcptr)lds + (slot) + ka + (t) * 3072)
#define LDK1(slot, t) ld64((lcptr)lds + (slot) + kb + (t) * 3072)
#define LDV0(slot, t) ld128((lcptr)lds + (slot) + va + (t) * 3072)
#define LDV1(slot, t) ld128((lcptr)lds + (slot) + va + (t) * 3072 + 768)
    WAIT_BAR(2 * (P2_R - 2));
    STAMP(c.ws, 0, 3);
    s16x8 q0[2]; s16x4 q1z[2], q1[2];
#pragma unroll
    for (int i = 0; i < 2; ++i) { q0[i] = ld128((lcptr)lds + P2_Q + (2 * qg + i) * 1536 + lane * 16); q1z[i] = ld64((lcptr)lds + P2_Q + (2 * qg + i) * 1536 + 1024 + lane * 8); }
    { const s16x8 k0 = ld128((lcptr)lds + lane * 16); const s16x4 k1 = ld64((lcptr)lds + 1024 + lane * 8);
#pragma unroll
      for (int i = 0; i < 2; ++i) { f32x16 s = MFMA32(k0, q0[i], (f32x16){}); s = MFMA32K8(k1, q1z[i], s);
          float m = s[0];
#pragma unroll
          for (int e = 1; e < 16; ++e) m = fmaxf(m, s[e]);
          m = swap32_max(m);
          q1[i] = q1z[i]; if (h) q1[i][0] = (short)(cvtpk(-m, 0.f) & 0xffffu); } }
    s16x8 kA0 = LDK0(0, 0), kB0, vA0 = LDV0(0, 0), vA1 = LDV1(0, 0), vB0, vB1 = vA1; s16x4 kA1 = LDK1(0, 0), kB1;
    f32x16 sA, sB, o[2] = {(f32x16){}, (f32x16){}};
    sA = MFMA32(kA0, q0[0], (f32x16){}); sA = MFMA32K8(kA1, q1[0], sA);
    u32x4 p0, p1 = {0u, 0u, 0u, 0u};
    int cur = 0, nxt = P2_SLOT, fre = (P2_R - 1) * P2_SLOT;
#define EX(v) __builtin_amdgcn_exp2f(v)
#define P2_STEP(C, N, K0, K1, NQ, OC, V0, OP, VP, LOADS, LAST) do { \
        OP = MFMA32(VP, __builtin_bit_cast(s16x8, p1), OP); SBAR(); \
        LOADS; SBAR(); \
        C[0] = EX(C[0]); C[1] = EX(C[1]); C[2] = EX(C[2]); C[3] = EX(C[3]); p0[0] = cvtpk(C[0], C[1]); p0[1] = cvtpk(C[2], C[3]); PIN(p0); SBAR(); \
        if (!(LAST)) N = MFMA32(K0, q0[NQ], (f32x16){}); SBAR(); \
        C[4] = EX(C[4]); C[5] = EX(C[5]); C[6] = EX(C[6]); C[7] = EX(C[7]); p0[2] = cvtpk(C[4], C[5]); p0[3] = cvtpk(C[6], C[7]); PIN(p0); SBAR(); \
        if (!(LAST)) N = MFMA32K8(K1, q1[NQ], N); SBAR(); \
        C[8] = EX(C[8]); C[9] = EX(C[9]); C[10] = EX(C[10]); C[11] = EX(C[11]); p1[0] = cvtpk(C[8], C[9]); p1[1] = cvtpk(C[10], C[11]); PIN(p1); SBAR(); \
        OC = MFMA32(V0, __builtin_bit_cast(s16x8, p0), OC); SBAR(); \
        C[12] = EX(C[12]); C[13] = EX(C[13]); C[14] = EX(C[14]); C[15] = EX(C[15]); p1[2] = cvtpk(C[12], C[13]); p1[3] = cvtpk(C[14], C[15]); PIN(p1); SBAR(); \
    } while (0)
#define P2_STAGE(LAST) do { \
        P2_STEP(sA, sB, kA0, kA1, 1, o[0], vA0, o[1], vB1, { kB0 = LDK0(cur, 1); kB1 = LDK1(cur, 1); vB0 = LDV0(cur, 1); vB1 = LDV1(cur, 1); }, false); \
        P2_STEP(sB, sA, kB0, kB1, 0, o[1], vA0, o[0], vA1, { }, false); \
        P2_STEP(sA, sB, kB0, kB1, 1, o[0], vB0, o[1], vA1, { if (!(LAST)) { kA0 = LDK0(nxt, 0); kA1 = LDK1(nxt, 0); vA0 = LDV0(nxt, 0); vA1 = LDV1(nxt, 0); } }, false); \
        P2_STEP(sB, sA, kA0, kA1, 0, o[1], vB0, o[0], vB1, { }, LAST); \
        fre = cur; cur = nxt; nxt = (nxt == (P2_R - 1) * P2_SLOT) ? 0 : nxt + P2_SLOT; } while (0)
    for (int s = 0; s < P2_NS - 3; ++s) { WAIT_BAR(2); P2_ISSUE(s + P2_R - 1, fre); P2_STAGE(false); }
    WAIT_BAR(2); P2_STAGE(false);
    WAIT_BAR(0); P2_STAGE(false);
    P2_STAGE(true);
    o[1] = MFMA32(vB1, __builtin_bit_cast(s16x8, p1), o[1]);
#undef P2_STEP
#undef P2_STAGE
#undef P2_ISSUE
    STAMP(c.ws, 0, 4);
    LAS f32x4* ex = (LAS f32x4*)(lds + P2_EX);
    { const unsigned m = kh ? 0xffffffffu : 0u;
#pragma unroll
      for (int i = 0; i < 3; ++i) { const f32x4 a = {o[0][4 * i], o[0][4 * i + 1], o[0][4 * i + 2], o[0][4 * i + 3]}, e = {o[1][4 * i], o[1][4 * i + 1], o[1][4 * i + 2], o[1][4 * i + 3]};
          ex[(wid * 3 + i) * 64 + lane] = sel4(m, a, e); }
      LDS_BAR();
      f32x16 of = {};
#pragma unroll
      for (int i = 0; i < 3; ++i) { const f32x4 a = {o[0][4 * i], o[0][4 * i + 1], o[0][4 * i + 2], o[0][4 * i + 3]}, e = {o[1][4 * i], o[1][4 * i + 1], o[1][4 * i + 2], o[1][4 * i + 3]};
          const f32x4 t = sel4(m, e, a) + ex[((wid ^ 4) * 3 + i) * 64 + lane];
          of[4 * i] = t[0]; of[4 * i + 1] = t[1]; of[4 * i + 2] = t[2]; of[4 * i + 3] = t[3]; }
      float l = upper_half(of[8]);
      bool bad = !(l < 1.8446744e19f);
#pragma unroll
      for (int i = 0; i < 12; ++i) bad |= !(fabsf(of[i]) < 3.0e38f);
      if (__any(bad)) {
          const s16x8 qq0 = ld128((lcptr)lds + P2_Q + (2 * qg + kh) * 1536 + lane * 16); const s16x4 qq1 = ld64((lcptr)lds + P2_Q + (2 * qg + kh) * 1536 + 1024 + lane * 8);
          attn_safe(c, WS_KV + (unsigned)hh * 64 * 3072, qq0, qq1, of); l = upper_half(of[8]); }
      const float il = __builtin_amdgcn_rcpf(l);
      const unsigned a0 = cvtpk(of[0] * il, of[1] * il), a1 = cvtpk(of[2] * il, of[3] * il), b0 = cvtpk(of[4] * il, of[5] * il), b1 = cvtpk(of[6] * il, of[7] * il);
      auto x0 = __builtin_amdgcn_permlane32_swap(a0, b0, false, false), x1 = __builtin_amdgcn_permlane32_swap(a1, b1, false, false);
      const unsigned t16 = 2 * (8 * qb + 2 * qg + kh) + (r >> 4);
      st128(c.rs, WS_O + (t16 * 80 + 2 * hh + h) * 256 + (r & 15) * 16, (u32x4){x0[0], x1[0], x0[1], x1[1]});
      if (h == 0) st64(c.rs, WS_O + (t16 * 80 + 64 + (hh >> 1)) * 256 + (r & 15) * 16 + (hh & 1) * 8, (u32x2){cvtpk(of[8] * il, of[9] * il), cvtpk(of[10] * il, of[11] * il)});
    }
#undef LDK0
#undef LDK1
#undef LDV0
#undef LDV1
#undef EX
}

constexpr int P3_R = 8, P3_SLOT = 18432;

template <int NKS, bool FIRST, int CG>
DI void p3_body(const Ctx& c, unsigned abase, unsigned wbase, int pp, int cc, u32x2 (&res)[3]) {
    constexpr int NS = NKS / 2, NG = NKS * 4, NCT = CG ? 2 : 3, CT0 = CG ? 3 : 0;
    const int lane = c.lane, wid = c.wid, kh = wid & 1, tt = (wid >> 1) & 1, r = lane & 15, gq = lane >> 4;
    lptr lds = c.lds;
    const unsigned adst = c.lds0 + wid * 1024, wdst = c.lds0 + 8192 + wid * 1280;
    const unsigned aoff = abase + ((unsigned)(4 * pp + (wid >> 1)) * NG + 4 * (wid & 1)) * 256 + lane * 16;
    const unsigned o0 = wid * 1280 + lane * 16, o1 = wid * 1280 + 1024 + (lane & 15) * 16;
    const unsigned woff0 = wbase + (unsigned)(5 * cc + (o0 >> 11)) * NKS * 1024 + (o0 & 2047), woff1 = wbase + (unsigned)(5 * cc + (o1 >> 11)) * NKS * 1024 + (o1 & 2047);
    const unsigned aa = ((2 * tt) * 8 + 4 * kh + gq) * 256 + r * 16, wa = 8192 + (CT0 * 2 + kh) * 1024 + lane * 16;
    const int tok = pp * 64 + 32 * tt + 16 * kh + r;
    if (FIRST) {
#pragma unroll
        for (int i = 0; i < NCT; ++i) res[i] = *(const GAS u32x2*)(c.x + (size_t)tok * 1280 + cc * 160 + (CT0 + i) * 32 + gq * 8);
    }
#define P3_ISSUE(st, slot) do { dma16<true>(c.ws + (st) * 2048, aoff, adst + (slot)); const char* w_ = c.ws + (st) * 2048; \
        dma16<true>(w_, woff0, wdst + (slot)); dma16m<true>(w_, woff1, wdst + (slot) + 1024, 0xffffull); } while (0)
#define P3_READ(A0, A1, W, slot) do { lcptr b_ = (lcptr)lds + (slot); A0 = ld128(b_ + aa); A1 = ld128(b_ + aa + 2048); \
        _Pragma("unroll") for (int i = 0; i < NCT; ++i) W[i] = ld128(b_ + wa + i * 2048); } while (0)
#define P3_MMA(A0, A1, W) do { _Pragma("unroll") for (int i = 0; i < NCT; ++i) { acc[0][i] = MFMA16(W[i], A0, acc[0][i]); acc[1][i] = MFMA16(W[i], A1, acc[1][i]); } } while (0)
    f32x4 acc[2][NCT];
#pragma unroll
    for (int i = 0; i < NCT; ++i) { acc[0][i] = (f32x4){}; acc[1][i] = (f32x4){}; }
#pragma unroll
    for (int s = 0; s < P3_R - 1; ++s) P3_ISSUE(s, s * P3_SLOT);
    WAIT_BAR(3 * (P3_R - 2));
    s16x8 a0[2], a1[2], w[2][NCT];
    P3_READ(a0[0], a1[0], w[0], 0);
#pragma unroll
    for (int s = 0; s < NS; ++s) {
        WAIT_BAR(3 * fly(s, P3_R, NS));
        SBAR();
        if (s + P3_R - 1 < NS) P3_ISSUE(s + P3_R - 1, ((s + P3_R - 1) % P3_R) * P3_SLOT);
        if (s + 1 < NS) P3_READ(a0[(s + 1) & 1], a1[(s + 1) & 1], w[(s + 1) & 1], ((s + 1) % P3_R) * P3_SLOT);
        SBAR();
        P3_MMA(a0[s & 1], a1[s & 1], w[s & 1]);
        SBAR();
    }
#undef P3_ISSUE
#undef P3_READ
#undef P3_MMA
    LDS_BAR();
    LAS f32x4* red = (LAS f32x4*)lds;
    const unsigned khm = kh ? 0xffffffffu : 0u;
#pragma unroll
    for (int i = 0; i < NCT; ++i) red[(wid * 3 + i) * 64 + lane] = sel4(khm, acc[0][i], acc[1][i]);
    LDS_BAR();
    float s1 = 0.f, s2 = 0.f;
    const unsigned t16 = 4 * pp + 2 * tt + kh;
#pragma unroll
    for (int i = 0; i < NCT; ++i) {
        const f32x4 o = red[((wid ^ 1) * 3 + i) * 64 + lane], m = sel4(khm, acc[1][i], acc[0][i]);
        const unsigned w0 = cvtpk(bflo(res[i].x) + m[0] + o[0], bfhi(res[i].x) + m[1] + o[1]), w1 = cvtpk(bflo(res[i].y) + m[2] + o[2], bfhi(res[i].y) + m[3] + o[3]);
        if (FIRST) {
            res[i] = (u32x2){w0, w1};
            st64(c.rs, WS_H + (t16 * 80 + 10 * cc + 2 * (CT0 + i) + (gq >> 1)) * 256 + r * 16 + (gq & 1) * 8, res[i]);
            const float a = bflo(w0), b_ = bfhi(w0), c_ = bflo(w1), d = bfhi(w1); s1 += (a + b_) + (c_ + d); s2 += (a * a + b_ * b_) + (c_ * c_ + d * d);
        } else {
            *(GAS u32x2*)(c.out + (size_t)tok * 1280 + cc * 160 + (CT0 + i) * 32 + gq * 8) = (u32x2){w0, w1};
        }
    }
    if (FIRST) {
        s1 += __shfl_xor(s1, 16); s2 += __shfl_xor(s2, 16); s1 = swap32_add(s1); s2 = swap32_add(s2);
        LAS f32x2* ex = (LAS f32x2*)(lds + 32768);
        if (CG == 1 && gq == 0) ex[(wid & 3) * 16 + r] = (f32x2){s1, s2};
        LDS_BAR();
        if (CG == 0 && gq == 0) { const f32x2 e = ex[(wid & 3) * 16 + r];
            st64(c.rs, WS_HST + ((t16 * 8 + cc) * 16 + r) * 8, (u32x2){__float_as_uint(s1 + e.x), __float_as_uint(s2 + e.y)}); }
    }
}
template <int NKS, bool FIRST>
DI void phase_proj(const Ctx& c, unsigned abase, unsigned wbase, u32x2 (&res)[3]) {
    const int g = c.b & 7, idx = c.b >> 3, pp = 8 * (g >> 1) + (idx & 7), cc = 4 * (g & 1) + (idx >> 3);
    if (c.wid < 4) p3_body<NKS, FIRST, 0>(c, abase, wbase, pp, cc, res);
    else p3_body<NKS, FIRST, 1>(c, abase, wbase, pp, cc, res);
}

constexpr int P4_R = 7, P4_SLOT = 22528, P4_NS = 20;
constexpr int P4_CS = P4_R * P4_SLOT;

template <int UG>
DI void p4_body(const Ctx& c, int qb, int fc) {
    constexpr int NU = UG ? 3 : 4, U0 = UG ? 4 : 0;
    const int lane = c.lane, wid = c.wid, tt = wid & 3, r = lane & 31, h = lane >> 5;
    lptr lds = c.lds;
    const unsigned adst = c.lds0 + wid * 1024, wdst = c.lds0 + 8192 + wid * 1792;
    const unsigned aoff = WS_H + (unsigned)(8 * qb + wid) * 80 * 256 + lane * 16;
    const unsigned o0 = wid * 1792 + lane * 16, o1 = wid * 1792 + 1024 + (lane < 48 ? lane : 47) * 16;
    const unsigned woff0 = WS_W13P + (unsigned)imin(7 * fc + (o0 >> 11), 107) * 40960 + (o0 & 2047), woff1 = WS_W13P + (unsigned)imin(7 * fc + (o1 >> 11), 107) * 40960 + (o1 & 2047);
    const unsigned xa = ((2 * tt + (r >> 4)) * 4 + h) * 256 + (r & 15) * 16, wa = 8192 + U0 * 2048 + lane * 16;
    const unsigned t16 = 8 * qb + 2 * tt + (r >> 4);
    u32x2 hs[4];
#pragma unroll
    for (int i = 0; i < 4; ++i) hs[i] = lg64(c.rs, WS_HST + ((t16 * 8 + 4 * h + i) * 16 + (r & 15)) * 8);
#define P4_ISSUE(st, slot) do { dma16<true>(c.ws + (st) * 1024, aoff, adst + (slot)); const char* w_ = c.ws + (st) * 2048; \
        dma16<true>(w_, woff0, wdst + (slot)); dma16m<true>(w_, woff1, wdst + (slot) + 1024, 0xffffffffffffull); } while (0)
#define P4_READ(X, W, slot, ks) do { lcptr b_ = (lcptr)lds + (slot); X = ld128(b_ + xa + (ks) * 512); \
        _Pragma("unroll") for (int u = 0; u < NU; ++u) W[u] = ld128(b_ + wa + u * 2048 + (ks) * 1024); } while (0)
#define P4_MMA(X, W) do { _Pragma("unroll") for (int u = 0; u < NU; ++u) acc[u] = MFMA32(W[u], X, acc[u]); } while (0)
    f32x16 acc[NU];
#pragma unroll
    for (int u = 0; u < NU; ++u) acc[u] = (f32x16){};
#pragma unroll
    for (int s = 0; s < P4_R - 1; ++s) P4_ISSUE(s, s * P4_SLOT);
    WAIT_BAR(3 * (P4_R - 2));
    s16x8 xa_, wa_[NU], xb, wb_[NU];
    P4_READ(xa_, wa_, 0, 0);
#pragma unroll
    for (int s = 0; s < P4_NS; ++s) {
        WAIT_BAR(3 * fly(s, P4_R, P4_NS));
        SBAR();
        if (s + P4_R - 1 < P4_NS) P4_ISSUE(s + P4_R - 1, ((s + P4_R - 1) % P4_R) * P4_SLOT);
        P4_READ(xb, wb_, (s % P4_R) * P4_SLOT, 1);
        SBAR();
        P4_MMA(xa_, wa_);
        SBAR();
        if (s + 1 < P4_NS) P4_READ(xa_, wa_, ((s + 1) % P4_R) * P4_SLOT, 0);
        SBAR();
        P4_MMA(xb, wb_);
        SBAR();
    }
#undef P4_ISSUE
#undef P4_READ
#undef P4_MMA
    STAMP(c.ws, 1, 2);
    float s1 = (__uint_as_float(hs[0].x) + __uint_as_float(hs[1].x)) + (__uint_as_float(hs[2].x) + __uint_as_float(hs[3].x));
    float s2 = (__uint_as_float(hs[0].y) + __uint_as_float(hs[1].y)) + (__uint_as_float(hs[2].y) + __uint_as_float(hs[3].y));
    s1 = swap32_add(s1); s2 = swap32_add(s2);
    const float mean = s1 * (1.0f / D), var = fmaxf(s2 * (1.0f / D) - mean * mean, 0.f), rstd = __builtin_amdgcn_rsqf(var + LN_EPS), rm = rstd * mean;
    const LAS float* csl = (const LAS float*)(lds + P4_CS);
#pragma unroll
    for (int u = 0; u < NU; ++u) {
        if (7 * fc + U0 + u < 108) {
            u32x2 w[2];
#pragma unroll
            for (int gq = 0; gq < 2; ++gq) {
                const f32x4 cg_ = *(const LAS f32x4*)(csl + (U0 + u) * 16 + 8 * gq + 4 * h), cu = *(const LAS f32x4*)(csl + 128 + (U0 + u) * 16 + 8 * gq + 4 * h);
                float o[4];
#pragma unroll
                for (int e = 0; e < 4; ++e) { const float gt = rstd * acc[u][4 * gq + e] - rm * cg_[e], uq = rstd * acc[u][8 + 4 * gq + e] - rm * cu[e];
                    o[e] = gt * uq * __builtin_amdgcn_rcpf(1.0f + __builtin_amdgcn_exp2f(-1.4426950408889634f * gt)); }
                w[gq] = (u32x2){cvtpk(o[0], o[1]), cvtpk(o[2], o[3])};
            }
            auto x0 = __builtin_amdgcn_permlane32_swap(w[0].x, w[1].x, false, false), x1 = __builtin_amdgcn_permlane32_swap(w[0].y, w[1].y, false, false);
            st128(c.rs, WS_HM + (t16 * 216 + 14 * fc + 2 * (U0 + u) + h) * 256 + (r & 15) * 16, (u32x4){x0[0], x1[0], x0[1], x1[1]});
        }
    }
}
DI void phase4(const Ctx& c, int qb, int fc) {
    if (c.wid < 4) p4_body<0>(c, qb, fc); else p4_body<1>(c, qb, fc);
}
DI void p4_colsums(const Ctx& c, int fc) {
    const int tid = threadIdx.x;
    if (tid < 224) { const int mat = tid >= 112, cl = tid - 112 * mat, col = imin(fc * 112 + cl, F - 1);
        float v[20];
#pragma unroll
        for (int i = 0; i < 20; ++i) v[i] = __uint_as_float(lg32(c.rs, WS_CSP + (unsigned)((mat * 20 + i) * F + col) * 4));
        float s = 0.f;
#pragma unroll
        for (int i = 0; i < 20; ++i) s += v[i];
        *(LAS float*)(c.lds + P4_CS + (mat * 128 + cl) * 4) = s; }
}

template <int PH0, int PH1>
__global__ __launch_bounds__(512, 2) void block_fwd(const char* __restrict__ x, const char* __restrict__ wqkv, const char* __restrict__ wo, const char* __restrict__ w1,
                                                    const char* __restrict__ w3, const char* __restrict__ w2, char* __restrict__ out, char* __restrict__ ws) {
    extern __shared__ __attribute__((aligned(16))) char smem[];
    Ctx c; c.x = x; c.wqkv = wqkv; c.wo = wo; c.w1 = w1; c.w3 = w3; c.w2 = w2; c.out = out; c.ws = ws;
    c.rs = __builtin_amdgcn_make_buffer_rsrc(ws, 0, WS_END, 0x00020000);
    c.lds = (lptr)smem; c.lds0 = (unsigned)(uintptr_t)smem; c.b = blockIdx.x; c.lane = threadIdx.x & 63; c.wid = __builtin_amdgcn_readfirstlane(threadIdx.x >> 6);
    const int lane = c.lane, g = c.b & 7, idx = c.b >> 3;
    STAMP(ws, 0, 0);
    const unsigned e0 = lg32(c.rs, WS_E);
    u32x2 res[3] = {(u32x2){0u, 0u}, (u32x2){0u, 0u}, (u32x2){0u, 0u}};
    if (PH0 <= 0) {
        phase1(c);
        STAMP(c.ws, 2, 5);
        c.tag = __builtin_amdgcn_readfirstlane(e0) + 1u;
        STAMP(ws, 0, 1);
        LDS_BAR();
        repack_issue(c);
        WAIT_VM(4);
        publish(c, FL_KV);
        WAIT_BAR(0);
        repack_store(c);
        WAIT_VM(0);
        publish(c, FL_PK);
        STAMP(ws, 0, 2);
    } else c.tag = __builtin_amdgcn_readfirstlane(e0) + 1u;
    if (PH0 <= 1 && PH1 >= 1) {
        { const int hh = 4 * g + (idx & 3), qb = idx >> 2;
          const int wg = lane < 16 ? wg_p1(lane, (32 + hh) / 6) : lane < 32 ? wg_p1(lane - 16, (64 + hh) / 6) : wg_p1(2 * qb + (lane & 1), hh / 6);
          const unsigned off[1] = {flag_off(FL_KV, wg)}; wait_flags(c, off); }
        phase2(c);
        WAIT_VM(0);
        publish(c, FL_O);
        STAMP(ws, 0, 5);
    }
    if (PH0 <= 2 && PH1 >= 2) {
        const int pp = 8 * (g >> 1) + (idx & 7);
        { const unsigned off[5] = {flag_off(FL_O, wg_p2(lane & 31, pp >> 2)), flag_off(FL_PK, lane), flag_off(FL_PK, 64 + lane), flag_off(FL_PK, 128 + lane), flag_off(FL_PK, 192 + lane)};
          wait_flags(c, off); }
        STAMP(ws, 0, 6);
        phase_proj<20, true>(c, WS_O, WS_WOP, res);
        WAIT_VM(0);
        publish(c, FL_H);
        STAMP(ws, 0, 7);
    }
    if (PH0 <= 3 && PH1 >= 3) {
        const int qb = 8 * (g & 1) + (idx & 7), fc = 4 * (g >> 1) + (idx >> 3);
        p4_colsums(c, fc);
        { const unsigned off[1] = {flag_off(FL_H, wg_p3(2 * qb + (lane & 1), (lane >> 1) & 7))}; wait_flags(c, off); }
        STAMP(ws, 1, 1);
        phase4(c, qb, fc);
        WAIT_VM(0);
        publish(c, FL_M);
        STAMP(ws, 1, 3);
    }
    if (PH0 <= 4 && PH1 >= 4) {
        const int pp = 8 * (g >> 1) + (idx & 7);
        const unsigned fm = flag_off(FL_M, wg_p4(pp >> 1, lane & 15));
        if (c.b == 0) {
            const unsigned off[5] = {fm, flag_off(FL_O, lane), flag_off(FL_O, 64 + lane), flag_off(FL_O, 128 + lane), flag_off(FL_O, 192 + lane)}; wait_flags(c, off);
        } else { const unsigned off[1] = {fm}; wait_flags(c, off); }
        STAMP(ws, 1, 4);
        if (PH0 > 2) {
            const int cc = 4 * (g & 1) + (idx >> 3), kh = c.wid & 1, tt = (c.wid >> 1) & 1, ct0 = c.wid >> 2 ? 3 : 0, gq = lane >> 4;
#pragma unroll
            for (int i = 0; i < 3; ++i) res[i] = lg64(c.rs, WS_H + ((4 * pp + 2 * tt + kh) * 80 + 10 * cc + 2 * imin(ct0 + i, 4) + (gq >> 1)) * 256 + (lane & 15) * 16 + (gq & 1) * 8);
        }
        phase_proj<54, false>(c, WS_HM, WS_W2P, res);
        STAMP(ws, 1, 5);
        if (c.b == 0 && threadIdx.x == 0) st32(c.rs, WS_E, c.tag);
    }
}

}

extern "C" void kernel_launch(void* const* d_in, const int* in_sizes, int n_in, void* d_out, int out_size, void* d_ws, size_t ws_size, hipStream_t stream) {
    using namespace blk;
    const char* x = (const char*)d_in[0]; const char* wqkv = (const char*)d_in[1]; const char* wo = (const char*)d_in[2];
    const char* w1 = (const char*)d_in[4]; const char* w3 = (const char*)d_in[5]; const char* w2 = (const char*)d_in[6];
    char* ws = (char*)d_ws; char* out = (char*)d_out;
#define LAUNCH(A, B) do { hipFuncSetAttribute((const void*)block_fwd<A, B>, hipFuncAttributeMaxDynamicSharedMemorySize, LDS_BYTES); \
        block_fwd<A, B><<<256, 512, LDS_BYTES, stream>>>(x, wqkv, wo, w1, w3, w2, out, ws); } while (0)
    LAUNCH(0, 4);
#undef LAUNCH
}
```

```cpp
#include <hip/hip_runtime.h>
#include <hip/hip_bf16.h>
#include <stdint.h>

namespace blk {

constexpr int S = 2048, D = 640, H = 32, F = 1728;
constexpr float LN_EPS = 1e-5f;
constexpr float QSCALE = 0.22360679774997896f * 1.4426950408889634f;

#define LAS __attribute__((address_space(3)))
#define GAS __attribute__((address_space(1)))
#define DI __device__ __forceinline__
typedef short s16x8 __attribute__((ext_vector_type(8)));
typedef short s16x4 __attribute__((ext_vector_type(4)));
typedef float f32x16 __attribute__((ext_vector_type(16)));
typedef float f32x4 __attribute__((ext_vector_type(4)));
typedef float f32x2 __attribute__((ext_vector_type(2)));
typedef unsigned u32x4 __attribute__((ext_vector_type(4)));
typedef unsigned u32x2 __attribute__((ext_vector_type(2)));
typedef __bf16 bf16x2 __attribute__((ext_vector_type(2)));
typedef LAS char* lptr;
typedef LAS const char* lcptr;
typedef __amdgpu_buffer_rsrc_t rsrc_t;

constexpr unsigned WS_E = 0;
constexpr unsigned WS_FLAG = 4096;
constexpr unsigned WS_Q = 1u << 20;
constexpr unsigned WS_KV = 4u << 20;
constexpr unsigned WS_O = 12u << 20;
constexpr unsigned WS_H = 16u << 20;
constexpr unsigned WS_HST = 20u << 20;
constexpr unsigned WS_CSP = 21u << 20;
constexpr unsigned WS_HM = 24u << 20;
constexpr unsigned WS_W13P = 32u << 20;
constexpr unsigned WS_W2P = 40u << 20;
constexpr unsigned WS_WOP = 44u << 20;
constexpr unsigned WS_END = 48u << 20;
enum { FL_KV = 0, FL_PK = 1, FL_O = 2, FL_H = 3, FL_M = 4 };
constexpr int LDS_BYTES = 163840;
constexpr unsigned SPIN_CAP = 1u << 16;

#define STAMP(ws, kid, pt) do { } while (0)
#define STAMPW(ws, kid, pt, wv) do { } while (0)
#define ABL 0
#define SBAR() __builtin_amdgcn_sched_barrier(0)
#define PIN(x) asm volatile("" : "+v"(x))
#define MFMA32(a, b, c) __builtin_amdgcn_mfma_f32_32x32x16_bf16(a, b, c, 0, 0, 0)
#define MFMA32K8(a, b, c) __builtin_amdgcn_mfma_f32_32x32x8bf16_1k(a, b, c, 0, 0, 0)
#define MFMA16(a, b, c) __builtin_amdgcn_mfma_f32_16x16x32_bf16(a, b, c, 0, 0, 0)
#define WAIT_BAR(N) asm volatile("s_waitcnt vmcnt(%0) lgkmcnt(0)\n\ts_barrier" :: "i"(N) : "memory")
#define WAIT_VM(N) asm volatile("s_waitcnt vmcnt(%0)" :: "i"(N) : "memory")
#define LDS_BAR() asm volatile("s_waitcnt lgkmcnt(0)\n\ts_barrier" ::: "memory")

#define SCON true
template <bool SC> DI void dma16(const void* sbase, unsigned voff, unsigned ldsaddr) {
    if (SC && SCON) asm volatile("s_mov_b32 m0, %2\n\ts_nop 0\n\tglobal_load_lds_dwordx4 %0, %1 sc1" :: "v"(voff), "s"(sbase), "s"(ldsaddr) : "memory");
    else asm volatile("s_mov_b32 m0, %2\n\ts_nop 0\n\tglobal_load_lds_dwordx4 %0, %1" :: "v"(voff), "s"(sbase), "s"(ldsaddr) : "memory");
}
template <bool SC> DI void dma16m(const void* sbase, unsigned voff, unsigned ldsaddr, unsigned long long mask) {
    if (SC && SCON) asm volatile("s_mov_b32 m0, %2\n\ts_mov_b64 exec, %3\n\tglobal_load_lds_dwordx4 %0, %1 sc1\n\ts_mov_b64 exec, -1" :: "v"(voff), "s"(sbase), "s"(ldsaddr), "s"(mask) : "memory");
    else asm volatile("s_mov_b32 m0, %2\n\ts_mov_b64 exec, %3\n\tglobal_load_lds_dwordx4 %0, %1\n\ts_mov_b64 exec, -1" :: "v"(voff), "s"(sbase), "s"(ldsaddr), "s"(mask) : "memory");
}
DI void touch_lines(const void* sbase, unsigned voff, unsigned ldsaddr) {
    asm volatile("s_mov_b32 m0, %2\n\ts_nop 0\n\tglobal_load_lds_dword %0, %1" :: "v"(voff), "s"(sbase), "s"(ldsaddr) : "memory");
}
constexpr int imin(int a, int b) { return a < b ? a : b; }
constexpr int imax(int a, int b) { return a > b ? a : b; }
constexpr int fly(int s, int R, int NS) { return imax(0, imin(R - 3, NS - 2 - s)); }
constexpr int fly2(int s, int R, int NS, int na, int nw) {
    const int need = imin(s + 1, NS - 1);
    const int pro = imin(R - 1, NS);
    const int issued = pro * na + imax(0, imin(s, NS - R + 1)) * (na + nw);
    const int endpos = need < pro ? (need + 1) * na : pro * na + (need - pro + 1) * (na + nw);
    return imax(0, issued - endpos);
}
DI unsigned cvtpk(float lo, float hi) { f32x2 v = {lo, hi}; return __builtin_bit_cast(unsigned, __builtin_convertvector(v, bf16x2)); }
DI float bflo(unsigned w) { return __uint_as_float(w << 16); }
DI float bfhi(unsigned w) { return __uint_as_float(w & 0xffff0000u); }
DI s16x8 ld128(lcptr p) { return *(const LAS s16x8*)p; }
DI s16x4 ld64(lcptr p) { return *(const LAS s16x4*)p; }
DI s16x4 ldtr(lcptr p) { return __builtin_bit_cast(s16x4, __builtin_amdgcn_ds_read_tr16_b64_v4i16((LAS s16x4*)p)); }
DI s16x8 cat(s16x4 a, s16x4 b) { return __builtin_shufflevector(a, b, 0, 1, 2, 3, 4, 5, 6, 7); }
DI f32x4 sel4(unsigned m, f32x4 a, f32x4 b) { const u32x4 x = __builtin_bit_cast(u32x4, a), y = __builtin_bit_cast(u32x4, b); return __builtin_bit_cast(f32x4, (x & m) | (y & ~m)); }
DI float swap32_add(float v) { auto r = __builtin_amdgcn_permlane32_swap(__float_as_uint(v), __float_as_uint(v), false, false); return __uint_as_float(r[0]) + __uint_as_float(r[1]); }
DI float swap32_max(float v) { auto r = __builtin_amdgcn_permlane32_swap(__float_as_uint(v), __float_as_uint(v), false, false); return fmaxf(__uint_as_float(r[0]), __uint_as_float(r[1])); }
DI float upper_half(float v) { auto r = __builtin_amdgcn_permlane32_swap(__float_as_uint(v), __float_as_uint(v), false, false); return __uint_as_float(r[1]); }
DI void st128(rsrc_t rs, unsigned off, u32x4 v) { __builtin_amdgcn_raw_buffer_store_b128(v, rs, off, 0, 16); }
DI void st64(rsrc_t rs, unsigned off, u32x2 v) { __builtin_amdgcn_raw_buffer_store_b64(v, rs, off, 0, 16); }
DI void st32(rsrc_t rs, unsigned off, unsigned v) { __builtin_amdgcn_raw_buffer_store_b32(v, rs, off, 0, 16); }
DI u32x4 lg128(rsrc_t rs, unsigned off) { return __builtin_amdgcn_raw_buffer_load_b128(rs, off, 0, 16); }
DI u32x2 lg64(rsrc_t rs, unsigned off) { return __builtin_amdgcn_raw_buffer_load_b64(rs, off, 0, 16); }
DI unsigned lg32(rsrc_t rs, unsigned off) { return __builtin_amdgcn_raw_buffer_load_b32(rs, off, 0, 16); }

DI void ldg_async(s16x8& dst, const void* sbase, unsigned voff) { asm volatile("global_load_dwordx4 %0, %1, %2 sc1" : "=v"(dst) : "v"(voff), "s"(sbase) : "memory"); }
#define WAIT_USE(N, x) asm volatile("s_waitcnt vmcnt(%1)" : "+v"(x) : "i"(N) : "memory")
struct Ctx { const char *x, *wqkv, *wo, *w1, *w3, *w2; char *out, *ws; rsrc_t rs; lptr lds; unsigned lds0, tag; int b, lane, wid; };

DI unsigned flag_off(int kind, int wg) { return WS_FLAG + (unsigned)(kind * 256 + wg) * 128u; }
DI void publish(const Ctx& c, int kind) {
    asm volatile("s_barrier" ::: "memory");
    if (threadIdx.x == 0) st32(c.rs, flag_off(kind, c.b), c.tag);
}
template <int N> DI void wait_flags(const Ctx& c, const unsigned (&off)[N]) {
    if (c.wid == 0) {
        for (unsigned spins = 0; spins < SPIN_CAP; ++spins) {
            unsigned v[N];
#pragma unroll
            for (int i = 0; i < N; ++i) v[i] = lg32(c.rs, off[i]);
            bool ok = true;
#pragma unroll
            for (int i = 0; i < N; ++i) ok &= v[i] == c.tag;
            if (__all(ok)) break;
            __builtin_amdgcn_s_sleep(1);
        }
    }
    asm volatile("s_waitcnt lgkmcnt(0)\n\ts_barrier" ::: "memory");
}
DI int wg_p1(int pb, int nb) { return ((pb >> 3) | ((nb >> 2) << 1)) + 8 * ((pb & 7) + 8 * (nb & 3)); }
DI int wg_p2(int hh, int qb) { return (hh >> 2) + 8 * ((hh & 3) + 4 * qb); }
DI int wg_p3(int pp, int cc) { return (((pp >> 3) << 1) | (cc >> 2)) + 8 * ((pp & 7) + 8 * (cc & 3)); }
DI int wg_p4(int qb, int fc) { return ((qb >> 3) | ((fc >> 2) << 1)) + 8 * ((qb & 7) + 8 * (fc & 3)); }

constexpr int P1_R = 5, P1_SLOT = 32768, P1_NS = 10;
constexpr int P1_STG = 264;
constexpr int P1_OUT = 65536;
constexpr int P1_ST = P1_OUT + 40960;
constexpr int P1_CS = P1_ST + 1024;

DI void repack_issue(const Ctx& c);
template <int UP>
DI void p1_loop(const Ctx& c, unsigned xoff, unsigned woff, int tt, f32x16& acc0, f32x16& acc1, f32x16& accx) {
    const int lane = c.lane, wid = c.wid, r = lane & 31, h = lane >> 5, q = (lane >> 2) & 3, p = lane & 3, cg = (lane >> 4) & 1;
    constexpr unsigned long long WMASK = ~((1ull << 15) | (1ull << 27) | (1ull << 39) | (1ull << 51));
    const unsigned xdst = c.lds0 + wid * 2048, wdst = c.lds0 + 16384 + wid * 2048;
    const int xrow = 32 * tt + r;
    const unsigned xa = xrow * 128 + ((h ^ ((xrow >> 1) & 7)) * 16);
    const unsigned wk = 16384 + (8 * h + q) * 256 + (p & 1) * 8;
    const unsigned wa0 = wk + (((4 * (2 * UP) + 2 * cg + (p >> 1)) ^ (4 * q)) * 16);
    const unsigned wa1 = wk + (((4 * (2 * UP + 1) + 2 * cg + (p >> 1)) ^ (4 * q)) * 16);
    const unsigned waz = wk + (((4 * tt + 2 * cg + (p >> 1)) ^ (4 * q)) * 16);
    const s16x8 ones = {0x3f80, 0x3f80, 0x3f80, 0x3f80, 0x3f80, 0x3f80, 0x3f80, 0x3f80};
#define P1_ISSUE(st, slot) do { if (ABL & 1) break; dma16<false>(c.x + (st) * 128, xoff, xdst + (slot)); dma16<false>(c.x + (st) * 128 + 8 * 1280, xoff ^ 64, xdst + (slot) + 1024); \
        const char* w_ = c.wqkv + (size_t)(st) * (64 * 3840); dma16m<false>(w_, woff, wdst + (slot), WMASK); dma16m<false>(w_ + 4 * 3840, woff, wdst + (slot) + 1024, WMASK); } while (0)
#define P1_READ(X, W0, W1, WZ, slot, ks) do { if ((ABL & 2) && ((slot) != 0 || (ks) > 1)) break; lcptr b_ = (lcptr)c.lds + (slot); \
        X = ld128(b_ + (xa ^ ((ks) * 32))); \
        W0 = cat(ldtr(b_ + wa0 + (ks) * 4096), ldtr(b_ + wa0 + (ks) * 4096 + 1024)); \
        W1 = cat(ldtr(b_ + wa1 + (ks) * 4096), ldtr(b_ + wa1 + (ks) * 4096 + 1024)); \
        if (UP) WZ = cat(ldtr(b_ + waz + (ks) * 4096), ldtr(b_ + waz + (ks) * 4096 + 1024)); } while (0)
#define P1_MMA(X, W0, W1, WZ) do { if (ABL & 4) { PIN(X); PIN(W0); PIN(W1); if (UP) PIN(WZ); break; } acc0 = MFMA32(W0, X, acc0); acc1 = MFMA32(W1, X, acc1); \
        if (UP) accx = MFMA32(WZ, ones, accx); else accx = MFMA32(X, X, accx); } while (0)
    P1_ISSUE(0, 0); P1_ISSUE(1, P1_SLOT); P1_ISSUE(2, 2 * P1_SLOT);
    WAIT_BAR(8);
    STAMP(c.ws, 2, 1);
    P1_ISSUE(3, 3 * P1_SLOT);
    s16x8 x0, w00, w01, z0 = {}, x1, w10, w11, z1 = {};
    P1_READ(x0, w00, w01, z0, 0, 0);
#pragma unroll
    for (int s = 0; s < P1_NS; ++s) {
        const int cur = (s % P1_R) * P1_SLOT, nxt = ((s + 1) % P1_R) * P1_SLOT;
        constexpr int PKS = P1_NS - P1_R + 1;
        WAIT_BAR(4 * fly(s, P1_R, P1_NS) + (s > PKS ? 4 : 0));
        SBAR();
        if (s + P1_R - 1 < P1_NS) P1_ISSUE(s + P1_R - 1, ((s + P1_R - 1) % P1_R) * P1_SLOT);
        if (s == PKS) repack_issue(c);
        P1_READ(x1, w10, w11, z1, cur, 1); SBAR(); P1_MMA(x0, w00, w01, z0); SBAR();
        P1_READ(x0, w00, w01, z0, cur, 2); SBAR(); P1_MMA(x1, w10, w11, z1); SBAR();
        P1_READ(x1, w10, w11, z1, cur, 3); SBAR(); P1_MMA(x0, w00, w01, z0); SBAR();
        if (s + 1 < P1_NS) P1_READ(x0, w00, w01, z0, nxt, 0);
        SBAR(); P1_MMA(x1, w10, w11, z1); SBAR();
    }
#undef P1_ISSUE
#undef P1_READ
#undef P1_MMA
}

DI void phase1(const Ctx& c) {
    const int lane = c.lane, wid = c.wid, tid = threadIdx.x, g = c.b & 7, idx = c.b >> 3;
    const int pb = 8 * (g & 1) + (idx & 7), nb = 4 * (g >> 1) + (idx >> 3);
    const int tt = wid & 3, up = wid >> 2, r = lane & 31, h = lane >> 5;
    lptr lds = c.lds;
    if (tid < P1_R * 64) { const int sl = tid >> 6, k = tid & 63;
        *(LAS u32x4*)(lds + sl * P1_SLOT + 16384 + k * 256 + ((15 ^ (4 * (k & 3))) * 16)) = (u32x4){0x3f803f80u, 0x3f803f80u, 0x3f803f80u, 0x3f803f80u}; }
    const int xr = 16 * wid + (lane >> 3), xc = (lane & 7) ^ ((xr >> 1) & 7);
    const unsigned xoff = (pb * 128 + xr) * 1280 + xc * 16;
    const int wkk = 8 * wid + (lane >> 4), wp = (lane & 15) ^ (4 * (wkk & 3)), wpc = wp < 15 ? wp : 14;
    const unsigned woff = wkk * 3840 + nb * 240 + wpc * 16;
    f32x16 acc0 = {}, acc1 = {}, accx = {};
    STAMP(c.ws, 2, 0);
    if (up == 0) p1_loop<0>(c, xoff, woff, tt, acc0, acc1, accx);
    else p1_loop<1>(c, xoff, woff, tt, acc0, acc1, accx);
    STAMP(c.ws, 2, 2);
    LDS_BAR();
    LAS float* st = (LAS float*)(lds + P1_ST); LAS float* cs = (LAS float*)(lds + P1_CS);
    if (up == 0) {
        const int tr = (r & 3) + 4 * (r >> 3); float v = 0.f;
#pragma unroll
        for (int i = 0; i < 16; ++i) v = (tr == i) ? accx[i] : v;
        if (h == ((r >> 2) & 1)) st[(tt * 32 + r) * 2 + 1] = v;
    } else {
        if (h == 0) st[(tt * 32 + r) * 2] = acc1[12];
        if (r == 0) {
#pragma unroll
            for (int gq = 0; gq < 4; ++gq) *(LAS f32x4*)(cs + tt * 32 + 8 * gq + 4 * h) = (f32x4){accx[4 * gq], accx[4 * gq + 1], accx[4 * gq + 2], accx[4 * gq + 3]};
        }
    }
    LDS_BAR();
    STAMP(c.ws, 2, 3);
    { const f32x2 sv = *(const LAS f32x2*)(st + (tt * 32 + r) * 2);
      const float mean = sv.x * (1.0f / D), var = fmaxf(sv.y * (1.0f / D) - mean * mean, 0.f), rstd = __builtin_amdgcn_rsqf(var + LN_EPS), rm = rstd * mean;
      lptr stg = lds + P1_OUT + (32 * tt + r) * P1_STG;
#pragma unroll
      for (int u = 0; u < 2; ++u)
#pragma unroll
        for (int gq = 0; gq < 4; ++gq) {
            const int col = (2 * up + u) * 32 + 8 * gq + 4 * h;
            const f32x4 c4 = *(const LAS f32x4*)(cs + col);
            const float sc = (nb * 120 + col < D) ? QSCALE : 1.0f, a = rstd * sc, bb = rm * sc;
            float v[4];
#pragma unroll
            for (int e = 0; e < 4; ++e) v[e] = a * (u ? acc1[4 * gq + e] : acc0[4 * gq + e]) - bb * c4[e];
            *(LAS u32x2*)(stg + col * 2) = (u32x2){cvtpk(v[0], v[1]), cvtpk(v[2], v[3])};
        }
    }
    LDS_BAR();
    STAMP(c.ws, 2, 4);
#pragma unroll
    for (int j = 0; j < 3; ++j) {
        const int slot = up + 2 * j, gidx = 6 * nb + slot, type = gidx >> 5, head = gidx & 31, tile = 4 * pb + tt;
        if (type < 2) {
            lcptr row = (lcptr)lds + P1_OUT + (32 * tt + r) * P1_STG + slot * 40;
            const u32x2 a0 = *(const LAS u32x2*)(row + 16 * h), a1 = *(const LAS u32x2*)(row + 16 * h + 8), a2 = *(const LAS u32x2*)(row + 32);
            const unsigned dst = type ? WS_KV + (unsigned)(head * 64 + tile) * 3072 : WS_Q + (unsigned)(head * 64 + tile) * 1536;
            st128(c.rs, dst + lane * 16, (u32x4){a0.x, a0.y, a1.x, a1.y});
            st64(c.rs, dst + 1024 + lane * 8, h ? (u32x2){type ? 0x00003f80u : 0u, 0u} : a2);
        } else {
            const int q = (lane >> 2) & 3, p = lane & 3, cg = (lane >> 4) & 1;
            lcptr vb = (lcptr)lds + P1_OUT + (32 * tt + 4 * h + q) * P1_STG + slot * 40 + (cg * 4 + p < 5 ? (cg * 4 + p) * 8 : 0);
            const unsigned dst = WS_KV + (unsigned)(head * 64 + tile) * 3072 + 1536;
#pragma unroll
            for (int s = 0; s < 2; ++s) {
                const u32x2 a = __builtin_bit_cast(u32x2, ldtr(vb + (16 * s) * P1_STG)), e = __builtin_bit_cast(u32x2, ldtr(vb + (16 * s + 8) * P1_STG));
                u32x4 v = {a.x, a.y, e.x, e.y};
                if (r == 20) v = (u32x4){0x3f803f80u, 0x3f803f80u, 0x3f803f80u, 0x3f803f80u};
                if (r > 20) v = (u32x4){0u, 0u, 0u, 0u};
                if (r < 24) st128(c.rs, dst + s * 768 + h * 384 + r * 16, v);
            }
        }
    }
}

constexpr int PK_OFF = 0;
DI int pk_job(int b, int i) { const int j = b + 256 * i; return j < 910 ? j : j - 256; }
DI void repack_issue(const Ctx& c) {
    const int row = 8 * c.wid + (c.lane >> 3), pc = c.lane & 7;
#pragma unroll
    for (int i = 0; i < 4; ++i) {
        const int j = pk_job(c.b, i);
        const char* src; unsigned off;
        if (j < 540) { const int jj = j < 270 ? j : j - 270, kb = jj / 27, cb = jj % 27; src = j < 270 ? c.w1 : c.w3; off = (64 * kb + row) * 3456 + cb * 128 + pc * 16; }
        else if (j < 810) { const int jj = j - 540, kb = jj / 10, cb = jj % 10; src = c.w2; off = (64 * kb + row) * 1280 + cb * 128 + pc * 16; }
        else { const int jj = j - 810, kb = jj / 10, cb = jj % 10, kp = 64 * kb + row, G = kp >> 3, e = kp & 7;
            const int k = G < 64 ? 20 * (G >> 1) + 8 * (G & 1) + e : 40 * (G - 64) + 16 + (e < 4 ? e : e + 16);
            src = c.wo; off = k * 1280 + cb * 128 + pc * 16; }
        dma16<false>(src, off, c.lds0 + PK_OFF + i * 8192 + c.wid * 1024);
    }
}
DI void repack_store(const Ctx& c) {
    const int lane = c.lane, w = c.wid, g = lane >> 4, i16 = lane & 15, q = i16 >> 2, p = i16 & 3;
#pragma unroll
    for (int i = 0; i < 4; ++i) {
        const int j = pk_job(c.b, i);
        lcptr blk = (lcptr)c.lds + PK_OFF + i * 8192 + (w >> 1) * 32 + p * 8;
        if (j < 540) {
            const int mat = j >= 270, jj = mat ? j - 270 : j, kb = jj / 27, cb = jj % 27, ks = 2 * (w & 1) + (g >> 1), hh = g & 1;
            lcptr a = blk + (16 * ks + 8 * hh + q) * 128;
            const u32x2 v0 = __builtin_bit_cast(u32x2, ldtr(a)), v1 = __builtin_bit_cast(u32x2, ldtr(a + 512));
            st128(c.rs, WS_W13P + (unsigned)((4 * cb + (w >> 1)) * 40 + 4 * kb + ks) * 1024 + (mat * 16 + i16 + 32 * hh) * 16, (u32x4){v0.x, v0.y, v1.x, v1.y});
            float s = ((bflo(v0.x) + bfhi(v0.x)) + (bflo(v0.y) + bfhi(v0.y))) + ((bflo(v1.x) + bfhi(v1.x)) + (bflo(v1.y) + bfhi(v1.y)));
            s += __shfl_xor(s, 16); s += __shfl_xor(s, 32);
            if (lane < 16) st32(c.rs, WS_CSP + (unsigned)((mat * 20 + 2 * kb + (w & 1)) * F + 64 * cb + 16 * (w >> 1) + i16) * 4, __float_as_uint(s));
        } else {
            const bool isw2 = j < 810; const int jj = isw2 ? j - 540 : j - 810, kb = jj / 10, cb = jj % 10, ks = w & 1;
            lcptr a = blk + (32 * ks + 8 * g + q) * 128;
            const u32x2 v0 = __builtin_bit_cast(u32x2, ldtr(a)), v1 = __builtin_bit_cast(u32x2, ldtr(a + 512));
            const unsigned dst = isw2 ? WS_W2P + (unsigned)((4 * cb + (w >> 1)) * 54 + 2 * kb + ks) * 1024 : WS_WOP + (unsigned)((4 * cb + (w >> 1)) * 20 + 2 * kb + ks) * 1024;
            st128(c.rs, dst + lane * 16, (u32x4){v0.x, v0.y, v1.x, v1.y});
        }
    }
}

constexpr int P2_R = 4, P2_SLOT = 24576, P2_NS = 8;
constexpr int P2_Q = 110592;
constexpr int XCH = 135168;
constexpr int P2_EX = XCH;

DI void attn_safe(const Ctx& c, unsigned kv, s16x8 q0, s16x4 q1z, f32x16& o) {
    const int lane = c.lane, r = lane & 31, h = lane >> 5, vr = r < 24 ? r : r - 16;
    float m = -INFINITY; o = (f32x16){};
    for (int t = 0; t < 64; ++t) {
        const unsigned p = kv + t * 3072;
        const u32x4 k0 = lg128(c.rs, p + lane * 16); const u32x2 k1 = lg64(c.rs, p + 1024 + lane * 8);
        const u32x4 v0 = lg128(c.rs, p + 1536 + h * 384 + vr * 16), v1 = lg128(c.rs, p + 1536 + 768 + h * 384 + vr * 16);
        f32x16 s = MFMA32(__builtin_bit_cast(s16x8, k0), q0, (f32x16){}); s = MFMA32K8(__builtin_bit_cast(s16x4, k1), q1z, s);
        float tm = s[0];
#pragma unroll
        for (int i = 1; i < 16; ++i) tm = fmaxf(tm, s[i]);
        tm = swap32_max(tm);
        const float mn = fmaxf(m, tm), al = __builtin_amdgcn_exp2f(m - mn); m = mn;
#pragma unroll
        for (int i = 0; i < 16; ++i) { o[i] *= al; s[i] = __builtin_amdgcn_exp2f(s[i] - mn); }
        const u32x4 p0 = {cvtpk(s[0], s[1]), cvtpk(s[2], s[3]), cvtpk(s[4], s[5]), cvtpk(s[6], s[7])};
        const u32x4 p1 = {cvtpk(s[8], s[9]), cvtpk(s[10], s[11]), cvtpk(s[12], s[13]), cvtpk(s[14], s[15])};
        o = MFMA32(__builtin_bit_cast(s16x8, v0), __builtin_bit_cast(s16x8, p0), o); o = MFMA32(__builtin_bit_cast(s16x8, v1), __builtin_bit_cast(s16x8, p1), o);
    }
}

DI void phase2(const Ctx& c, bool pub_pk) {
    const int lane = c.lane, wid = c.wid, g = c.b & 7, idx = c.b >> 3, hh = 4 * g + (idx & 3), qb = idx >> 2;
    const int qg = wid & 3, kh = wid >> 2, r = lane & 31, h = lane >> 5;
    lptr lds = c.lds;
    const char* kvsrc = c.ws + WS_KV + (size_t)hh * 64 * 3072 + wid * 3072;
    const unsigned dst0 = c.lds0 + wid * 3072, voff = lane * 16;
#define P2_ISSUE(st, slot) do { const char* s_ = kvsrc + (size_t)(st) * 24576; dma16<true>(s_, voff, dst0 + (slot)); dma16<true>(s_ + 1024, voff, dst0 + (slot) + 1024); dma16<true>(s_ + 2048, voff, dst0 + (slot) + 2048); } while (0)
    { const char* qsrc = c.ws + WS_Q + ((size_t)hh * 64 + 8 * qb + wid) * 1536;
      dma16<true>(qsrc, voff, c.lds0 + P2_Q + wid * 1536); dma16m<true>(qsrc + 1024, voff, c.lds0 + P2_Q + wid * 1536 + 1024, 0xffffffffull); }
    P2_ISSUE(0, 0); P2_ISSUE(1, P2_SLOT);
    const int vr = r < 24 ? r : 21 + (r & 1);
    const unsigned ka = 4 * kh * 3072 + lane * 16, kb = 4 * kh * 3072 + 1024 + lane * 8, va = 4 * kh * 3072 + 1536 + h * 384 + vr * 16;
#define LDK0(slot, t) ld128((lcptr)lds + (slot) + ka + (t) * 3072)
#define LDK1(slot, t) ld64((lcptr)lds + (slot) + kb + (t) * 3072)
#define LDV0(slot, t) ld128((lcptr)lds + (slot) + va + (t) * 3072)
#define LDV1(slot, t) ld128((lcptr)lds + (slot) + va + (t) * 3072 + 768)
    WAIT_BAR(3);
    if (pub_pk && threadIdx.x == 0) st32(c.rs, flag_off(FL_PK, c.b), c.tag);
    STAMP(c.ws, 0, 3);
    P2_ISSUE(2, 2 * P2_SLOT);
    s16x8 q0[2]; s16x4 q1z[2], q1[2];
#pragma unroll
    for (int i = 0; i < 2; ++i) { q0[i] = ld128((lcptr)lds + P2_Q + (2 * qg + i) * 1536 + lane * 16); q1z[i] = ld64((lcptr)lds + P2_Q + (2 * qg + i) * 1536 + 1024 + lane * 8); }
    { const s16x8 k0 = ld128((lcptr)lds + lane * 16); const s16x4 k1 = ld64((lcptr)lds + 1024 + lane * 8);
#pragma unroll
      for (int i = 0; i < 2; ++i) { f32x16 s = MFMA32(k0, q0[i], (f32x16){}); s = MFMA32K8(k1, q1z[i], s);
          float m = s[0];
#pragma unroll
          for (int e = 1; e < 16; ++e) m = fmaxf(m, s[e]);
          m = swap32_max(m);
          q1[i] = q1z[i]; if (h) q1[i][0] = (short)(cvtpk(-m, 0.f) & 0xffffu); } }
    s16x8 kA0 = LDK0(0, 0), kB0, vA0 = LDV0(0, 0), vA1 = LDV1(0, 0), vB0, vB1 = vA1; s16x4 kA1 = LDK1(0, 0), kB1;
    f32x16 sA, sB, o[2] = {(f32x16){}, (f32x16){}};
    sA = MFMA32(kA0, q0[0], (f32x16){}); sA = MFMA32K8(kA1, q1[0], sA);
    u32x4 p0, p1 = {0u, 0u, 0u, 0u};
    int cur = 0, nxt = P2_SLOT, fre = (P2_R - 1) * P2_SLOT;
#define EX(v) __builtin_amdgcn_exp2f(v)
#define P2_STEP(C, N, K0, K1, NQ, OC, V0, OP, VP, LOADS, LAST) do { \
        OP = MFMA32(VP, __builtin_bit_cast(s16x8, p1), OP); SBAR(); \
        LOADS; SBAR(); \
        C[0] = EX(C[0]); C[1] = EX(C[1]); C[2] = EX(C[2]); C[3] = EX(C[3]); p0[0] = cvtpk(C[0], C[1]); p0[1] = cvtpk(C[2], C[3]); PIN(p0); SBAR(); \
        if (!(LAST)) N = MFMA32(K0, q0[NQ], (f32x16){}); SBAR(); \
        C[4] = EX(C[4]); C[5] = EX(C[5]); C[6] = EX(C[6]); C[7] = EX(C[7]); p0[2] = cvtpk(C[4], C[5]); p0[3] = cvtpk(C[6], C[7]); PIN(p0); SBAR(); \
        if (!(LAST)) N = MFMA32K8(K1, q1[NQ], N); SBAR(); \
        C[8] = EX(C[8]); C[9] = EX(C[9]); C[10] = EX(C[10]); C[11] = EX(C[11]); p1[0] = cvtpk(C[8], C[9]); p1[1] = cvtpk(C[10], C[11]); PIN(p1); SBAR(); \
        OC = MFMA32(V0, __builtin_bit_cast(s16x8, p0), OC); SBAR(); \
        C[12] = EX(C[12]); C[13] = EX(C[13]); C[14] = EX(C[14]); C[15] = EX(C[15]); p1[2] = cvtpk(C[12], C[13]); p1[3] = cvtpk(C[14], C[15]); PIN(p1); SBAR(); \
    } while (0)
#define P2_PAIR(T, NS_, NT, LAST) do { \
        P2_STEP(sA, sB, kA0, kA1, 1, o[0], vA0, o[1], vB1, { kB0 = LDK0(cur, (T) + 1); kB1 = LDK1(cur, (T) + 1); vB0 = LDV0(cur, (T) + 1); vB1 = LDV1(cur, (T) + 1); }, false); \
        P2_STEP(sB, sA, kB0, kB1, 0, o[1], vA0, o[0], vA1, { }, false); \
        P2_STEP(sA, sB, kB0, kB1, 1, o[0], vB0, o[1], vA1, { if (!(LAST)) { kA0 = LDK0(NS_, NT); kA1 = LDK1(NS_, NT); vA0 = LDV0(NS_, NT); vA1 = LDV1(NS_, NT); } }, false); \
        P2_STEP(sB, sA, kA0, kA1, 0, o[1], vB0, o[0], vB1, { }, LAST); } while (0)
#define P2_STAGE(LAST) do { P2_PAIR(0, cur, 2, false); P2_PAIR(2, nxt, 0, LAST); \
        fre = cur; cur = nxt; nxt = (nxt == (P2_R - 1) * P2_SLOT) ? 0 : nxt + P2_SLOT; } while (0)
    for (int s = 0; s < P2_NS - 3; ++s) { WAIT_BAR(3); P2_ISSUE(s + P2_R - 1, fre); P2_STAGE(false); }
    WAIT_BAR(3);
    unsigned pk[4];
    if (wid == 0) {
#pragma unroll
        for (int i = 0; i < 4; ++i) pk[i] = lg32(c.rs, flag_off(FL_PK, 64 * i + lane));
    }
    P2_STAGE(false);
    if (wid == 0) {
        for (unsigned spins = 0; spins < SPIN_CAP; ++spins) {
            if (__all(pk[0] == c.tag && pk[1] == c.tag && pk[2] == c.tag && pk[3] == c.tag)) break;
            __builtin_amdgcn_s_sleep(1);
#pragma unroll
            for (int i = 0; i < 4; ++i) pk[i] = lg32(c.rs, flag_off(FL_PK, 64 * i + lane));
        }
    }
    WAIT_BAR(0);
    P2_STAGE(false);
    P2_STAGE(true);
    o[1] = MFMA32(vB1, __builtin_bit_cast(s16x8, p1), o[1]);
#undef P2_STEP
#undef P2_PAIR
#undef P2_STAGE
#undef P2_ISSUE
    STAMP(c.ws, 0, 4);
    LAS f32x4* ex = (LAS f32x4*)(lds + P2_EX);
    { const unsigned m = kh ? 0xffffffffu : 0u;
#pragma unroll
      for (int i = 0; i < 3; ++i) { const f32x4 a = {o[0][4 * i], o[0][4 * i + 1], o[0][4 * i + 2], o[0][4 * i + 3]}, e = {o[1][4 * i], o[1][4 * i + 1], o[1][4 * i + 2], o[1][4 * i + 3]};
          ex[(wid * 3 + i) * 64 + lane] = sel4(m, a, e); }
      LDS_BAR();
      f32x16 of = {};
#pragma unroll
      for (int i = 0; i < 3; ++i) { const f32x4 a = {o[0][4 * i], o[0][4 * i + 1], o[0][4 * i + 2], o[0][4 * i + 3]}, e = {o[1][4 * i], o[1][4 * i + 1], o[1][4 * i + 2], o[1][4 * i + 3]};
          const f32x4 t = sel4(m, e, a) + ex[((wid ^ 4) * 3 + i) * 64 + lane];
          of[4 * i] = t[0]; of[4 * i + 1] = t[1]; of[4 * i + 2] = t[2]; of[4 * i + 3] = t[3]; }
      float l = upper_half(of[8]);
      bool bad = !(l < 1.8446744e19f);
#pragma unroll
      for (int i = 0; i < 12; ++i) bad |= !(fabsf(of[i]) < 3.0e38f);
      if (__any(bad)) {
          const s16x8 qq0 = ld128((lcptr)lds + P2_Q + (2 * qg + kh) * 1536 + lane * 16); const s16x4 qq1 = ld64((lcptr)lds + P2_Q + (2 * qg + kh) * 1536 + 1024 + lane * 8);
          attn_safe(c, WS_KV + (unsigned)hh * 64 * 3072, qq0, qq1, of); l = upper_half(of[8]); }
      const float il = __builtin_amdgcn_rcpf(l);
      const unsigned a0 = cvtpk(of[0] * il, of[1] * il), a1 = cvtpk(of[2] * il, of[3] * il), b0 = cvtpk(of[4] * il, of[5] * il), b1 = cvtpk(of[6] * il, of[7] * il);
      auto x0 = __builtin_amdgcn_permlane32_swap(a0, b0, false, false), x1 = __builtin_amdgcn_permlane32_swap(a1, b1, false, false);
      const unsigned t16 = 2 * (8 * qb + 2 * qg + kh) + (r >> 4);
      st128(c.rs, WS_O + (t16 * 80 + 2 * hh + h) * 256 + (r & 15) * 16, (u32x4){x0[0], x1[0], x0[1], x1[1]});
      if (h == 0) st64(c.rs, WS_O + (t16 * 80 + 64 + (hh >> 1)) * 256 + (r & 15) * 16 + (hh & 1) * 8, (u32x2){cvtpk(of[8] * il, of[9] * il), cvtpk(of[10] * il, of[11] * il)});
    }
#undef LDK0
#undef LDK1
#undef LDV0
#undef LDV1
#undef EX
}

constexpr int PJ_R = 5;
template <int NKS, int KSS> struct PJ {
    static constexpr int NS = NKS / KSS, SLOT = 5 * KSS * 1024, NWI = (5 * KSS + 7) / 8, NA = NKS / 2, PRE = imin(PJ_R, NS);
};
template <int NKS, int KSS> DI void pj_issue_w(const Ctx& c, const unsigned (&voff)[(5 * KSS + 7) / 8], int st) {
    using P = PJ<NKS, KSS>;
#pragma unroll
    for (int i = 0; i < P::NWI; ++i) dma16<true>(c.ws + st * (KSS * 1024), voff[i], c.lds0 + (st % PJ_R) * P::SLOT + imin(c.wid + 8 * i, 5 * KSS - 1) * 1024);
}
template <int NKS, int KSS> DI void pj_voff(const Ctx& c, unsigned wbase, int cc, unsigned (&voff)[(5 * KSS + 7) / 8]) {
#pragma unroll
    for (int i = 0; i < PJ<NKS, KSS>::NWI; ++i) { const int f = imin(c.wid + 8 * i, 5 * KSS - 1); voff[i] = wbase + (unsigned)((5 * cc + f / KSS) * NKS + f % KSS) * 1024 + c.lane * 16; }
}
DI int pj_pp(int b) { return 8 * ((b & 7) >> 1) + ((b >> 3) & 7); }
DI int pj_cc(int b) { return 4 * (b & 1) + (b >> 6); }
template <int NKS, int KSS> DI void pj_prefetch(const Ctx& c, unsigned wbase) {
    unsigned voff[PJ<NKS, KSS>::NWI]; pj_voff<NKS, KSS>(c, wbase, pj_cc(c.b), voff);
#pragma unroll
    for (int s = 0; s < PJ<NKS, KSS>::PRE; ++s) pj_issue_w<NKS, KSS>(c, voff, s);
}

template <int NA, int WIN, int HK, int NS, int NWI, int NRES> constexpr int pj_younger(int j) {
    int n = imin(NA - 1, j + WIN) - j;
    if (j < WIN) n += NRES;
    for (int s = 1; s + PJ_R - 1 < NS; ++s)
        if (HK * s <= j && (j < WIN || HK * s > j - WIN)) n += NWI;
    return n;
}
template <int NA, int WIN, int HK, int NS, int NWI> constexpr int pj_bar_younger(int s) {
    const int s0 = s + 2 - PJ_R;
    int n = 0;
    for (int t = s0 + 1; t < s; ++t) if (t + PJ_R - 1 < NS) n += NWI;
    for (int j = HK * s0; j < HK * s; ++j) if (j + WIN < NA) ++n;
    return n;
}
template <int NKS, int KSS, bool FIRST, int KH>
DI void pj_body(const Ctx& c, unsigned abase, unsigned wbase, u32x2 (&res)[3]) {
    using P = PJ<NKS, KSS>;
    constexpr int NS = P::NS, NG = NKS * 4, NA = P::NA, HK = KSS / 2, NCT = KH ? 2 : 3, CT0 = KH ? 3 : 0;
    const int lane = c.lane, wid = c.wid, t16l = wid & 3, r = lane & 15, gq = lane >> 4, pp = pj_pp(c.b), cc = pj_cc(c.b);
    lptr lds = c.lds;
    const unsigned t16 = 4 * pp + t16l;
    const int tok = 16 * t16 + r;
    constexpr int WIN = imin(NA, FIRST ? 4 : 9);
    s16x8 a[NA];
    const unsigned aoff = abase + (t16 * NG + 4 * KH) * 256 + lane * 16;
#pragma unroll
    for (int j = 0; j < WIN; ++j) ldg_async(a[j], c.ws + j * 2048, aoff);
    if (FIRST) {
#pragma unroll
        for (int i = 0; i < NCT; ++i) res[i] = *(const GAS u32x2*)(c.x + (size_t)tok * 1280 + cc * 160 + (CT0 + i) * 32 + gq * 8);
    }
    unsigned voff[P::NWI]; pj_voff<NKS, KSS>(c, wbase, cc, voff);
    constexpr int NRES = FIRST ? NCT : 0;
    const unsigned wa = KH * 1024 + lane * 16;
#define PJ_READ(W, st, i) do { lcptr b_ = (lcptr)lds + ((st) % PJ_R) * P::SLOT + wa + (i) * 2048; \
        _Pragma("unroll") for (int ct = 0; ct < 5; ++ct) W[ct] = ld128(b_ + ct * KSS * 1024); } while (0)
    f32x4 acc[5];
#pragma unroll
    for (int ct = 0; ct < 5; ++ct) acc[ct] = (f32x4){};
    STAMP(c.ws, 3, FIRST ? 0 : 4);
    s16x8 w[2][5];
    PJ_READ(w[0], 0, 0);
#pragma unroll
    for (int s = 0; s < NS; ++s) {
        if (NS > PJ_R && s >= 1) {
            if (s + 1 >= PJ_R && s + 1 < NS) WAIT_BAR((pj_bar_younger<NA, WIN, HK, NS, P::NWI>(s))); else LDS_BAR();
            SBAR();
            if (s + PJ_R - 1 < NS) pj_issue_w<NKS, KSS>(c, voff, s + PJ_R - 1);
        }
#pragma unroll
        for (int i = 0; i < HK; ++i) {
            const int j = s * HK + i;
            if (i + 1 < HK) PJ_READ(w[(j + 1) & 1], s, i + 1); else if (s + 1 < NS) PJ_READ(w[(j + 1) & 1], s + 1, 0);
            if (j + WIN < NA) ldg_async(a[j + WIN], c.ws + (j + WIN) * 2048, aoff);
            WAIT_USE((pj_younger<NA, WIN, HK, NS, P::NWI, NRES>(j)), a[j]);
            SBAR();
#pragma unroll
            for (int ct = 0; ct < 5; ++ct) acc[ct] = MFMA16(w[j & 1][ct], a[j], acc[ct]);
            SBAR();
        }
    }
#undef PJ_READ
    STAMP(c.ws, 3, FIRST ? 1 : 5);
    LDS_BAR();
    LAS f32x4* red = (LAS f32x4*)(lds + XCH);
#pragma unroll
    for (int i = 0; i < 5 - NCT; ++i) red[(wid * 3 + i) * 64 + lane] = acc[(KH ? 0 : 3) + i];
    LDS_BAR();
    STAMP(c.ws, 3, FIRST ? 2 : 6);
    float s1 = 0.f, s2 = 0.f;
#pragma unroll
    for (int i = 0; i < NCT; ++i) {
        const f32x4 o = red[((wid ^ 4) * 3 + i) * 64 + lane], m = acc[CT0 + i];
        const unsigned w0 = cvtpk(bflo(res[i].x) + m[0] + o[0], bfhi(res[i].x) + m[1] + o[1]), w1 = cvtpk(bflo(res[i].y) + m[2] + o[2], bfhi(res[i].y) + m[3] + o[3]);
        if (FIRST) {
            res[i] = (u32x2){w0, w1};
            st64(c.rs, WS_H + (t16 * 80 + 10 * cc + 2 * (CT0 + i) + (gq >> 1)) * 256 + r * 16 + (gq & 1) * 8, res[i]);
            const float a_ = bflo(w0), b_ = bfhi(w0), c_ = bflo(w1), d = bfhi(w1); s1 += (a_ + b_) + (c_ + d); s2 += (a_ * a_ + b_ * b_) + (c_ * c_ + d * d);
        } else {
            *(GAS u32x2*)(c.out + (size_t)tok * 1280 + cc * 160 + (CT0 + i) * 32 + gq * 8) = (u32x2){w0, w1};
        }
    }
    if (FIRST) {
        s1 += __shfl_xor(s1, 16); s2 += __shfl_xor(s2, 16); s1 = swap32_add(s1); s2 = swap32_add(s2);
        LAS f32x2* ex = (LAS f32x2*)(lds + XCH + 2048);
        if (KH == 1 && gq == 0) ex[t16l * 16 + r] = (f32x2){s1, s2};
        LDS_BAR();
        if (KH == 0 && gq == 0) { const f32x2 e = ex[t16l * 16 + r];
            st64(c.rs, WS_HST + ((t16 * 8 + cc) * 16 + r) * 8, (u32x2){__float_as_uint(s1 + e.x), __float_as_uint(s2 + e.y)}); }
    }
}
template <int NKS, int KSS, bool FIRST>
DI void phase_proj(const Ctx& c, unsigned abase, unsigned wbase, u32x2 (&res)[3]) {
    if (c.wid < 4) pj_body<NKS, KSS, FIRST, 0>(c, abase, wbase, res);
    else pj_body<NKS, KSS, FIRST, 1>(c, abase, wbase, res);
}

constexpr int P4_R = 7, P4_SLOT = 22528, P4_NS = 20;
constexpr int P4_CS = XCH + 24576;

DI void p4_wvoff(const Ctx& c, int fc, unsigned& woff0, unsigned& woff1) {
    const unsigned o0 = c.wid * 1792 + c.lane * 16, o1 = c.wid * 1792 + 1024 + (c.lane < 48 ? c.lane : 47) * 16;
    woff0 = WS_W13P + (unsigned)imin(7 * fc + (o0 >> 11), 107) * 40960 + (o0 & 2047); woff1 = WS_W13P + (unsigned)imin(7 * fc + (o1 >> 11), 107) * 40960 + (o1 & 2047);
}
DI void p4_issue_w(const Ctx& c, unsigned woff0, unsigned woff1, int st) {
    const char* w_ = c.ws + st * 2048; const unsigned wdst = c.lds0 + 8192 + c.wid * 1792 + (st % P4_R) * P4_SLOT;
    dma16<true>(w_, woff0, wdst); dma16m<true>(w_, woff1, wdst + 1024, 0xffffffffffffull);
}
DI void p4_prefetch(const Ctx& c, int fc) {
    unsigned woff0, woff1; p4_wvoff(c, fc, woff0, woff1);
#pragma unroll
    for (int s = 0; s < P4_R - 1; ++s) p4_issue_w(c, woff0, woff1, s);
}
template <int UG>
DI void p4_body(const Ctx& c, int qb, int fc) {
    constexpr int NU = UG ? 3 : 4, U0 = UG ? 4 : 0;
    const int lane = c.lane, wid = c.wid, tt = wid & 3, r = lane & 31, h = lane >> 5;
    lptr lds = c.lds;
    const unsigned adst = c.lds0 + wid * 1024;
    const unsigned aoff = WS_H + (unsigned)(8 * qb + wid) * 80 * 256 + lane * 16;
    unsigned woff0, woff1; p4_wvoff(c, fc, woff0, woff1);
#pragma unroll
    for (int s = 0; s < P4_R - 1; ++s) dma16<true>(c.ws + s * 1024, aoff, adst + s * P4_SLOT);
    const unsigned xa = ((2 * tt + (r >> 4)) * 4 + h) * 256 + (r & 15) * 16, wa = 8192 + U0 * 2048 + lane * 16;
    const unsigned t16 = 8 * qb + 2 * tt + (r >> 4);
    u32x2 hs[4];
#pragma unroll
    for (int i = 0; i < 4; ++i) hs[i] = lg64(c.rs, WS_HST + ((t16 * 8 + 4 * h + i) * 16 + (r & 15)) * 8);
#define P4_ISSUE(st) do { dma16<true>(c.ws + (st) * 1024, aoff, adst + ((st) % P4_R) * P4_SLOT); p4_issue_w(c, woff0, woff1, st); } while (0)
#define P4_READ(X, W, slot, ks) do { if ((ABL & 32) && ((slot) != 0)) break; lcptr b_ = (lcptr)lds + (slot); X = ld128(b_ + xa + (ks) * 512); \
        _Pragma("unroll") for (int u = 0; u < NU; ++u) W[u] = ld128(b_ + wa + u * 2048 + (ks) * 1024); } while (0)
#define P4_MMA(X, W) do { if (ABL & 64) { PIN(X); _Pragma("unroll") for (int u = 0; u < NU; ++u) PIN(W[u]); break; } _Pragma("unroll") for (int u = 0; u < NU; ++u) acc[u] = MFMA32(W[u], X, acc[u]); } while (0)
    f32x16 acc[NU];
#pragma unroll
    for (int u = 0; u < NU; ++u) acc[u] = (f32x16){};
    WAIT_BAR((P4_R - 2) + 4);
    s16x8 xa_, wa_[NU], xb, wb_[NU];
    P4_READ(xa_, wa_, 0, 0);
#pragma unroll
    for (int s = 0; s < P4_NS; ++s) {
        if (!(ABL & 16)) WAIT_BAR(fly2(s, P4_R, P4_NS, 1, 2) + (s + 1 < P4_R - 1 ? 4 : 0));
        SBAR();
        if (s + P4_R - 1 < P4_NS && !(ABL & 8)) P4_ISSUE(s + P4_R - 1);
        P4_READ(xb, wb_, (s % P4_R) * P4_SLOT, 1);
        SBAR();
        P4_MMA(xa_, wa_);
        SBAR();
        if (s + 1 < P4_NS) P4_READ(xa_, wa_, ((s + 1) % P4_R) * P4_SLOT, 0);
        SBAR();
        P4_MMA(xb, wb_);
        SBAR();
    }
#undef P4_ISSUE
#undef P4_READ
#undef P4_MMA
    LDS_BAR();
    STAMP(c.ws, 1, 2);
    float s1 = (__uint_as_float(hs[0].x) + __uint_as_float(hs[1].x)) + (__uint_as_float(hs[2].x) + __uint_as_float(hs[3].x));
    float s2 = (__uint_as_float(hs[0].y) + __uint_as_float(hs[1].y)) + (__uint_as_float(hs[2].y) + __uint_as_float(hs[3].y));
    s1 = swap32_add(s1); s2 = swap32_add(s2);
    const float mean = s1 * (1.0f / D), var = fmaxf(s2 * (1.0f / D) - mean * mean, 0.f), rstd = __builtin_amdgcn_rsqf(var + LN_EPS), rm = rstd * mean;
    const LAS float* csl = (const LAS float*)(lds + P4_CS);
#pragma unroll
    for (int u = 0; u < NU; ++u) {
        if (7 * fc + U0 + u < 108) {
            u32x2 w[2];
#pragma unroll
            for (int gq = 0; gq < 2; ++gq) {
                const f32x4 cg_ = *(const LAS f32x4*)(csl + (U0 + u) * 16 + 8 * gq + 4 * h), cu = *(const LAS f32x4*)(csl + 128 + (U0 + u) * 16 + 8 * gq + 4 * h);
                float o[4];
#pragma unroll
                for (int e = 0; e < 4; ++e) { const float gt = rstd * acc[u][4 * gq + e] - rm * cg_[e], uq = rstd * acc[u][8 + 4 * gq + e] - rm * cu[e];
                    o[e] = gt * uq * __builtin_amdgcn_rcpf(1.0f + __builtin_amdgcn_exp2f(-1.4426950408889634f * gt)); }
                w[gq] = (u32x2){cvtpk(o[0], o[1]), cvtpk(o[2], o[3])};
            }
            auto x0 = __builtin_amdgcn_permlane32_swap(w[0].x, w[1].x, false, false), x1 = __builtin_amdgcn_permlane32_swap(w[0].y, w[1].y, false, false);
            st128(c.rs, WS_HM + (t16 * 216 + 14 * fc + 2 * (U0 + u) + h) * 256 + (r & 15) * 16, (u32x4){x0[0], x1[0], x0[1], x1[1]});
        }
    }
}
DI void phase4(const Ctx& c, int qb, int fc) {
    if (c.wid < 4) p4_body<0>(c, qb, fc); else p4_body<1>(c, qb, fc);
}
DI void p4_colsums(const Ctx& c, int fc) {
    const int tid = threadIdx.x;
    if (tid < 224) { const int mat = tid >= 112, cl = tid - 112 * mat, col = imin(fc * 112 + cl, F - 1);
        float v[20];
#pragma unroll
        for (int i = 0; i < 20; ++i) v[i] = __uint_as_float(lg32(c.rs, WS_CSP + (unsigned)((mat * 20 + i) * F + col) * 4));
        float s = 0.f;
#pragma unroll
        for (int i = 0; i < 20; ++i) s += v[i];
        *(LAS float*)(c.lds + P4_CS + (mat * 128 + cl) * 4) = s; }
}

template <int PH0, int PH1>
__global__ __launch_bounds__(512, 2) void block_fwd(const char* __restrict__ x, const char* __restrict__ wqkv, const char* __restrict__ wo, const char* __restrict__ w1,
                                                    const char* __restrict__ w3, const char* __restrict__ w2, char* __restrict__ out, char* __restrict__ ws) {
    extern __shared__ __attribute__((aligned(16))) char smem[];
    Ctx c; c.x = x; c.wqkv = wqkv; c.wo = wo; c.w1 = w1; c.w3 = w3; c.w2 = w2; c.out = out; c.ws = ws;
    c.rs = __builtin_amdgcn_make_buffer_rsrc(ws, 0, WS_END, 0x00020000);
    c.lds = (lptr)smem; c.lds0 = (unsigned)(uintptr_t)smem; c.b = blockIdx.x; c.lane = threadIdx.x & 63; c.wid = __builtin_amdgcn_readfirstlane(threadIdx.x >> 6);
    const int lane = c.lane, g = c.b & 7, idx = c.b >> 3;
    STAMP(ws, 0, 0);
    const unsigned e0 = lg32(c.rs, WS_E);
    u32x2 res[3] = {(u32x2){0u, 0u}, (u32x2){0u, 0u}, (u32x2){0u, 0u}};
    if (PH0 <= 0) {
        phase1(c);
        STAMP(c.ws, 2, 5);
        c.tag = __builtin_amdgcn_readfirstlane(e0) + 1u;
        STAMP(ws, 0, 1);
        WAIT_VM(0);
        publish(c, FL_KV);
        repack_store(c);
        if (PH1 == 0) { WAIT_VM(0); publish(c, FL_PK); }
        STAMP(ws, 0, 2);
    } else c.tag = __builtin_amdgcn_readfirstlane(e0) + 1u;
    const int qb4 = 8 * (g & 1) + (idx & 7), fc = 4 * (g >> 1) + (idx >> 3), pp = pj_pp(c.b);
    if (PH0 <= 1 && PH1 >= 1) {
        { const int hh = 4 * g + (idx & 3), qb = idx >> 2;
          const int wg = lane < 16 ? wg_p1(lane, (32 + hh) / 6) : lane < 32 ? wg_p1(lane - 16, (64 + hh) / 6) : wg_p1(2 * qb + (lane & 1), hh / 6);
          const unsigned off[1] = {flag_off(FL_KV, wg)}; wait_flags(c, off); }
        phase2(c, PH0 == 0);
        WAIT_VM(0);
        publish(c, FL_O);
        if (PH1 >= 2) pj_prefetch<20, 4>(c, WS_WOP);
        STAMP(ws, 0, 5);
    }
    if (PH0 <= 2 && PH1 >= 2) {
        if (PH0 == 2) pj_prefetch<20, 4>(c, WS_WOP);
        if (PH1 >= 3) p4_colsums(c, fc);
        WAIT_VM(0);
        { const unsigned off[1] = {flag_off(FL_O, wg_p2(lane & 31, pp >> 2))}; wait_flags(c, off); }
        STAMP(ws, 0, 6);
        phase_proj<20, 4, true>(c, WS_O, WS_WOP, res);
        WAIT_VM(0);
        publish(c, FL_H);
        if (PH1 >= 3) p4_prefetch(c, fc);
        STAMP(ws, 0, 7);
    }
    if (PH0 <= 3 && PH1 >= 3) {
        if (PH0 == 3) { p4_colsums(c, fc); p4_prefetch(c, fc); }
        { const unsigned off[1] = {flag_off(FL_H, wg_p3(2 * qb4 + (lane & 1), (lane >> 1) & 7))}; wait_flags(c, off); }
        STAMP(ws, 1, 1);
        phase4(c, qb4, fc);
        WAIT_VM(0);
        publish(c, FL_M);
        if (PH1 >= 4) pj_prefetch<54, 6>(c, WS_W2P);
        STAMP(ws, 1, 3);
    }
    if (PH0 <= 4 && PH1 >= 4) {
        if (PH0 == 4) pj_prefetch<54, 6>(c, WS_W2P);
        WAIT_VM(0);
        const unsigned fm = flag_off(FL_M, wg_p4(pp >> 1, lane & 15));
        if (c.b == 0) {
            const unsigned off[5] = {fm, flag_off(FL_O, lane), flag_off(FL_O, 64 + lane), flag_off(FL_O, 128 + lane), flag_off(FL_O, 192 + lane)}; wait_flags(c, off);
        } else { const unsigned off[1] = {fm}; wait_flags(c, off); }
        STAMP(ws, 1, 4);
        if (PH0 > 2) {
            const int cc = pj_cc(c.b), ct0 = c.wid >> 2 ? 3 : 0, gq = lane >> 4;
#pragma unroll
            for (int i = 0; i < 3; ++i) res[i] = lg64(c.rs, WS_H + ((4 * pp + (c.wid & 3)) * 80 + 10 * cc + 2 * imin(ct0 + i, 4) + (gq >> 1)) * 256 + (lane & 15) * 16 + (gq & 1) * 8);
            WAIT_VM(0); PIN(res[0]); PIN(res[1]); PIN(res[2]);
        }
        phase_proj<54, 6, false>(c, WS_HM, WS_W2P, res);
        STAMP(ws, 1, 5);
        if (c.b == 0 && threadIdx.x == 0) st32(c.rs, WS_E, c.tag);
    }
}

}

extern "C" void kernel_launch(void* const* d_in, const int* in_sizes, int n_in, void* d_out, int out_size, void* d_ws, size_t ws_size, hipStream_t stream) {
    using namespace blk;
    const char* x = (const char*)d_in[0]; const char* wqkv = (const char*)d_in[1]; const char* wo = (const char*)d_in[2];
    const char* w1 = (const char*)d_in[4]; const char* w3 = (const char*)d_in[5]; const char* w2 = (const char*)d_in[6];
    char* ws = (char*)d_ws; char* out = (char*)d_out;
#define LAUNCH(A, B) do { hipFuncSetAttribute((const void*)block_fwd<A, B>, hipFuncAttributeMaxDynamicSharedMemorySize, LDS_BYTES); \
        block_fwd<A, B><<<256, 512, LDS_BYTES, stream>>>(x, wqkv, wo, w1, w3, w2, out, ws); } while (0)
    LAUNCH(0, 4);
#undef LAUNCH
}
```

```cpp
#include <hip/hip_runtime.h>
#include <hip/hip_bf16.h>
#include <stdint.h>

namespace blk {

constexpr int S = 2048, D = 640, H = 32, F = 1728;
constexpr float LN_EPS = 1e-5f;
constexpr float QSCALE = 0.22360679774997896f * 1.4426950408889634f;

#define LAS __attribute__((address_space(3)))
#define GAS __attribute__((address_space(1)))
#define DI __device__ __forceinline__
typedef short s16x8 __attribute__((ext_vector_type(8)));
typedef short s16x4 __attribute__((ext_vector_type(4)));
typedef float f32x16 __attribute__((ext_vector_type(16)));
typedef float f32x4 __attribute__((ext_vector_type(4)));
typedef float f32x2 __attribute__((ext_vector_type(2)));
typedef unsigned u32x4 __attribute__((ext_vector_type(4)));
typedef unsigned u32x2 __attribute__((ext_vector_type(2)));
typedef __bf16 bf16x2 __attribute__((ext_vector_type(2)));
typedef LAS char* lptr;
typedef LAS const char* lcptr;
typedef __amdgpu_buffer_rsrc_t rsrc_t;

constexpr unsigned WS_E = 0;
constexpr unsigned WS_FLAG = 4096;
constexpr unsigned WS_ROWP = 256u << 10;
constexpr unsigned WS_COLP = 512u << 10;
constexpr unsigned WS_Q = 1u << 20;
constexpr unsigned WS_KV = 4u << 20;
constexpr unsigned WS_O = 12u << 20;
constexpr unsigned WS_H = 16u << 20;
constexpr unsigned WS_HST = 20u << 20;
constexpr unsigned WS_CSP = 21u << 20;
constexpr unsigned WS_HM = 24u << 20;
constexpr unsigned WS_W13P = 32u << 20;
constexpr unsigned WS_W2P = 40u << 20;
constexpr unsigned WS_WOP = 44u << 20;
constexpr unsigned WS_END = 48u << 20;
enum { FL_KV = 0, FL_PK = 1, FL_O = 2, FL_H = 3, FL_M = 4 };
constexpr int LDS_BYTES = 163840;
constexpr unsigned SPIN_CAP = 1u << 16;

#define STAMP(ws, kid, pt) do { } while (0)
#define STAMPW(ws, kid, pt, wv) do { } while (0)
#define ABL 0
#define STG_DECL() do { } while (0)
#define STG(i) do { } while (0)
#define STG_OUT(ws, kid, n) do { } while (0)
#define SBAR() __builtin_amdgcn_sched_barrier(0)
#define PIN(x) asm volatile("" : "+v"(x))
#define MFMA32(a, b, c) __builtin_amdgcn_mfma_f32_32x32x16_bf16(a, b, c, 0, 0, 0)
#define MFMA32K8(a, b, c) __builtin_amdgcn_mfma_f32_32x32x8bf16_1k(a, b, c, 0, 0, 0)
#define MFMA16(a, b, c) __builtin_amdgcn_mfma_f32_16x16x32_bf16(a, b, c, 0, 0, 0)
#define WAIT_BAR(N) asm volatile("s_waitcnt vmcnt(%0) lgkmcnt(0)\n\ts_barrier" :: "i"(N) : "memory")
#define WAIT_BARV(N) asm volatile("s_waitcnt vmcnt(%0)\n\ts_barrier" :: "i"(N) : "memory")
#define WAIT_VM(N) asm volatile("s_waitcnt vmcnt(%0)" :: "i"(N) : "memory")
#define LDS_BAR() asm volatile("s_waitcnt lgkmcnt(0)\n\ts_barrier" ::: "memory")

#define SCON true
template <bool SC> DI void dma16(const void* sbase, unsigned voff, unsigned ldsaddr) {
    if (SC && SCON) asm volatile("s_mov_b32 m0, %2\n\ts_nop 0\n\tglobal_load_lds_dwordx4 %0, %1 sc1" :: "v"(voff), "s"(sbase), "s"(ldsaddr) : "memory");
    else asm volatile("s_mov_b32 m0, %2\n\ts_nop 0\n\tglobal_load_lds_dwordx4 %0, %1" :: "v"(voff), "s"(sbase), "s"(ldsaddr) : "memory");
}
template <bool SC> DI void dma16m(const void* sbase, unsigned voff, unsigned ldsaddr, unsigned long long mask) {
    if (SC && SCON) asm volatile("s_mov_b32 m0, %2\n\ts_mov_b64 exec, %3\n\tglobal_load_lds_dwordx4 %0, %1 sc1\n\ts_mov_b64 exec, -1" :: "v"(voff), "s"(sbase), "s"(ldsaddr), "s"(mask) : "memory");
    else asm volatile("s_mov_b32 m0, %2\n\ts_mov_b64 exec, %3\n\tglobal_load_lds_dwordx4 %0, %1\n\ts_mov_b64 exec, -1" :: "v"(voff), "s"(sbase), "s"(ldsaddr), "s"(mask) : "memory");
}
DI void touch_lines(const void* sbase, unsigned voff, unsigned ldsaddr) {
    asm volatile("s_mov_b32 m0, %2\n\ts_nop 0\n\tglobal_load_lds_dword %0, %1" :: "v"(voff), "s"(sbase), "s"(ldsaddr) : "memory");
}
constexpr int imin(int a, int b) { return a < b ? a : b; }
constexpr int imax(int a, int b) { return a > b ? a : b; }
constexpr int fly(int s, int R, int NS) { return imax(0, imin(R - 3, NS - 2 - s)); }
constexpr int fly2(int s, int R, int NS, int na, int nw) {
    const int need = imin(s + 1, NS - 1);
    const int pro = imin(R - 1, NS);
    const int issued = pro * na + imax(0, imin(s, NS - R + 1)) * (na + nw);
    const int endpos = need < pro ? (need + 1) * na : pro * na + (need - pro + 1) * (na + nw);
    return imax(0, issued - endpos);
}
#define WARM2 1
#define WARM4 1
#define ROT1 1
#define ROT4 0
#define ROTJ 0
DI int rot(int i, int rho, int ns) { const int t = i + rho; return t >= ns ? t - ns : t; }
DI unsigned cvtpk(float lo, float hi) { f32x2 v = {lo, hi}; return __builtin_bit_cast(unsigned, __builtin_convertvector(v, bf16x2)); }
DI float bflo(unsigned w) { return __uint_as_float(w << 16); }
DI float bfhi(unsigned w) { return __uint_as_float(w & 0xffff0000u); }
DI s16x8 ld128(lcptr p) { return *(const LAS s16x8*)p; }
DI s16x4 ld64(lcptr p) { return *(const LAS s16x4*)p; }
DI s16x4 ldtr(lcptr p) { return __builtin_bit_cast(s16x4, __builtin_amdgcn_ds_read_tr16_b64_v4i16((LAS s16x4*)p)); }
DI s16x8 cat(s16x4 a, s16x4 b) { return __builtin_shufflevector(a, b, 0, 1, 2, 3, 4, 5, 6, 7); }
DI f32x4 sel4(unsigned m, f32x4 a, f32x4 b) { const u32x4 x = __builtin_bit_cast(u32x4, a), y = __builtin_bit_cast(u32x4, b); return __builtin_bit_cast(f32x4, (x & m) | (y & ~m)); }
DI float swap32_add(float v) { auto r = __builtin_amdgcn_permlane32_swap(__float_as_uint(v), __float_as_uint(v), false, false); return __uint_as_float(r[0]) + __uint_as_float(r[1]); }
DI float swap32_max(float v) { auto r = __builtin_amdgcn_permlane32_swap(__float_as_uint(v), __float_as_uint(v), false, false); return fmaxf(__uint_as_float(r[0]), __uint_as_float(r[1])); }
DI float upper_half(float v) { auto r = __builtin_amdgcn_permlane32_swap(__float_as_uint(v), __float_as_uint(v), false, false); return __uint_as_float(r[1]); }
DI void st128(rsrc_t rs, unsigned off, u32x4 v) { __builtin_amdgcn_raw_buffer_store_b128(v, rs, off, 0, 16); }
DI void st64(rsrc_t rs, unsigned off, u32x2 v) { __builtin_amdgcn_raw_buffer_store_b64(v, rs, off, 0, 16); }
DI void st32(rsrc_t rs, unsigned off, unsigned v) { __builtin_amdgcn_raw_buffer_store_b32(v, rs, off, 0, 16); }
DI u32x4 lg128(rsrc_t rs, unsigned off) { return __builtin_amdgcn_raw_buffer_load_b128(rs, off, 0, 16); }
DI u32x2 lg64(rsrc_t rs, unsigned off) { return __builtin_amdgcn_raw_buffer_load_b64(rs, off, 0, 16); }
DI unsigned lg32(rsrc_t rs, unsigned off) { return __builtin_amdgcn_raw_buffer_load_b32(rs, off, 0, 16); }

DI void ldg_async(s16x8& dst, const void* sbase, unsigned voff) { asm volatile("global_load_dwordx4 %0, %1, %2 sc1" : "=v"(dst) : "v"(voff), "s"(sbase) : "memory"); }
#define WAIT_USE(N, x) asm volatile("s_waitcnt vmcnt(%1)" : "+v"(x) : "i"(N) : "memory")
struct Ctx { const char *x, *wqkv, *wo, *w1, *w3, *w2; char *out, *ws; rsrc_t rs; lptr lds; unsigned lds0, tag; int b, lane, wid; };

DI unsigned flag_off(int kind, int wg) { return WS_FLAG + (unsigned)(kind * 256 + wg) * 128u; }
DI void publish(const Ctx& c, int kind) {
    asm volatile("s_barrier" ::: "memory");
    if (threadIdx.x == 0) st32(c.rs, flag_off(kind, c.b), c.tag);
}
template <int N> DI void wait_flags(const Ctx& c, const unsigned (&off)[N]) {
    if (c.wid == 0) {
        for (unsigned spins = 0; spins < SPIN_CAP; ++spins) {
            unsigned v[N];
#pragma unroll
            for (int i = 0; i < N; ++i) v[i] = lg32(c.rs, off[i]);
            bool ok = true;
#pragma unroll
            for (int i = 0; i < N; ++i) ok &= v[i] == c.tag;
            if (__all(ok)) break;
            __builtin_amdgcn_s_sleep(1);
        }
    }
    asm volatile("s_waitcnt lgkmcnt(0)\n\ts_barrier" ::: "memory");
}
DI int wg_p1(int pb, int nb) { return ((pb >> 3) | ((nb >> 2) << 1)) + 8 * ((pb & 7) + 8 * (nb & 3)); }
DI int wg_p2(int hh, int qb) { return (hh >> 2) + 8 * ((hh & 3) + 4 * qb); }
DI int wg_p3(int pp, int cc) { return (((pp >> 3) << 1) | (cc >> 2)) + 8 * ((pp & 7) + 8 * (cc & 3)); }
DI int wg_p4(int qb, int fc) { return ((qb >> 3) | ((fc >> 2) << 1)) + 8 * ((qb & 7) + 8 * (fc & 3)); }

constexpr int P1_R = 5, P1_SLOT = 32768, P1_NS = 10;
constexpr int P1_STG = 264;
constexpr int P1_OUT = 65536;
constexpr int P1_ST = P1_OUT + 40960;
constexpr int P1_CS = P1_ST + 1024;

DI void repack_issue_one(const Ctx& c, int i);
template <int UP>
DI void p1_loop(Ctx& c, unsigned xoff, unsigned woff, int tt, int rho, unsigned put, unsigned get, f32x16& acc0, f32x16& acc1, u32x2 (&part)[10]) {
    f32x16 accx = {};
    const int lane = c.lane, wid = c.wid, r = lane & 31, h = lane >> 5, q = (lane >> 2) & 3, p = lane & 3, cg = (lane >> 4) & 1;
    constexpr unsigned long long WMASK = ~((1ull << 15) | (1ull << 27) | (1ull << 39) | (1ull << 51));
    const unsigned xdst = c.lds0 + wid * 2048, wdst = c.lds0 + 16384 + wid * 2048;
    const int xrow = 32 * tt + r;
    const unsigned xa = xrow * 128 + ((h ^ ((xrow >> 1) & 7)) * 16);
    const unsigned wk = 16384 + (8 * h + q) * 256 + (p & 1) * 8;
    const unsigned wa0 = wk + (((4 * (2 * UP) + 2 * cg + (p >> 1)) ^ (4 * q)) * 16);
    const unsigned wa1 = wk + (((4 * (2 * UP + 1) + 2 * cg + (p >> 1)) ^ (4 * q)) * 16);
    const unsigned waz = wk + (((4 * tt + 2 * cg + (p >> 1)) ^ (4 * q)) * 16);
    const s16x8 ones = {0x3f80, 0x3f80, 0x3f80, 0x3f80, 0x3f80, 0x3f80, 0x3f80, 0x3f80};
#define P1_PIECE(st, slot, k) do { if (ABL & 1) break; const int p_ = rot(st, rho, P1_NS); \
        if ((k) == 0) dma16<false>(c.x + p_ * 128, xoff, xdst + (slot)); \
        if ((k) == 1) dma16<false>(c.x + p_ * 128 + 8 * 1280, xoff ^ 64, xdst + (slot) + 1024); \
        if ((k) == 2) dma16m<false>(c.wqkv + (size_t)p_ * (64 * 3840), woff, wdst + (slot), WMASK); \
        if ((k) == 3) dma16m<false>(c.wqkv + (size_t)p_ * (64 * 3840) + 4 * 3840, woff, wdst + (slot) + 1024, WMASK); } while (0)
#define P1_ISSUE(st, slot) do { P1_PIECE(st, slot, 0); P1_PIECE(st, slot, 1); P1_PIECE(st, slot, 2); P1_PIECE(st, slot, 3); } while (0)
#define P1_READ(n) do { if (ABL & 2) break; lcptr b_ = (lcptr)c.lds + (((n) >> 2) % P1_R) * P1_SLOT; \
        X[(n) % 3] = ld128(b_ + (xa ^ (((n) & 3) * 32))); \
        W0[(n) % 3] = cat(ldtr(b_ + wa0 + ((n) & 3) * 4096), ldtr(b_ + wa0 + ((n) & 3) * 4096 + 1024)); \
        W1[(n) % 3] = cat(ldtr(b_ + wa1 + ((n) & 3) * 4096), ldtr(b_ + wa1 + ((n) & 3) * 4096 + 1024)); \
        if (UP && (n) < 4) WZ[(n) & 3] = cat(ldtr(b_ + waz + ((n) & 3) * 4096), ldtr(b_ + waz + ((n) & 3) * 4096 + 1024)); } while (0)
#define P1_MMA(n) do { if (ABL & 4) { PIN(X[(n) % 3]); PIN(W0[(n) % 3]); PIN(W1[(n) % 3]); break; } \
        acc0 = MFMA32(W0[(n) % 3], X[(n) % 3], acc0); acc1 = MFMA32(W1[(n) % 3], X[(n) % 3], acc1); \
        if ((n) < 4) { if (UP) accx = MFMA32(WZ[(n) & 3], ones, accx); else accx = MFMA32(X[(n) % 3], X[(n) % 3], accx); } } while (0)
    P1_ISSUE(0, 0); P1_ISSUE(1, P1_SLOT); P1_ISSUE(2, 2 * P1_SLOT);
    WAIT_BAR(8);
    asm volatile("" : "+s"(c.tag));
    STAMP(c.ws, 2, 1);
    P1_ISSUE(3, 3 * P1_SLOT);
    s16x8 X[3] = {}, W0[3] = {}, W1[3] = {}, WZ[4] = {};
    STG_DECL(); STG(0);
    P1_READ(0); P1_READ(1);
#pragma unroll
    for (int s = 0; s < P1_NS; ++s) {
        constexpr int PKS = P1_NS - P1_R + 1;
        constexpr int NX = P1_R - 1;
        WAIT_BARV(4 * fly(s, P1_R, P1_NS) + (s >= 1 && s + 1 <= P1_R - 1 ? 1 : 0) + (s > PKS ? 4 : 0) + (s > PKS + 1 ? 10 : 0));
        STG(1 + s);
        SBAR();
#pragma unroll
        for (int ks = 0; ks < 4; ++ks) {
            const int n = 4 * s + ks;
            if (n + 2 < 4 * P1_NS) P1_READ(n + 2);
            SBAR(); P1_MMA(n); SBAR();
            if (s + NX < P1_NS) P1_PIECE(s + NX, ((s + NX) % P1_R) * P1_SLOT, ks);
            if (s == PKS) repack_issue_one(c, ks);
            SBAR();
        }
        if (s == 0) {
            const int tr = (r & 3) + 4 * (r >> 3); float v = 0.f;
#pragma unroll
            for (int i = 0; i < 16; ++i) v = (tr == i) ? accx[i] : v;
            st64(c.rs, put + (h == ((r >> 2) & 1) ? (tt * 32 + r) * 8 : 0x7fffff00u), (u32x2){__float_as_uint(v), c.tag});
            SBAR();
        }
        if (s == PKS + 1) {
#pragma unroll
            for (int i = 0; i < 10; ++i) part[i] = lg64(c.rs, get + i * 1024);
            SBAR();
        }
    }
    STG(11); STG_OUT(c.ws, 0, 12);
#undef P1_PIECE
#undef P1_ISSUE
#undef P1_READ
#undef P1_MMA
}

DI void phase1(Ctx& c) {
    const int lane = c.lane, wid = c.wid, tid = threadIdx.x, g = c.b & 7, idx = c.b >> 3;
    const int pb = 8 * (g & 1) + (idx & 7), nb = 4 * (g >> 1) + (idx >> 3);
    const int tt = wid & 3, up = wid >> 2, r = lane & 31, h = lane >> 5;
    lptr lds = c.lds;
    if (tid < P1_R * 64) { const int sl = tid >> 6, k = tid & 63;
        *(LAS u32x4*)(lds + sl * P1_SLOT + 16384 + k * 256 + ((15 ^ (4 * (k & 3))) * 16)) = (u32x4){0x3f803f80u, 0x3f803f80u, 0x3f803f80u, 0x3f803f80u}; }
    const int xr = 16 * wid + (lane >> 3), xc = (lane & 7) ^ ((xr >> 1) & 7);
    const unsigned xoff = (pb * 128 + xr) * 1280 + xc * 16;
    const int wkk = 8 * wid + (lane >> 4), wp = (lane & 15) ^ (4 * (wkk & 3)), wpc = wp < 15 ? wp : 14;
    const unsigned woff = wkk * 3840 + nb * 240 + wpc * 16;
    f32x16 acc0 = {}, acc1 = {};
    STAMP(c.ws, 2, 0);
    const int rho = ROT1 ? (pb + 3 * nb) % P1_NS : 0;
    const unsigned put = up ? WS_COLP + (unsigned)(nb * 16 + pb) * 1024 : WS_ROWP + (unsigned)(pb * 16 + nb) * 1024;
    const int gi = (wid & 1) * 64 + lane;
    const unsigned get = ((wid & 2) ? WS_COLP + (unsigned)nb * 16384 : WS_ROWP + (unsigned)pb * 16384) + gi * 8;
    u32x2 part[10];
    if (up == 0) p1_loop<0>(c, xoff, woff, tt, rho, put, get, acc0, acc1, part);
    else p1_loop<1>(c, xoff, woff, tt, rho, put, get, acc0, acc1, part);
    STAMP(c.ws, 2, 2);
    for (unsigned spins = 0; spins < SPIN_CAP; ++spins) {
        bool ok = true;
#pragma unroll
        for (int i = 0; i < 10; ++i) ok &= part[i].y == c.tag;
        if (__all(ok)) break;
        __builtin_amdgcn_s_sleep(1);
#pragma unroll
        for (int i = 0; i < 10; ++i) part[i] = lg64(c.rs, get + i * 1024);
    }
    float psum = 0.f;
#pragma unroll
    for (int i = 0; i < 10; ++i) psum += __uint_as_float(part[i].x);
    LDS_BAR();
    LAS float* st = (LAS float*)(lds + P1_ST); LAS float* cs = (LAS float*)(lds + P1_CS);
    if (wid < 2) st[gi * 2 + 1] = psum; else if (wid < 4) cs[gi] = psum;
    if (up == 1 && h == 0) st[(tt * 32 + r) * 2] = acc1[12];
    LDS_BAR();
    STAMP(c.ws, 2, 3);
    { const f32x2 sv = *(const LAS f32x2*)(st + (tt * 32 + r) * 2);
      const float mean = sv.x * (1.0f / D), var = fmaxf(sv.y * (1.0f / D) - mean * mean, 0.f), rstd = __builtin_amdgcn_rsqf(var + LN_EPS), rm = rstd * mean;
      lptr stg = lds + P1_OUT + (32 * tt + r) * P1_STG;
#pragma unroll
      for (int u = 0; u < 2; ++u)
#pragma unroll
        for (int gq = 0; gq < 4; ++gq) {
            const int col = (2 * up + u) * 32 + 8 * gq + 4 * h;
            const f32x4 c4 = *(const LAS f32x4*)(cs + col);
            const float sc = (nb * 120 + col < D) ? QSCALE : 1.0f, a = rstd * sc, bb = rm * sc;
            float v[4];
#pragma unroll
            for (int e = 0; e < 4; ++e) v[e] = a * (u ? acc1[4 * gq + e] : acc0[4 * gq + e]) - bb * c4[e];
            *(LAS u32x2*)(stg + col * 2) = (u32x2){cvtpk(v[0], v[1]), cvtpk(v[2], v[3])};
        }
    }
    LDS_BAR();
    STAMP(c.ws, 2, 4);
#pragma unroll
    for (int j = 0; j < 3; ++j) {
        const int slot = up + 2 * j, gidx = 6 * nb + slot, type = gidx >> 5, head = gidx & 31, tile = 4 * pb + tt;
        if (type < 2) {
            lcptr row = (lcptr)lds + P1_OUT + (32 * tt + r) * P1_STG + slot * 40;
            const u32x2 a0 = *(const LAS u32x2*)(row + 16 * h), a1 = *(const LAS u32x2*)(row + 16 * h + 8), a2 = *(const LAS u32x2*)(row + 32);
            const unsigned dst = type ? WS_KV + (unsigned)(head * 64 + tile) * 3072 : WS_Q + (unsigned)(head * 64 + tile) * 1536;
            st128(c.rs, dst + lane * 16, (u32x4){a0.x, a0.y, a1.x, a1.y});
            st64(c.rs, dst + 1024 + lane * 8, h ? (u32x2){type ? 0x00003f80u : 0u, 0u} : a2);
        } else {
            const int q = (lane >> 2) & 3, p = lane & 3, cg = (lane >> 4) & 1;
            lcptr vb = (lcptr)lds + P1_OUT + (32 * tt + 4 * h + q) * P1_STG + slot * 40 + (cg * 4 + p < 5 ? (cg * 4 + p) * 8 : 0);
            const unsigned dst = WS_KV + (unsigned)(head * 64 + tile) * 3072 + 1536;
#pragma unroll
            for (int s = 0; s < 2; ++s) {
                const u32x2 a = __builtin_bit_cast(u32x2, ldtr(vb + (16 * s) * P1_STG)), e = __builtin_bit_cast(u32x2, ldtr(vb + (16 * s + 8) * P1_STG));
                u32x4 v = {a.x, a.y, e.x, e.y};
                if (r == 20) v = (u32x4){0x3f803f80u, 0x3f803f80u, 0x3f803f80u, 0x3f803f80u};
                if (r > 20) v = (u32x4){0u, 0u, 0u, 0u};
                if (r < 24) st128(c.rs, dst + s * 768 + h * 384 + r * 16, v);
            }
        }
    }
}

constexpr int PK_OFF = 0;
DI int pk_job(int b, int i) { const int j = b + 256 * i; return j < 910 ? j : j - 256; }
DI void repack_issue_one(const Ctx& c, int i) {
    const int row = 8 * c.wid + (c.lane >> 3), pc = c.lane & 7;
    const int j = pk_job(c.b, i);
    const char* src; unsigned off;
    if (j < 540) { const int jj = j < 270 ? j : j - 270, kb = jj / 27, cb = jj % 27; src = j < 270 ? c.w1 : c.w3; off = (64 * kb + row) * 3456 + cb * 128 + pc * 16; }
    else if (j < 810) { const int jj = j - 540, kb = jj / 10, cb = jj % 10; src = c.w2; off = (64 * kb + row) * 1280 + cb * 128 + pc * 16; }
    else { const int jj = j - 810, kb = jj / 10, cb = jj % 10, kp = 64 * kb + row, G = kp >> 3, e = kp & 7;
        const int k = G < 64 ? 20 * (G >> 1) + 8 * (G & 1) + e : 40 * (G - 64) + 16 + (e < 4 ? e : e + 16);
        src = c.wo; off = k * 1280 + cb * 128 + pc * 16; }
    dma16<false>(src, off, c.lds0 + PK_OFF + i * 8192 + c.wid * 1024);
}
DI void repack_store(const Ctx& c) {
    const int lane = c.lane, w = c.wid, g = lane >> 4, i16 = lane & 15, q = i16 >> 2, p = i16 & 3;
#pragma unroll
    for (int i = 0; i < 4; ++i) {
        const int j = pk_job(c.b, i);
        lcptr blk = (lcptr)c.lds + PK_OFF + i * 8192 + (w >> 1) * 32 + p * 8;
        if (j < 540) {
            const int mat = j >= 270, jj = mat ? j - 270 : j, kb = jj / 27, cb = jj % 27, ks = 2 * (w & 1) + (g >> 1), hh = g & 1;
            lcptr a = blk + (16 * ks + 8 * hh + q) * 128;
            const u32x2 v0 = __builtin_bit_cast(u32x2, ldtr(a)), v1 = __builtin_bit_cast(u32x2, ldtr(a + 512));
            st128(c.rs, WS_W13P + (unsigned)((4 * cb + (w >> 1)) * 40 + 4 * kb + ks) * 1024 + (mat * 16 + i16 + 32 * hh) * 16, (u32x4){v0.x, v0.y, v1.x, v1.y});
            float s = ((bflo(v0.x) + bfhi(v0.x)) + (bflo(v0.y) + bfhi(v0.y))) + ((bflo(v1.x) + bfhi(v1.x)) + (bflo(v1.y) + bfhi(v1.y)));
            s += __shfl_xor(s, 16); s += __shfl_xor(s, 32);
            if (lane < 16) st32(c.rs, WS_CSP + (unsigned)((mat * 20 + 2 * kb + (w & 1)) * F + 64 * cb + 16 * (w >> 1) + i16) * 4, __float_as_uint(s));
        } else {
            const bool isw2 = j < 810; const int jj = isw2 ? j - 540 : j - 810, kb = jj / 10, cb = jj % 10, ks = w & 1;
            lcptr a = blk + (32 * ks + 8 * g + q) * 128;
            const u32x2 v0 = __builtin_bit_cast(u32x2, ldtr(a)), v1 = __builtin_bit_cast(u32x2, ldtr(a + 512));
            const unsigned dst = isw2 ? WS_W2P + (unsigned)((4 * cb + (w >> 1)) * 54 + 2 * kb + ks) * 1024 : WS_WOP + (unsigned)((4 * cb + (w >> 1)) * 20 + 2 * kb + ks) * 1024;
            st128(c.rs, dst + lane * 16, (u32x4){v0.x, v0.y, v1.x, v1.y});
        }
    }
}

constexpr int P2_R = 4, P2_SLOT = 24576, P2_NS = 8;
constexpr int P2_Q = 110592;
constexpr int XCH = 135168;
constexpr int P2_EX = XCH;

DI void attn_safe(const Ctx& c, unsigned kv, s16x8 q0, s16x4 q1z, f32x16& o) {
    const int lane = c.lane, r = lane & 31, h = lane >> 5, vr = r < 24 ? r : r - 16;
    float m = -INFINITY; o = (f32x16){};
    for (int t = 0; t < 64; ++t) {
        const unsigned p = kv + t * 3072;
        const u32x4 k0 = lg128(c.rs, p + lane * 16); const u32x2 k1 = lg64(c.rs, p + 1024 + lane * 8);
        const u32x4 v0 = lg128(c.rs, p + 1536 + h * 384 + vr * 16), v1 = lg128(c.rs, p + 1536 + 768 + h * 384 + vr * 16);
        f32x16 s = MFMA32(__builtin_bit_cast(s16x8, k0), q0, (f32x16){}); s = MFMA32K8(__builtin_bit_cast(s16x4, k1), q1z, s);
        float tm = s[0];
#pragma unroll
        for (int i = 1; i < 16; ++i) tm = fmaxf(tm, s[i]);
        tm = swap32_max(tm);
        const float mn = fmaxf(m, tm), al = __builtin_amdgcn_exp2f(m - mn); m = mn;
#pragma unroll
        for (int i = 0; i < 16; ++i) { o[i] *= al; s[i] = __builtin_amdgcn_exp2f(s[i] - mn); }
        const u32x4 p0 = {cvtpk(s[0], s[1]), cvtpk(s[2], s[3]), cvtpk(s[4], s[5]), cvtpk(s[6], s[7])};
        const u32x4 p1 = {cvtpk(s[8], s[9]), cvtpk(s[10], s[11]), cvtpk(s[12], s[13]), cvtpk(s[14], s[15])};
        o = MFMA32(__builtin_bit_cast(s16x8, v0), __builtin_bit_cast(s16x8, p0), o); o = MFMA32(__builtin_bit_cast(s16x8, v1), __builtin_bit_cast(s16x8, p1), o);
    }
}

DI void phase2(const Ctx& c, bool pub_pk) {
    const int lane = c.lane, wid = c.wid, g = c.b & 7, idx = c.b >> 3, hh = 4 * g + (idx & 3), qb = idx >> 2;
    const int qg = wid & 3, kh = wid >> 2, r = lane & 31, h = lane >> 5;
    lptr lds = c.lds;
    const char* kvsrc = c.ws + WS_KV + (size_t)hh * 64 * 3072 + wid * 3072;
    const unsigned dst0 = c.lds0 + wid * 3072, voff = lane * 16;
#define P2_ISSUE(st, slot) do { const char* s_ = kvsrc + (size_t)(st) * 24576; dma16<true>(s_, voff, dst0 + (slot)); dma16<true>(s_ + 1024, voff, dst0 + (slot) + 1024); dma16<true>(s_ + 2048, voff, dst0 + (slot) + 2048); } while (0)
    { const char* qsrc = c.ws + WS_Q + ((size_t)hh * 64 + 8 * qb + wid) * 1536;
      dma16<true>(qsrc, voff, c.lds0 + P2_Q + wid * 1536); dma16m<true>(qsrc + 1024, voff, c.lds0 + P2_Q + wid * 1536 + 1024, 0xffffffffull); }
    P2_ISSUE(0, 0); P2_ISSUE(1, P2_SLOT);
    const int vr = r < 24 ? r : 21 + (r & 1);
    const unsigned ka = 4 * kh * 3072 + lane * 16, kb = 4 * kh * 3072 + 1024 + lane * 8, va = 4 * kh * 3072 + 1536 + h * 384 + vr * 16;
#define LDK0(slot, t) ld128((lcptr)lds + (slot) + ka + (t) * 3072)
#define LDK1(slot, t) ld64((lcptr)lds + (slot) + kb + (t) * 3072)
#define LDV0(slot, t) ld128((lcptr)lds + (slot) + va + (t) * 3072)
#define LDV1(slot, t) ld128((lcptr)lds + (slot) + va + (t) * 3072 + 768)
    WAIT_BAR(3);
    if (pub_pk && threadIdx.x == 0) st32(c.rs, flag_off(FL_PK, c.b), c.tag);
    STAMP(c.ws, 0, 3);
    P2_ISSUE(2, 2 * P2_SLOT);
    s16x8 q0[2]; s16x4 q1z[2], q1[2];
#pragma unroll
    for (int i = 0; i < 2; ++i) { q0[i] = ld128((lcptr)lds + P2_Q + (2 * qg + i) * 1536 + lane * 16); q1z[i] = ld64((lcptr)lds + P2_Q + (2 * qg + i) * 1536 + 1024 + lane * 8); }
    { const s16x8 k0 = ld128((lcptr)lds + lane * 16); const s16x4 k1 = ld64((lcptr)lds + 1024 + lane * 8);
#pragma unroll
      for (int i = 0; i < 2; ++i) { f32x16 s = MFMA32(k0, q0[i], (f32x16){}); s = MFMA32K8(k1, q1z[i], s);
          float m = s[0];
#pragma unroll
          for (int e = 1; e < 16; ++e) m = fmaxf(m, s[e]);
          m = swap32_max(m);
          q1[i] = q1z[i]; if (h) q1[i][0] = (short)(cvtpk(-m, 0.f) & 0xffffu); } }
    s16x8 kA0 = LDK0(0, 0), kB0, vA0 = LDV0(0, 0), vA1 = LDV1(0, 0), vB0, vB1 = vA1; s16x4 kA1 = LDK1(0, 0), kB1;
    f32x16 sA, sB, o[2] = {(f32x16){}, (f32x16){}};
    sA = MFMA32(kA0, q0[0], (f32x16){}); sA = MFMA32K8(kA1, q1[0], sA);
    u32x4 p0, p1 = {0u, 0u, 0u, 0u};
    int cur = 0, nxt = P2_SLOT, fre = (P2_R - 1) * P2_SLOT;
#define EX(v) __builtin_amdgcn_exp2f(v)
#define P2_STEP(C, N, K0, K1, NQ, OC, V0, OP, VP, LOADS, LAST) do { \
        OP = MFMA32(VP, __builtin_bit_cast(s16x8, p1), OP); SBAR(); \
        LOADS; SBAR(); \
        C[0] = EX(C[0]); C[1] = EX(C[1]); C[2] = EX(C[2]); C[3] = EX(C[3]); p0[0] = cvtpk(C[0], C[1]); p0[1] = cvtpk(C[2], C[3]); PIN(p0); SBAR(); \
        if (!(LAST)) N = MFMA32(K0, q0[NQ], (f32x16){}); SBAR(); \
        C[4] = EX(C[4]); C[5] = EX(C[5]); C[6] = EX(C[6]); C[7] = EX(C[7]); p0[2] = cvtpk(C[4], C[5]); p0[3] = cvtpk(C[6], C[7]); PIN(p0); SBAR(); \
        if (!(LAST)) N = MFMA32K8(K1, q1[NQ], N); SBAR(); \
        C[8] = EX(C[8]); C[9] = EX(C[9]); C[10] = EX(C[10]); C[11] = EX(C[11]); p1[0] = cvtpk(C[8], C[9]); p1[1] = cvtpk(C[10], C[11]); PIN(p1); SBAR(); \
        OC = MFMA32(V0, __builtin_bit_cast(s16x8, p0), OC); SBAR(); \
        C[12] = EX(C[12]); C[13] = EX(C[13]); C[14] = EX(C[14]); C[15] = EX(C[15]); p1[2] = cvtpk(C[12], C[13]); p1[3] = cvtpk(C[14], C[15]); PIN(p1); SBAR(); \
    } while (0)
#define P2_PIECE(st, k) do { if ((st) >= 0) dma16<true>(kvsrc + (size_t)(st) * 24576 + (k) * 1024, voff, dst0 + fre + (k) * 1024); } while (0)
#define P2_PAIR(T, NS_, NT, LAST, ST, KA, KB) do { \
        P2_STEP(sA, sB, kA0, kA1, 1, o[0], vA0, o[1], vB1, { kB0 = LDK0(cur, (T) + 1); kB1 = LDK1(cur, (T) + 1); vB0 = LDV0(cur, (T) + 1); vB1 = LDV1(cur, (T) + 1); }, false); \
        P2_STEP(sB, sA, kB0, kB1, 0, o[1], vA0, o[0], vA1, { P2_PIECE(ST, KA); }, false); \
        P2_STEP(sA, sB, kB0, kB1, 1, o[0], vB0, o[1], vA1, { if (!(LAST)) { kA0 = LDK0(NS_, NT); kA1 = LDK1(NS_, NT); vA0 = LDV0(NS_, NT); vA1 = LDV1(NS_, NT); } }, false); \
        P2_STEP(sB, sA, kA0, kA1, 0, o[1], vB0, o[0], vB1, { if ((KB) >= 0) P2_PIECE(ST, KB); }, LAST); } while (0)
#define P2_STAGE(LAST, ST) do { P2_PAIR(0, cur, 2, false, ST, 0, 1); P2_PAIR(2, nxt, 0, LAST, ST, 2, -1); \
        fre = cur; cur = nxt; nxt = (nxt == (P2_R - 1) * P2_SLOT) ? 0 : nxt + P2_SLOT; } while (0)
    for (int s = 0; s < P2_NS - 3; ++s) { WAIT_BAR(3); P2_STAGE(false, s + P2_R - 1); }
    WAIT_BAR(3);
    unsigned pk[4];
    if (wid == 0) {
#pragma unroll
        for (int i = 0; i < 4; ++i) pk[i] = lg32(c.rs, flag_off(FL_PK, 64 * i + lane));
    }
    P2_STAGE(false, -1);
    if (wid == 0) {
        for (unsigned spins = 0; spins < SPIN_CAP; ++spins) {
            if (__all(pk[0] == c.tag && pk[1] == c.tag && pk[2] == c.tag && pk[3] == c.tag)) break;
            __builtin_amdgcn_s_sleep(1);
#pragma unroll
            for (int i = 0; i < 4; ++i) pk[i] = lg32(c.rs, flag_off(FL_PK, 64 * i + lane));
        }
    }
    WAIT_BAR(0);
    const unsigned wsc = c.lds0 + P2_Q + 12288 + wid * 1024;
    unsigned wu[6];
#pragma unroll
    for (int i = 0; i < 6; ++i) { const int L = imin((idx * 8 + wid) * 6 + i, 1519);
        wu[i] = (L < 400 ? WS_WOP + (g & 1) * 409600 + L * 1024 : WS_W13P + imin((g >> 1) * 1146880 + (L - 400) * 1024, 108 * 40960 - 1024)) + lane * 16; }
#define P2_WARM(i) do { if (WARM2) dma16<true>(c.ws, wu[i], wsc); } while (0)
    P2_PAIR(0, cur, 2, false, -1, 0, 1); P2_WARM(0); P2_WARM(1); P2_PAIR(2, nxt, 0, false, -1, 2, -1); P2_WARM(2);
    fre = cur; cur = nxt; nxt = (nxt == (P2_R - 1) * P2_SLOT) ? 0 : nxt + P2_SLOT;
    P2_WARM(3); P2_PAIR(0, cur, 2, false, -1, 0, 1); P2_WARM(4); P2_WARM(5); P2_PAIR(2, nxt, 0, true, -1, 2, -1);
#undef P2_WARM
    o[1] = MFMA32(vB1, __builtin_bit_cast(s16x8, p1), o[1]);
#undef P2_STEP
#undef P2_PAIR
#undef P2_PIECE
#undef P2_STAGE
#undef P2_ISSUE
    STAMP(c.ws, 0, 4);
    LAS f32x4* ex = (LAS f32x4*)(lds + P2_EX);
    { const unsigned m = kh ? 0xffffffffu : 0u;
#pragma unroll
      for (int i = 0; i < 3; ++i) { const f32x4 a = {o[0][4 * i], o[0][4 * i + 1], o[0][4 * i + 2], o[0][4 * i + 3]}, e = {o[1][4 * i], o[1][4 * i + 1], o[1][4 * i + 2], o[1][4 * i + 3]};
          ex[(wid * 3 + i) * 64 + lane] = sel4(m, a, e); }
      LDS_BAR();
      STAMP(c.ws, 3, 3);
      f32x16 of = {};
#pragma unroll
      for (int i = 0; i < 3; ++i) { const f32x4 a = {o[0][4 * i], o[0][4 * i + 1], o[0][4 * i + 2], o[0][4 * i + 3]}, e = {o[1][4 * i], o[1][4 * i + 1], o[1][4 * i + 2], o[1][4 * i + 3]};
          const f32x4 t = sel4(m, e, a) + ex[((wid ^ 4) * 3 + i) * 64 + lane];
          of[4 * i] = t[0]; of[4 * i + 1] = t[1]; of[4 * i + 2] = t[2]; of[4 * i + 3] = t[3]; }
      float l = upper_half(of[8]);
      bool bad = !(l < 1.8446744e19f);
#pragma unroll
      for (int i = 0; i < 12; ++i) bad |= !(fabsf(of[i]) < 3.0e38f);
      if (__any(bad)) {
          const s16x8 qq0 = ld128((lcptr)lds + P2_Q + (2 * qg + kh) * 1536 + lane * 16); const s16x4 qq1 = ld64((lcptr)lds + P2_Q + (2 * qg + kh) * 1536 + 1024 + lane * 8);
          attn_safe(c, WS_KV + (unsigned)hh * 64 * 3072, qq0, qq1, of); l = upper_half(of[8]); }
      const float il = __builtin_amdgcn_rcpf(l);
      const unsigned a0 = cvtpk(of[0] * il, of[1] * il), a1 = cvtpk(of[2] * il, of[3] * il), b0 = cvtpk(of[4] * il, of[5] * il), b1 = cvtpk(of[6] * il, of[7] * il);
      auto x0 = __builtin_amdgcn_permlane32_swap(a0, b0, false, false), x1 = __builtin_amdgcn_permlane32_swap(a1, b1, false, false);
      const unsigned t16 = 2 * (8 * qb + 2 * qg + kh) + (r >> 4);
      st128(c.rs, WS_O + (t16 * 80 + 2 * hh + h) * 256 + (r & 15) * 16, (u32x4){x0[0], x1[0], x0[1], x1[1]});
      if (h == 0) st64(c.rs, WS_O + (t16 * 80 + 64 + (hh >> 1)) * 256 + (r & 15) * 16 + (hh & 1) * 8, (u32x2){cvtpk(of[8] * il, of[9] * il), cvtpk(of[10] * il, of[11] * il)});
    }
#undef LDK0
#undef LDK1
#undef LDV0
#undef LDV1
#undef EX
}

constexpr int PJ_R = 5;
template <int NKS, int KSS> struct PJ {
    static constexpr int NS = NKS / KSS, SLOT = 5 * KSS * 1024, NWI = (5 * KSS + 7) / 8, NA = NKS / 2, PRE = imin(PJ_R, NS);
};
template <int NKS, int KSS> DI int pj_rho(int b) { return ROTJ ? (((b >> 3) & 7) + 2 * (b >> 6)) % (NKS / KSS) : 0; }
template <int NKS, int KSS> DI void pj_issue_w(const Ctx& c, const unsigned (&voff)[(5 * KSS + 7) / 8], int st) {
    using P = PJ<NKS, KSS>;
    const char* base = c.ws + rot(st, pj_rho<NKS, KSS>(c.b), P::NS) * (KSS * 1024);
#pragma unroll
    for (int i = 0; i < P::NWI; ++i) dma16<true>(base, voff[i], c.lds0 + (st % PJ_R) * P::SLOT + imin(c.wid + 8 * i, 5 * KSS - 1) * 1024);
}
template <int NKS, int KSS> DI void pj_voff(const Ctx& c, unsigned wbase, int cc, unsigned (&voff)[(5 * KSS + 7) / 8]) {
#pragma unroll
    for (int i = 0; i < PJ<NKS, KSS>::NWI; ++i) { const int f = imin(c.wid + 8 * i, 5 * KSS - 1); voff[i] = wbase + (unsigned)((5 * cc + f / KSS) * NKS + f % KSS) * 1024 + c.lane * 16; }
}
DI int pj_pp(int b) { return 8 * ((b & 7) >> 1) + ((b >> 3) & 7); }
DI int pj_cc(int b) { return 4 * (b & 1) + (b >> 6); }
template <int NKS, int KSS> DI void pj_prefetch(const Ctx& c, unsigned wbase) {
    unsigned voff[PJ<NKS, KSS>::NWI]; pj_voff<NKS, KSS>(c, wbase, pj_cc(c.b), voff);
#pragma unroll
    for (int s = 0; s < PJ<NKS, KSS>::PRE; ++s) pj_issue_w<NKS, KSS>(c, voff, s);
}

template <int NA, int WIN, int HK, int NS, int NWI, int NRES> constexpr int pj_younger(int j) {
    int n = imin(NA - 1, j + WIN) - j;
    if (j < WIN) n += NRES;
    for (int s = 1; s + PJ_R - 1 < NS; ++s)
        if (HK * s <= j && (j < WIN || HK * s > j - WIN)) n += NWI;
    return n;
}
template <int NA, int WIN, int HK, int NS, int NWI> constexpr int pj_bar_younger(int s) {
    const int s0 = s + 2 - PJ_R;
    int n = 0;
    for (int t = s0 + 1; t < s; ++t) if (t + PJ_R - 1 < NS) n += NWI;
    for (int j = HK * s0; j < HK * s; ++j) if (j + WIN < NA) ++n;
    return n;
}
template <int NKS, int KSS, bool FIRST, int KH>
DI void pj_body(const Ctx& c, unsigned abase, unsigned wbase, u32x2 (&res)[3]) {
    using P = PJ<NKS, KSS>;
    constexpr int NS = P::NS, NG = NKS * 4, NA = P::NA, HK = KSS / 2, NCT = KH ? 2 : 3, CT0 = KH ? 3 : 0;
    const int lane = c.lane, wid = c.wid, t16l = wid & 3, r = lane & 15, gq = lane >> 4, pp = pj_pp(c.b), cc = pj_cc(c.b);
    lptr lds = c.lds;
    const unsigned t16 = 4 * pp + t16l;
    const int tok = 16 * t16 + r;
    constexpr int WIN = imin(NA, FIRST ? 4 : 9);
    s16x8 a[NA];
    const unsigned aoff = abase + (t16 * NG + 4 * KH) * 256 + lane * 16;
    const int rho = pj_rho<NKS, KSS>(c.b);
#define PJ_ABASE(j) (c.ws + rot((j) / HK, rho, NS) * (KSS * 1024) + ((j) % HK) * 2048)
#pragma unroll
    for (int j = 0; j < WIN; ++j) ldg_async(a[j], PJ_ABASE(j), aoff);
    if (FIRST) {
#pragma unroll
        for (int i = 0; i < NCT; ++i) res[i] = *(const GAS u32x2*)(c.x + (size_t)tok * 1280 + cc * 160 + (CT0 + i) * 32 + gq * 8);
    }
    unsigned voff[P::NWI]; pj_voff<NKS, KSS>(c, wbase, cc, voff);
    constexpr int NRES = FIRST ? NCT : 0;
    const unsigned wa = KH * 1024 + lane * 16;
#define PJ_READ(W, st, i) do { lcptr b_ = (lcptr)lds + ((st) % PJ_R) * P::SLOT + wa + (i) * 2048; \
        _Pragma("unroll") for (int ct = 0; ct < 5; ++ct) W[ct] = ld128(b_ + ct * KSS * 1024); } while (0)
    f32x4 acc[5];
#pragma unroll
    for (int ct = 0; ct < 5; ++ct) acc[ct] = (f32x4){};
    STAMP(c.ws, 3, FIRST ? 0 : 4);
    s16x8 w[2][5];
    PJ_READ(w[0], 0, 0);
#pragma unroll
    for (int s = 0; s < NS; ++s) {
        if (NS > PJ_R && s >= 1) {
            if (s + 1 >= PJ_R && s + 1 < NS) WAIT_BAR((pj_bar_younger<NA, WIN, HK, NS, P::NWI>(s))); else LDS_BAR();
            SBAR();
            if (s + PJ_R - 1 < NS) pj_issue_w<NKS, KSS>(c, voff, s + PJ_R - 1);
        }
#pragma unroll
        for (int i = 0; i < HK; ++i) {
            const int j = s * HK + i;
            if (i + 1 < HK) PJ_READ(w[(j + 1) & 1], s, i + 1); else if (s + 1 < NS) PJ_READ(w[(j + 1) & 1], s + 1, 0);
            if (j + WIN < NA) ldg_async(a[j + WIN], PJ_ABASE(j + WIN), aoff);
            WAIT_USE((pj_younger<NA, WIN, HK, NS, P::NWI, NRES>(j)), a[j]);
            SBAR();
#pragma unroll
            for (int ct = 0; ct < 5; ++ct) acc[ct] = MFMA16(w[j & 1][ct], a[j], acc[ct]);
            SBAR();
        }
    }
#undef PJ_READ
#undef PJ_ABASE
    STAMP(c.ws, 3, FIRST ? 1 : 5);
    LDS_BAR();
    LAS f32x4* red = (LAS f32x4*)(lds + XCH);
#pragma unroll
    for (int i = 0; i < 5 - NCT; ++i) red[(wid * 3 + i) * 64 + lane] = acc[(KH ? 0 : 3) + i];
    LDS_BAR();
    STAMP(c.ws, 3, FIRST ? 2 : 6);
    float s1 = 0.f, s2 = 0.f;
#pragma unroll
    for (int i = 0; i < NCT; ++i) {
        const f32x4 o = red[((wid ^ 4) * 3 + i) * 64 + lane], m = acc[CT0 + i];
        const unsigned w0 = cvtpk(bflo(res[i].x) + m[0] + o[0], bfhi(res[i].x) + m[1] + o[1]), w1 = cvtpk(bflo(res[i].y) + m[2] + o[2], bfhi(res[i].y) + m[3] + o[3]);
        if (FIRST) {
            res[i] = (u32x2){w0, w1};
            st64(c.rs, WS_H + (t16 * 80 + 10 * cc + 2 * (CT0 + i) + (gq >> 1)) * 256 + r * 16 + (gq & 1) * 8, res[i]);
            const float a_ = bflo(w0), b_ = bfhi(w0), c_ = bflo(w1), d = bfhi(w1); s1 += (a_ + b_) + (c_ + d); s2 += (a_ * a_ + b_ * b_) + (c_ * c_ + d * d);
        } else {
            *(GAS u32x2*)(c.out + (size_t)tok * 1280 + cc * 160 + (CT0 + i) * 32 + gq * 8) = (u32x2){w0, w1};
        }
    }
    if (FIRST) {
        s1 += __shfl_xor(s1, 16); s2 += __shfl_xor(s2, 16); s1 = swap32_add(s1); s2 = swap32_add(s2);
        LAS f32x2* ex = (LAS f32x2*)(lds + XCH + 2048);
        if (KH == 1 && gq == 0) ex[t16l * 16 + r] = (f32x2){s1, s2};
        LDS_BAR();
        if (KH == 0 && gq == 0) { const f32x2 e = ex[t16l * 16 + r];
            st64(c.rs, WS_HST + ((t16 * 8 + cc) * 16 + r) * 8, (u32x2){__float_as_uint(s1 + e.x), __float_as_uint(s2 + e.y)}); }
    }
}
template <int NKS, int KSS, bool FIRST>
DI void phase_proj(const Ctx& c, unsigned abase, unsigned wbase, u32x2 (&res)[3]) {
    if (c.wid < 4) pj_body<NKS, KSS, FIRST, 0>(c, abase, wbase, res);
    else pj_body<NKS, KSS, FIRST, 1>(c, abase, wbase, res);
}

constexpr int P4_R = 7, P4_SLOT = 22528, P4_NS = 20;
constexpr int P4_CS = XCH + 24576;

DI void p4_wvoff(const Ctx& c, int fc, unsigned& woff0, unsigned& woff1) {
    const unsigned o0 = c.wid * 1792 + c.lane * 16, o1 = c.wid * 1792 + 1024 + (c.lane < 48 ? c.lane : 47) * 16;
    woff0 = WS_W13P + (unsigned)imin(7 * fc + (o0 >> 11), 107) * 40960 + (o0 & 2047); woff1 = WS_W13P + (unsigned)imin(7 * fc + (o1 >> 11), 107) * 40960 + (o1 & 2047);
}
DI int p4_rho(int b) { return ROT4 == 1 ? (((b >> 3) & 7) + 5 * (b >> 6)) % P4_NS : ROT4 == 2 ? 5 * (b >> 6) : ROT4 == 3 ? ((b >> 3) & 7) : 0; }
DI void p4_issue_w(const Ctx& c, unsigned woff0, unsigned woff1, int st) {
    const char* w_ = c.ws + rot(st, p4_rho(c.b), P4_NS) * 2048; const unsigned wdst = c.lds0 + 8192 + c.wid * 1792 + (st % P4_R) * P4_SLOT;
    dma16<true>(w_, woff0, wdst); dma16m<true>(w_, woff1, wdst + 1024, 0xffffffffffffull);
}
DI void p4_prefetch(const Ctx& c, int fc) {
    unsigned woff0, woff1; p4_wvoff(c, fc, woff0, woff1);
#pragma unroll
    for (int s = 0; s < P4_R - 1; ++s) p4_issue_w(c, woff0, woff1, s);
}
constexpr int p4_extra(int s) { int n = 0; for (int t = s - 5 < 0 ? 0 : s - 5; t < s; ++t) if (WARM4 && t <= 14 && t % 3 == 2) ++n; return n; }
template <int UG>
DI void p4_body(const Ctx& c, int qb, int fc) {
    constexpr int NU = UG ? 3 : 4, U0 = UG ? 4 : 0;
    const int lane = c.lane, wid = c.wid, tt = wid & 3, r = lane & 31, h = lane >> 5;
    lptr lds = c.lds;
    const unsigned adst = c.lds0 + wid * 1024;
    const unsigned aoff = WS_H + (unsigned)(8 * qb + wid) * 80 * 256 + lane * 16;
    unsigned woff0, woff1; p4_wvoff(c, fc, woff0, woff1);
    const int rho = p4_rho(c.b);
#pragma unroll
    for (int s = 0; s < P4_R - 1; ++s) dma16<true>(c.ws + rot(s, rho, P4_NS) * 1024, aoff, adst + s * P4_SLOT);
    const unsigned xa = ((2 * tt + (r >> 4)) * 4 + h) * 256 + (r & 15) * 16, wa = 8192 + U0 * 2048 + lane * 16;
    const unsigned t16 = 8 * qb + 2 * tt + (r >> 4);
    u32x2 hs[4];
#pragma unroll
    for (int i = 0; i < 4; ++i) hs[i] = lg64(c.rs, WS_HST + ((t16 * 8 + 4 * h + i) * 16 + (r & 15)) * 8);
#define P4_PIECE(st, k) do { const int p_ = rot(st, rho, P4_NS); const unsigned sl_ = ((st) % P4_R) * P4_SLOT; \
        if ((k) == 0) dma16<true>(c.ws + p_ * 1024, aoff, adst + sl_); \
        if ((k) == 1) dma16<true>(c.ws + p_ * 2048, woff0, c.lds0 + 8192 + wid * 1792 + sl_); \
        if ((k) == 2) dma16m<true>(c.ws + p_ * 2048, woff1, c.lds0 + 8192 + wid * 1792 + sl_ + 1024, 0xffffffffffffull); } while (0)
#define P4_READ(n) do { if ((ABL & 32) && (n) > 2) break; lcptr b_ = (lcptr)lds + (((n) >> 1) % P4_R) * P4_SLOT; X[(n) % 3] = ld128(b_ + xa + ((n) & 1) * 512); \
        _Pragma("unroll") for (int u = 0; u < NU; ++u) W[(n) % 3][u] = ld128(b_ + wa + u * 2048 + ((n) & 1) * 1024); } while (0)
    f32x16 acc[NU];
#pragma unroll
    for (int u = 0; u < NU; ++u) acc[u] = (f32x16){};
    WAIT_BAR((P4_R - 2) + 4);
    s16x8 X[3], W[3][NU];
    STG_DECL(); STG(0);
    P4_READ(0); P4_READ(1);
#pragma unroll
    for (int s = 0; s < P4_NS; ++s) {
        if (!(ABL & 16)) WAIT_BARV(fly2(s, P4_R, P4_NS, 1, 2) + (s + 1 < P4_R - 1 ? 4 : 0) + p4_extra(s));
        STG(1 + s);
        SBAR();
#define P4_NEXT(k) do { if (s + P4_R - 1 < P4_NS && !(ABL & 8)) P4_PIECE(s + P4_R - 1, k); SBAR(); } while (0)
#pragma unroll
        for (int ks = 0; ks < 2; ++ks) {
            const int n = 2 * s + ks;
            if (n + 2 < 2 * P4_NS) P4_READ(n + 2);
            SBAR();
            if (ABL & 64) { PIN(X[n % 3]); _Pragma("unroll") for (int u = 0; u < NU; ++u) PIN(W[n % 3][u]); P4_NEXT(ks); if (ks) P4_NEXT(2); }
            else {
                acc[0] = MFMA32(W[n % 3][0], X[n % 3], acc[0]); SBAR(); P4_NEXT(ks);
                acc[1] = MFMA32(W[n % 3][1], X[n % 3], acc[1]); acc[2] = MFMA32(W[n % 3][2], X[n % 3], acc[2]); SBAR();
                if (ks) P4_NEXT(2);
                if (NU > 3) acc[NU - 1] = MFMA32(W[n % 3][NU - 1], X[n % 3], acc[NU - 1]);
            }
            SBAR();
        }
#undef P4_NEXT
        if (WARM4 && s <= 14 && s % 3 == 2) {
            const int L = imin(((c.b >> 3) * 8 + wid) * 5 + s / 3, 1079);
            dma16<true>(c.ws, WS_W2P + (c.b & 1) * 1105920 + L * 1024 + lane * 16, c.lds0 + P4_R * P4_SLOT + 1024); SBAR(); }
    }
    STG(21); STG_OUT(c.ws, 1, 22);
#undef P4_READ
    LDS_BAR();
    STAMP(c.ws, 1, 2);
    float s1 = (__uint_as_float(hs[0].x) + __uint_as_float(hs[1].x)) + (__uint_as_float(hs[2].x) + __uint_as_float(hs[3].x));
    float s2 = (__uint_as_float(hs[0].y) + __uint_as_float(hs[1].y)) + (__uint_as_float(hs[2].y) + __uint_as_float(hs[3].y));
    s1 = swap32_add(s1); s2 = swap32_add(s2);
    const float mean = s1 * (1.0f / D), var = fmaxf(s2 * (1.0f / D) - mean * mean, 0.f), rstd = __builtin_amdgcn_rsqf(var + LN_EPS);
    const f32x2 nm = {-mean, -mean}, k1 = {-1.4426950408889634f * rstd, -1.4426950408889634f * rstd}, r2 = {rstd * rstd, rstd * rstd}, one = {1.0f, 1.0f};
    const LAS float* csl = (const LAS float*)(lds + P4_CS);
#pragma unroll
    for (int u = 0; u < NU; ++u) {
        if (7 * fc + U0 + u < 108) {
            u32x2 w[2];
#pragma unroll
            for (int gq = 0; gq < 2; ++gq) {
                const f32x4 cg_ = *(const LAS f32x4*)(csl + (U0 + u) * 16 + 8 * gq + 4 * h), cu = *(const LAS f32x4*)(csl + 128 + (U0 + u) * 16 + 8 * gq + 4 * h);
                f32x2 o[2];
#pragma unroll
                for (int e = 0; e < 2; ++e) {
                    const f32x2 a = __builtin_elementwise_fma(nm, (f32x2){cg_[2 * e], cg_[2 * e + 1]}, (f32x2){acc[u][4 * gq + 2 * e], acc[u][4 * gq + 2 * e + 1]});
                    const f32x2 b2 = __builtin_elementwise_fma(nm, (f32x2){cu[2 * e], cu[2 * e + 1]}, (f32x2){acc[u][8 + 4 * gq + 2 * e], acc[u][8 + 4 * gq + 2 * e + 1]});
                    const f32x2 t = a * k1, d = (f32x2){__builtin_amdgcn_exp2f(t.x), __builtin_amdgcn_exp2f(t.y)} + one;
                    const f32x2 rc = {__builtin_amdgcn_rcpf(d.x), __builtin_amdgcn_rcpf(d.y)};
                    o[e] = (a * b2) * (rc * r2);
                }
                w[gq] = (u32x2){cvtpk(o[0].x, o[0].y), cvtpk(o[1].x, o[1].y)};
            }
            auto x0 = __builtin_amdgcn_permlane32_swap(w[0].x, w[1].x, false, false), x1 = __builtin_amdgcn_permlane32_swap(w[0].y, w[1].y, false, false);
            st128(c.rs, WS_HM + (t16 * 216 + 14 * fc + 2 * (U0 + u) + h) * 256 + (r & 15) * 16, (u32x4){x0[0], x1[0], x0[1], x1[1]});
        }
    }
}
DI void phase4(const Ctx& c, int qb, int fc) {
    if (c.wid < 4) p4_body<0>(c, qb, fc); else p4_body<1>(c, qb, fc);
}
DI void p4_colsums(const Ctx& c, int fc) {
    const int tid = (int)threadIdx.x - 256;
    if (tid >= 0 && tid < 224) { const int mat = tid >= 112, cl = tid - 112 * mat, col = imin(fc * 112 + cl, F - 1);
        float v[20];
#pragma unroll
        for (int i = 0; i < 20; ++i) v[i] = __uint_as_float(lg32(c.rs, WS_CSP + (unsigned)((mat * 20 + i) * F + col) * 4));
        float s = 0.f;
#pragma unroll
        for (int i = 0; i < 20; ++i) s += v[i];
        *(LAS float*)(c.lds + P4_CS + (mat * 128 + cl) * 4) = s; }
}

template <int PH0, int PH1>
__global__ __launch_bounds__(512, 2) void block_fwd(const char* __restrict__ x, const char* __restrict__ wqkv, const char* __restrict__ wo, const char* __restrict__ w1,
                                                    const char* __restrict__ w3, const char* __restrict__ w2, char* __restrict__ out, char* __restrict__ ws) {
    extern __shared__ __attribute__((aligned(16))) char smem[];
    Ctx c; c.x = x; c.wqkv = wqkv; c.wo = wo; c.w1 = w1; c.w3 = w3; c.w2 = w2; c.out = out; c.ws = ws;
    c.rs = __builtin_amdgcn_make_buffer_rsrc(ws, 0, WS_END, 0x00020000);
    c.lds = (lptr)smem; c.lds0 = (unsigned)(uintptr_t)smem; c.b = blockIdx.x; c.lane = threadIdx.x & 63; c.wid = __builtin_amdgcn_readfirstlane(threadIdx.x >> 6);
    const int lane = c.lane, g = c.b & 7, idx = c.b >> 3;
    STAMP(ws, 0, 0);
    c.tag = *(const __attribute__((address_space(4))) unsigned*)(ws + WS_E) + 1u;
    if (PH0 > 0) asm volatile("" : "+s"(c.tag));
    u32x2 res[3] = {(u32x2){0u, 0u}, (u32x2){0u, 0u}, (u32x2){0u, 0u}};
    if (PH0 <= 0) {
        phase1(c);
        STAMP(c.ws, 2, 5);
        STAMP(ws, 0, 1);
        WAIT_VM(0);
        publish(c, FL_KV);
        repack_store(c);
        if (PH1 == 0) { WAIT_VM(0); publish(c, FL_PK); }
        STAMP(ws, 0, 2);
    }
    const int qb4 = 8 * (g & 1) + (idx & 7), fc = 4 * (g >> 1) + (idx >> 3), pp = pj_pp(c.b);
    if (PH0 <= 1 && PH1 >= 1) {
        { const int hh = 4 * g + (idx & 3), qb = idx >> 2;
          const int wg = lane < 16 ? wg_p1(lane, (32 + hh) / 6) : lane < 32 ? wg_p1(lane - 16, (64 + hh) / 6) : wg_p1(2 * qb + (lane & 1), hh / 6);
          const unsigned off[1] = {flag_off(FL_KV, wg)}; wait_flags(c, off); }
        phase2(c, PH0 == 0);
        STAMP(ws, 2, 6);
        WAIT_VM(0);
        STAMP(ws, 3, 7);
        publish(c, FL_O);
        if (PH1 >= 2) pj_prefetch<20, 4>(c, WS_WOP);
        STAMP(ws, 0, 5);
    }
    if (PH0 <= 2 && PH1 >= 2) {
        if (PH0 == 2) pj_prefetch<20, 4>(c, WS_WOP);
        if (PH1 >= 3) p4_colsums(c, fc);
        WAIT_VM(0);
        { const unsigned off[1] = {flag_off(FL_O, wg_p2(lane & 31, pp >> 2))}; wait_flags(c, off); }
        STAMP(ws, 0, 6);
        phase_proj<20, 4, true>(c, WS_O, WS_WOP, res);
        WAIT_VM(0);
        publish(c, FL_H);
        if (PH1 >= 3) p4_prefetch(c, fc);
        STAMP(ws, 0, 7);
    }
    if (PH0 <= 3 && PH1 >= 3) {
        if (PH0 == 3) { p4_colsums(c, fc); p4_prefetch(c, fc); }
        { const unsigned off[1] = {flag_off(FL_H, wg_p3(2 * qb4 + (lane & 1), (lane >> 1) & 7))}; wait_flags(c, off); }
        STAMP(ws, 1, 1);
        phase4(c, qb4, fc);
        STAMP(ws, 2, 7);
        WAIT_VM(0);
        STAMP(ws, 1, 6);
        publish(c, FL_M);
        if (PH1 >= 4) pj_prefetch<54, 6>(c, WS_W2P);
        STAMP(ws, 1, 3);
    }
    if (PH0 <= 4 && PH1 >= 4) {
        if (PH0 == 4) pj_prefetch<54, 6>(c, WS_W2P);
        WAIT_VM(0);
        const unsigned fm = flag_off(FL_M, wg_p4(pp >> 1, lane & 15));
        if (c.b == 0) {
            const unsigned off[5] = {fm, flag_off(FL_O, lane), flag_off(FL_O, 64 + lane), flag_off(FL_O, 128 + lane), flag_off(FL_O, 192 + lane)}; wait_flags(c, off);
        } else { const unsigned off[1] = {fm}; wait_flags(c, off); }
        STAMP(ws, 1, 4);
        if (PH0 > 2) {
            const int cc = pj_cc(c.b), ct0 = c.wid >> 2 ? 3 : 0, gq = lane >> 4;
#pragma unroll
            for (int i = 0; i < 3; ++i) res[i] = lg64(c.rs, WS_H + ((4 * pp + (c.wid & 3)) * 80 + 10 * cc + 2 * imin(ct0 + i, 4) + (gq >> 1)) * 256 + (lane & 15) * 16 + (gq & 1) * 8);
            WAIT_VM(0); PIN(res[0]); PIN(res[1]); PIN(res[2]);
        }
        phase_proj<54, 6, false>(c, WS_HM, WS_W2P, res);
        STAMP(ws, 1, 5);
        if (c.b == 0 && threadIdx.x == 0) st32(c.rs, WS_E, c.tag);
    }
}

}

extern "C" void kernel_launch(void* const* d_in, const int* in_sizes, int n_in, void* d_out, int out_size, void* d_ws, size_t ws_size, hipStream_t stream) {
    using namespace blk;
    const char* x = (const char*)d_in[0]; const char* wqkv = (const char*)d_in[1]; const char* wo = (const char*)d_in[2];
    const char* w1 = (const char*)d_in[4]; const char* w3 = (const char*)d_in[5]; const char* w2 = (const char*)d_in[6];
    char* ws = (char*)d_ws; char* out = (char*)d_out;
#define LAUNCH(A, B) do { hipFuncSetAttribute((const void*)block_fwd<A, B>, hipFuncAttributeMaxDynamicSharedMemorySize, LDS_BYTES); \
        block_fwd<A, B><<<256, 512, LDS_BYTES, stream>>>(x, wqkv, wo, w1, w3, w2, out, ws); } while (0)
    LAUNCH(0, 4);
#undef LAUNCH
}
```

```cpp
#include <hip/hip_runtime.h>
#include <hip/hip_bf16.h>
#include <stdint.h>

namespace blk {

constexpr int S = 2048, D = 640, H = 32, F = 1728;
constexpr float LN_EPS = 1e-5f;
constexpr float QSCALE = 0.22360679774997896f * 1.4426950408889634f;

#define LAS __attribute__((address_space(3)))
#define GAS __attribute__((address_space(1)))
#define DI __device__ __forceinline__
typedef short s16x8 __attribute__((ext_vector_type(8)));
typedef short s16x4 __attribute__((ext_vector_type(4)));
typedef float f32x16 __attribute__((ext_vector_type(16)));
typedef float f32x4 __attribute__((ext_vector_type(4)));
typedef float f32x2 __attribute__((ext_vector_type(2)));
typedef unsigned u32x4 __attribute__((ext_vector_type(4)));
typedef unsigned u32x2 __attribute__((ext_vector_type(2)));
typedef __bf16 bf16x2 __attribute__((ext_vector_type(2)));
typedef LAS char* lptr;
typedef LAS const char* lcptr;
typedef __amdgpu_buffer_rsrc_t rsrc_t;

constexpr unsigned WS_E = 0;
constexpr unsigned WS_FLAG = 4096;
constexpr unsigned WS_ROWP = 256u << 10;
constexpr unsigned WS_COLP = 512u << 10;
constexpr unsigned WS_Q = 1u << 20;
constexpr unsigned WS_KV = 4u << 20;
constexpr unsigned WS_O = 12u << 20;
constexpr unsigned WS_H = 16u << 20;
constexpr unsigned WS_HST = 20u << 20;
constexpr unsigned WS_CSP = 21u << 20;
constexpr unsigned WS_HM = 24u << 20;
constexpr unsigned WS_W13P = 32u << 20;
constexpr unsigned WS_W2P = 40u << 20;
constexpr unsigned WS_WOP = 44u << 20;
constexpr unsigned WS_END = 48u << 20;
enum { FL_KV = 0, FL_PK = 1, FL_O = 2, FL_H = 3, FL_M = 4 };
constexpr int LDS_BYTES = 163840;
constexpr unsigned SPIN_CAP = 1u << 16;

#define STAMP(ws, kid, pt) do { } while (0)
#define STAMPW(ws, kid, pt, wv) do { } while (0)
#define ABL 0
#define STG_DECL() do { } while (0)
#define STG(i) do { } while (0)
#define STGD(i) do { } while (0)
#define STG_OUT(ws, kid, n) do { } while (0)
#define SBAR() __builtin_amdgcn_sched_barrier(0)
#define PIN(x) asm volatile("" : "+v"(x))
#define MFMA32(a, b, c) __builtin_amdgcn_mfma_f32_32x32x16_bf16(a, b, c, 0, 0, 0)
#define MFMA32K8(a, b, c) __builtin_amdgcn_mfma_f32_32x32x8bf16_1k(a, b, c, 0, 0, 0)
#define MFMA16(a, b, c) __builtin_amdgcn_mfma_f32_16x16x32_bf16(a, b, c, 0, 0, 0)
#define WAIT_BAR(N) asm volatile("s_waitcnt vmcnt(%0) lgkmcnt(0)\n\ts_barrier" :: "i"(N) : "memory")
#define WAIT_BARV(N) asm volatile("s_waitcnt vmcnt(%0)\n\ts_barrier" :: "i"(N) : "memory")
#define WAIT_VM(N) asm volatile("s_waitcnt vmcnt(%0)" :: "i"(N) : "memory")
#define LDS_BAR() asm volatile("s_waitcnt lgkmcnt(0)\n\ts_barrier" ::: "memory")

#define SCON true
template <bool SC> DI void dma16(const void* sbase, unsigned voff, unsigned ldsaddr) {
    if (SC && SCON) asm volatile("s_mov_b32 m0, %2\n\ts_nop 0\n\tglobal_load_lds_dwordx4 %0, %1 sc1" :: "v"(voff), "s"(sbase), "s"(ldsaddr) : "memory");
    else asm volatile("s_mov_b32 m0, %2\n\ts_nop 0\n\tglobal_load_lds_dwordx4 %0, %1" :: "v"(voff), "s"(sbase), "s"(ldsaddr) : "memory");
}
template <bool SC> DI void dma16m(const void* sbase, unsigned voff, unsigned ldsaddr, unsigned long long mask) {
    if (SC && SCON) asm volatile("s_mov_b32 m0, %2\n\ts_mov_b64 exec, %3\n\tglobal_load_lds_dwordx4 %0, %1 sc1\n\ts_mov_b64 exec, -1" :: "v"(voff), "s"(sbase), "s"(ldsaddr), "s"(mask) : "memory");
    else asm volatile("s_mov_b32 m0, %2\n\ts_mov_b64 exec, %3\n\tglobal_load_lds_dwordx4 %0, %1\n\ts_mov_b64 exec, -1" :: "v"(voff), "s"(sbase), "s"(ldsaddr), "s"(mask) : "memory");
}
DI void touch_lines(const void* sbase, unsigned voff, unsigned ldsaddr) {
    asm volatile("s_mov_b32 m0, %2\n\ts_nop 0\n\tglobal_load_lds_dword %0, %1" :: "v"(voff), "s"(sbase), "s"(ldsaddr) : "memory");
}
constexpr int imin(int a, int b) { return a < b ? a : b; }
constexpr int imax(int a, int b) { return a > b ? a : b; }
constexpr int fly(int s, int R, int NS) { return imax(0, imin(R - 3, NS - 2 - s)); }
constexpr int fly2(int s, int R, int NS, int na, int nw) {
    const int need = imin(s + 1, NS - 1);
    const int pro = imin(R - 1, NS);
    const int issued = pro * na + imax(0, imin(s, NS - R + 1)) * (na + nw);
    const int endpos = need < pro ? (need + 1) * na : pro * na + (need - pro + 1) * (na + nw);
    return imax(0, issued - endpos);
}
#define WARM2 1
#define WARM4 1
#define ROT1 1
#define ROT4 0
#define ROTJ 0
DI int rot(int i, int rho, int ns) { const int t = i + rho; return t >= ns ? t - ns : t; }
DI unsigned cvtpk(float lo, float hi) { f32x2 v = {lo, hi}; return __builtin_bit_cast(unsigned, __builtin_convertvector(v, bf16x2)); }
DI float bflo(unsigned w) { return __uint_as_float(w << 16); }
DI float bfhi(unsigned w) { return __uint_as_float(w & 0xffff0000u); }
DI s16x8 ld128(lcptr p) { return *(const LAS s16x8*)p; }
DI s16x4 ld64(lcptr p) { return *(const LAS s16x4*)p; }
DI s16x4 ldtr(lcptr p) { return __builtin_bit_cast(s16x4, __builtin_amdgcn_ds_read_tr16_b64_v4i16((LAS s16x4*)p)); }
DI s16x8 cat(s16x4 a, s16x4 b) { return __builtin_shufflevector(a, b, 0, 1, 2, 3, 4, 5, 6, 7); }
DI f32x4 sel4(unsigned m, f32x4 a, f32x4 b) { const u32x4 x = __builtin_bit_cast(u32x4, a), y = __builtin_bit_cast(u32x4, b); return __builtin_bit_cast(f32x4, (x & m) | (y & ~m)); }
DI float swap32_add(float v) { auto r = __builtin_amdgcn_permlane32_swap(__float_as_uint(v), __float_as_uint(v), false, false); return __uint_as_float(r[0]) + __uint_as_float(r[1]); }
DI float swap32_max(float v) { auto r = __builtin_amdgcn_permlane32_swap(__float_as_uint(v), __float_as_uint(v), false, false); return fmaxf(__uint_as_float(r[0]), __uint_as_float(r[1])); }
DI float upper_half(float v) { auto r = __builtin_amdgcn_permlane32_swap(__float_as_uint(v), __float_as_uint(v), false, false); return __uint_as_float(r[1]); }
DI void st128(rsrc_t rs, unsigned off, u32x4 v) { __builtin_amdgcn_raw_buffer_store_b128(v, rs, off, 0, 16); }
DI void st64(rsrc_t rs, unsigned off, u32x2 v) { __builtin_amdgcn_raw_buffer_store_b64(v, rs, off, 0, 16); }
DI void st32(rsrc_t rs, unsigned off, unsigned v) { __builtin_amdgcn_raw_buffer_store_b32(v, rs, off, 0, 16); }
DI u32x4 lg128(rsrc_t rs, unsigned off) { return __builtin_amdgcn_raw_buffer_load_b128(rs, off, 0, 16); }
DI u32x2 lg64(rsrc_t rs, unsigned off) { return __builtin_amdgcn_raw_buffer_load_b64(rs, off, 0, 16); }
DI unsigned lg32(rsrc_t rs, unsigned off) { return __builtin_amdgcn_raw_buffer_load_b32(rs, off, 0, 16); }

DI void ldg_async(s16x8& dst, const void* sbase, unsigned voff) { asm volatile("global_load_dwordx4 %0, %1, %2 sc1" : "=v"(dst) : "v"(voff), "s"(sbase) : "memory"); }
#define WAIT_USE(N, x) asm volatile("s_waitcnt vmcnt(%1)" : "+v"(x) : "i"(N) : "memory")
struct Ctx { const char *x, *wqkv, *wo, *w1, *w3, *w2; char *out, *ws; rsrc_t rs; lptr lds; unsigned lds0, tag; int b, lane, wid; };

DI unsigned flag_off(int kind, int wg) { return WS_FLAG + (unsigned)(kind * 256 + wg) * 128u; }
DI void publish(const Ctx& c, int kind) {
    asm volatile("s_barrier" ::: "memory");
    if (threadIdx.x == 0) st32(c.rs, flag_off(kind, c.b), c.tag);
}
template <int N> DI void wait_flags(const Ctx& c, const unsigned (&off)[N]) {
    if (c.wid == 0) {
        for (unsigned spins = 0; spins < SPIN_CAP; ++spins) {
            unsigned v[N];
#pragma unroll
            for (int i = 0; i < N; ++i) v[i] = lg32(c.rs, off[i]);
            bool ok = true;
#pragma unroll
            for (int i = 0; i < N; ++i) ok &= v[i] == c.tag;
            if (__all(ok)) break;
            __builtin_amdgcn_s_sleep(1);
        }
    }
    asm volatile("s_waitcnt lgkmcnt(0)\n\ts_barrier" ::: "memory");
}
DI int wg_p1(int pb, int nb) { return ((pb >> 3) | ((nb >> 2) << 1)) + 8 * ((pb & 7) + 8 * (nb & 3)); }
DI int wg_p2(int hh, int qb) { return (hh >> 2) + 8 * ((hh & 3) + 4 * qb); }
DI int wg_p3(int pp, int cc) { return (((pp >> 3) << 1) | (cc >> 2)) + 8 * ((pp & 7) + 8 * (cc & 3)); }
DI int wg_p4(int qb, int fc) { return ((qb >> 3) | ((fc >> 2) << 1)) + 8 * ((qb & 7) + 8 * (fc & 3)); }

constexpr int P1_R = 5, P1_SLOT = 32768, P1_NS = 10;
constexpr int P1_STG = 264;
constexpr int P1_OUT = 65536;
constexpr int P1_ST = P1_OUT + 40960;
constexpr int P1_CS = P1_ST + 1024;

DI void repack_issue_one(const Ctx& c, int i);
template <int UP>
DI void p1_loop(Ctx& c, unsigned xoff, unsigned woff, int tt, int rho, unsigned put, unsigned get, f32x16& acc0, f32x16& acc1, u32x2 (&part)[10]) {
    f32x16 accx = {};
    const int lane = c.lane, wid = c.wid, r = lane & 31, h = lane >> 5, q = (lane >> 2) & 3, p = lane & 3, cg = (lane >> 4) & 1;
    constexpr unsigned long long WMASK = ~((1ull << 15) | (1ull << 27) | (1ull << 39) | (1ull << 51));
    const unsigned xdst = c.lds0 + wid * 2048, wdst = c.lds0 + 16384 + wid * 2048;
    const int xrow = 32 * tt + r;
    const unsigned xa = xrow * 128 + ((h ^ ((xrow >> 1) & 7)) * 16);
    const unsigned wk = 16384 + (8 * h + q) * 256 + (p & 1) * 8;
    const unsigned wa0 = wk + (((4 * (2 * UP) + 2 * cg + (p >> 1)) ^ (4 * q)) * 16);
    const unsigned wa1 = wk + (((4 * (2 * UP + 1) + 2 * cg + (p >> 1)) ^ (4 * q)) * 16);
    const unsigned waz = wk + (((4 * tt + 2 * cg + (p >> 1)) ^ (4 * q)) * 16);
    const s16x8 ones = {0x3f80, 0x3f80, 0x3f80, 0x3f80, 0x3f80, 0x3f80, 0x3f80, 0x3f80};
#define P1_PIECE(st, slot, k) do { if (ABL & 1) break; const int p_ = rot(st, rho, P1_NS); \
        if ((k) == 0) dma16<false>(c.x + p_ * 128, xoff, xdst + (slot)); \
        if ((k) == 1) dma16<false>(c.x + p_ * 128 + 8 * 1280, xoff ^ 64, xdst + (slot) + 1024); \
        if ((k) == 2) dma16m<false>(c.wqkv + (size_t)p_ * (64 * 3840), woff, wdst + (slot), WMASK); \
        if ((k) == 3) dma16m<false>(c.wqkv + (size_t)p_ * (64 * 3840) + 4 * 3840, woff, wdst + (slot) + 1024, WMASK); } while (0)
#define P1_ISSUE(st, slot) do { P1_PIECE(st, slot, 0); P1_PIECE(st, slot, 1); P1_PIECE(st, slot, 2); P1_PIECE(st, slot, 3); } while (0)
#define P1_READ(n) do { if (ABL & 2) break; lcptr b_ = (lcptr)c.lds + (((n) >> 2) % P1_R) * P1_SLOT; \
        X[(n) % 3] = ld128(b_ + (xa ^ (((n) & 3) * 32))); \
        W0[(n) % 3] = cat(ldtr(b_ + wa0 + ((n) & 3) * 4096), ldtr(b_ + wa0 + ((n) & 3) * 4096 + 1024)); \
        W1[(n) % 3] = cat(ldtr(b_ + wa1 + ((n) & 3) * 4096), ldtr(b_ + wa1 + ((n) & 3) * 4096 + 1024)); \
        if (UP && (n) < 4) WZ[(n) & 3] = cat(ldtr(b_ + waz + ((n) & 3) * 4096), ldtr(b_ + waz + ((n) & 3) * 4096 + 1024)); } while (0)
#define P1_MMA(n) do { if (ABL & 4) { PIN(X[(n) % 3]); PIN(W0[(n) % 3]); PIN(W1[(n) % 3]); break; } \
        acc0 = MFMA32(W0[(n) % 3], X[(n) % 3], acc0); acc1 = MFMA32(W1[(n) % 3], X[(n) % 3], acc1); \
        if ((n) < 4) { if (UP) accx = MFMA32(WZ[(n) & 3], ones, accx); else accx = MFMA32(X[(n) % 3], X[(n) % 3], accx); } } while (0)
    P1_ISSUE(0, 0); P1_ISSUE(1, P1_SLOT); P1_ISSUE(2, 2 * P1_SLOT);
    WAIT_BAR(8);
    asm volatile("" : "+s"(c.tag));
    STAMP(c.ws, 2, 1);
    P1_ISSUE(3, 3 * P1_SLOT);
    s16x8 X[3] = {}, W0[3] = {}, W1[3] = {}, WZ[4] = {};
    STG_DECL(); STG(0);
    P1_READ(0); P1_READ(1);
#pragma unroll
    for (int s = 0; s < P1_NS; ++s) {
        constexpr int NX = P1_R - 1, GS = P1_NS - P1_R;
        WAIT_BARV(4 * fly(s, P1_R, P1_NS) + (s >= 1 && s + 1 <= P1_R - 1 ? 1 : 0) + (s > GS && !UP ? 10 : 0) + imax(0, s - GS - 1));
        STG(1 + s);
        SBAR();
#pragma unroll
        for (int ks = 0; ks < 4; ++ks) {
            const int n = 4 * s + ks;
            if (n + 2 < 4 * P1_NS) P1_READ(n + 2);
            SBAR(); P1_MMA(n); SBAR();
            if (s + NX < P1_NS) P1_PIECE(s + NX, ((s + NX) % P1_R) * P1_SLOT, ks);
            if (s > GS && ks == 1) repack_issue_one(c, s - GS - 1);
            SBAR();
        }
        if (s == 0) {
            const int tr = (r & 3) + 4 * (r >> 3); float v = 0.f;
#pragma unroll
            for (int i = 0; i < 16; ++i) v = (tr == i) ? accx[i] : v;
            st64(c.rs, put + (h == ((r >> 2) & 1) ? (tt * 32 + r) * 8 : 0x7fffff00u), (u32x2){__float_as_uint(v), c.tag});
            SBAR();
        }
        if (s == GS && !UP) {
#pragma unroll
            for (int i = 0; i < 10; ++i) part[i] = lg64(c.rs, get + i * 1024);
            SBAR();
        }
    }
    STG(11); STG_OUT(c.ws, 0, 12);
#undef P1_PIECE
#undef P1_ISSUE
#undef P1_READ
#undef P1_MMA
}

DI void phase1(Ctx& c) {
    const int lane = c.lane, wid = c.wid, tid = threadIdx.x, g = c.b & 7, idx = c.b >> 3;
    const int pb = 8 * (g & 1) + (idx & 7), nb = 4 * (g >> 1) + (idx >> 3);
    const int tt = wid & 3, up = wid >> 2, r = lane & 31, h = lane >> 5;
    lptr lds = c.lds;
    if (tid < P1_R * 64) { const int sl = tid >> 6, k = tid & 63;
        *(LAS u32x4*)(lds + sl * P1_SLOT + 16384 + k * 256 + ((15 ^ (4 * (k & 3))) * 16)) = (u32x4){0x3f803f80u, 0x3f803f80u, 0x3f803f80u, 0x3f803f80u}; }
    const int xr = 16 * wid + (lane >> 3), xc = (lane & 7) ^ ((xr >> 1) & 7);
    const unsigned xoff = (pb * 128 + xr) * 1280 + xc * 16;
    const int wkk = 8 * wid + (lane >> 4), wp = (lane & 15) ^ (4 * (wkk & 3)), wpc = wp < 15 ? wp : 14;
    const unsigned woff = wkk * 3840 + nb * 240 + wpc * 16;
    f32x16 acc0 = {}, acc1 = {};
    STAMP(c.ws, 2, 0);
    const int rho = ROT1 ? (pb + 3 * nb) % P1_NS : 0;
    const unsigned put = up ? WS_COLP + (unsigned)(nb * 16 + pb) * 1024 : WS_ROWP + (unsigned)(pb * 16 + nb) * 1024;
    const int gi = (wid & 1) * 64 + lane;
    const unsigned get = ((wid & 2) ? WS_COLP + (unsigned)nb * 16384 : WS_ROWP + (unsigned)pb * 16384) + gi * 8;
    u32x2 part[10];
    if (up == 0) p1_loop<0>(c, xoff, woff, tt, rho, put, get, acc0, acc1, part);
    else p1_loop<1>(c, xoff, woff, tt, rho, put, get, acc0, acc1, part);
    STAMP(c.ws, 2, 2);
    float psum = 0.f;
    if (up == 0) {
        for (unsigned spins = 0; spins < SPIN_CAP; ++spins) {
            bool ok = true;
#pragma unroll
            for (int i = 0; i < 10; ++i) ok &= part[i].y == c.tag;
            if (__all(ok)) break;
            __builtin_amdgcn_s_sleep(1);
#pragma unroll
            for (int i = 0; i < 10; ++i) part[i] = lg64(c.rs, get + i * 1024);
        }
#pragma unroll
        for (int i = 0; i < 10; ++i) psum += __uint_as_float(part[i].x);
    }
    LDS_BAR();
    LAS float* st = (LAS float*)(lds + P1_ST); LAS float* cs = (LAS float*)(lds + P1_CS);
    if (wid < 2) st[gi * 2 + 1] = psum; else if (wid < 4) cs[gi] = psum;
    if (up == 1 && h == 0) st[(tt * 32 + r) * 2] = acc1[12];
    LDS_BAR();
    STAMP(c.ws, 2, 3);
    { const f32x2 sv = *(const LAS f32x2*)(st + (tt * 32 + r) * 2);
      const float mean = sv.x * (1.0f / D), var = fmaxf(sv.y * (1.0f / D) - mean * mean, 0.f), rstd = __builtin_amdgcn_rsqf(var + LN_EPS), rm = rstd * mean;
      lptr stg = lds + P1_OUT + (32 * tt + r) * P1_STG;
#pragma unroll
      for (int u = 0; u < 2; ++u)
#pragma unroll
        for (int gq = 0; gq < 4; ++gq) {
            const int col = (2 * up + u) * 32 + 8 * gq + 4 * h;
            const f32x4 c4 = *(const LAS f32x4*)(cs + col);
            const float sc = (nb * 120 + col < D) ? QSCALE : 1.0f, a = rstd * sc, bb = rm * sc;
            float v[4];
#pragma unroll
            for (int e = 0; e < 4; ++e) v[e] = a * (u ? acc1[4 * gq + e] : acc0[4 * gq + e]) - bb * c4[e];
            *(LAS u32x2*)(stg + col * 2) = (u32x2){cvtpk(v[0], v[1]), cvtpk(v[2], v[3])};
        }
    }
    LDS_BAR();
    STAMP(c.ws, 2, 4);
#pragma unroll
    for (int j = 0; j < 3; ++j) {
        const int slot = up + 2 * j, gidx = 6 * nb + slot, type = gidx >> 5, head = gidx & 31, tile = 4 * pb + tt;
        if (type < 2) {
            lcptr row = (lcptr)lds + P1_OUT + (32 * tt + r) * P1_STG + slot * 40;
            const u32x2 a0 = *(const LAS u32x2*)(row + 16 * h), a1 = *(const LAS u32x2*)(row + 16 * h + 8), a2 = *(const LAS u32x2*)(row + 32);
            const unsigned dst = type ? WS_KV + (unsigned)(head * 64 + tile) * 3072 : WS_Q + (unsigned)(head * 64 + tile) * 1536;
            st128(c.rs, dst + lane * 16, (u32x4){a0.x, a0.y, a1.x, a1.y});
            st64(c.rs, dst + 1024 + lane * 8, h ? (u32x2){type ? 0x00003f80u : 0u, 0u} : a2);
        } else {
            const int q = (lane >> 2) & 3, p = lane & 3, cg = (lane >> 4) & 1;
            lcptr vb = (lcptr)lds + P1_OUT + (32 * tt + 4 * h + q) * P1_STG + slot * 40 + (cg * 4 + p < 5 ? (cg * 4 + p) * 8 : 0);
            const unsigned dst = WS_KV + (unsigned)(head * 64 + tile) * 3072 + 1536;
#pragma unroll
            for (int s = 0; s < 2; ++s) {
                const u32x2 a = __builtin_bit_cast(u32x2, ldtr(vb + (16 * s) * P1_STG)), e = __builtin_bit_cast(u32x2, ldtr(vb + (16 * s + 8) * P1_STG));
                u32x4 v = {a.x, a.y, e.x, e.y};
                if (r == 20) v = (u32x4){0x3f803f80u, 0x3f803f80u, 0x3f803f80u, 0x3f803f80u};
                if (r > 20) v = (u32x4){0u, 0u, 0u, 0u};
                if (r < 24) st128(c.rs, dst + s * 768 + h * 384 + r * 16, v);
            }
        }
    }
}

constexpr int PK_OFF = 0;
DI int pk_job(int b, int i) { const int j = b + 256 * i; return j < 910 ? j : j - 256; }
DI void repack_issue_one(const Ctx& c, int i) {
    const int row = 8 * c.wid + (c.lane >> 3), pc = c.lane & 7;
    const int j = pk_job(c.b, i);
    const char* src; unsigned off;
    if (j < 540) { const int jj = j < 270 ? j : j - 270, kb = jj / 27, cb = jj % 27; src = j < 270 ? c.w1 : c.w3; off = (64 * kb + row) * 3456 + cb * 128 + pc * 16; }
    else if (j < 810) { const int jj = j - 540, kb = jj / 10, cb = jj % 10; src = c.w2; off = (64 * kb + row) * 1280 + cb * 128 + pc * 16; }
    else { const int jj = j - 810, kb = jj / 10, cb = jj % 10, kp = 64 * kb + row, G = kp >> 3, e = kp & 7;
        const int k = G < 64 ? 20 * (G >> 1) + 8 * (G & 1) + e : 40 * (G - 64) + 16 + (e < 4 ? e : e + 16);
        src = c.wo; off = k * 1280 + cb * 128 + pc * 16; }
    dma16<false>(src, off, c.lds0 + PK_OFF + i * 8192 + c.wid * 1024);
}
DI void repack_store(const Ctx& c) {
    const int lane = c.lane, w = c.wid, g = lane >> 4, i16 = lane & 15, q = i16 >> 2, p = i16 & 3;
#pragma unroll
    for (int i = 0; i < 4; ++i) {
        const int j = pk_job(c.b, i);
        lcptr blk = (lcptr)c.lds + PK_OFF + i * 8192 + (w >> 1) * 32 + p * 8;
        if (j < 540) {
            const int mat = j >= 270, jj = mat ? j - 270 : j, kb = jj / 27, cb = jj % 27, ks = 2 * (w & 1) + (g >> 1), hh = g & 1;
            lcptr a = blk + (16 * ks + 8 * hh + q) * 128;
            const u32x2 v0 = __builtin_bit_cast(u32x2, ldtr(a)), v1 = __builtin_bit_cast(u32x2, ldtr(a + 512));
            st128(c.rs, WS_W13P + (unsigned)((4 * cb + (w >> 1)) * 40 + 4 * kb + ks) * 1024 + (mat * 16 + i16 + 32 * hh) * 16, (u32x4){v0.x, v0.y, v1.x, v1.y});
            float s = ((bflo(v0.x) + bfhi(v0.x)) + (bflo(v0.y) + bfhi(v0.y))) + ((bflo(v1.x) + bfhi(v1.x)) + (bflo(v1.y) + bfhi(v1.y)));
            s += __shfl_xor(s, 16); s += __shfl_xor(s, 32);
            if (lane < 16) st32(c.rs, WS_CSP + (unsigned)((mat * 20 + 2 * kb + (w & 1)) * F + 64 * cb + 16 * (w >> 1) + i16) * 4, __float_as_uint(s));
        } else {
            const bool isw2 = j < 810; const int jj = isw2 ? j - 540 : j - 810, kb = jj / 10, cb = jj % 10, ks = w & 1;
            lcptr a = blk + (32 * ks + 8 * g + q) * 128;
            const u32x2 v0 = __builtin_bit_cast(u32x2, ldtr(a)), v1 = __builtin_bit_cast(u32x2, ldtr(a + 512));
            const unsigned dst = isw2 ? WS_W2P + (unsigned)((4 * cb + (w >> 1)) * 54 + 2 * kb + ks) * 1024 : WS_WOP + (unsigned)((4 * cb + (w >> 1)) * 20 + 2 * kb + ks) * 1024;
            st128(c.rs, dst + lane * 16, (u32x4){v0.x, v0.y, v1.x, v1.y});
        }
    }
}

constexpr int P2_R = 4, P2_SLOT = 24576, P2_NS = 8;
constexpr int P2_Q = 110592;
constexpr int XCH = 135168;
constexpr int P2_EX = XCH;
constexpr int P4_CS = XCH + 24576;

DI void attn_safe(const Ctx& c, unsigned kv, s16x8 q0, s16x4 q1z, f32x16& o) {
    const int lane = c.lane, r = lane & 31, h = lane >> 5, vr = r < 24 ? r : r - 16;
    float m = -INFINITY; o = (f32x16){};
    for (int t = 0; t < 64; ++t) {
        const unsigned p = kv + t * 3072;
        const u32x4 k0 = lg128(c.rs, p + lane * 16); const u32x2 k1 = lg64(c.rs, p + 1024 + lane * 8);
        const u32x4 v0 = lg128(c.rs, p + 1536 + h * 384 + vr * 16), v1 = lg128(c.rs, p + 1536 + 768 + h * 384 + vr * 16);
        f32x16 s = MFMA32(__builtin_bit_cast(s16x8, k0), q0, (f32x16){}); s = MFMA32K8(__builtin_bit_cast(s16x4, k1), q1z, s);
        float tm = s[0];
#pragma unroll
        for (int i = 1; i < 16; ++i) tm = fmaxf(tm, s[i]);
        tm = swap32_max(tm);
        const float mn = fmaxf(m, tm), al = __builtin_amdgcn_exp2f(m - mn); m = mn;
#pragma unroll
        for (int i = 0; i < 16; ++i) { o[i] *= al; s[i] = __builtin_amdgcn_exp2f(s[i] - mn); }
        const u32x4 p0 = {cvtpk(s[0], s[1]), cvtpk(s[2], s[3]), cvtpk(s[4], s[5]), cvtpk(s[6], s[7])};
        const u32x4 p1 = {cvtpk(s[8], s[9]), cvtpk(s[10], s[11]), cvtpk(s[12], s[13]), cvtpk(s[14], s[15])};
        o = MFMA32(__builtin_bit_cast(s16x8, v0), __builtin_bit_cast(s16x8, p0), o); o = MFMA32(__builtin_bit_cast(s16x8, v1), __builtin_bit_cast(s16x8, p1), o);
    }
}

DI void phase2(const Ctx& c, bool pub_pk) {
    const int lane = c.lane, wid = c.wid, g = c.b & 7, idx = c.b >> 3, hh = 4 * g + (idx & 3), qb = idx >> 2;
    const int qg = wid & 3, kh = wid >> 2, r = lane & 31, h = lane >> 5;
    lptr lds = c.lds;
    const char* kvsrc = c.ws + WS_KV + (size_t)hh * 64 * 3072 + wid * 3072;
    const unsigned dst0 = c.lds0 + wid * 3072, voff = lane * 16;
#define P2_ISSUE(st, slot) do { const char* s_ = kvsrc + (size_t)(st) * 24576; dma16<true>(s_, voff, dst0 + (slot)); dma16<true>(s_ + 1024, voff, dst0 + (slot) + 1024); dma16<true>(s_ + 2048, voff, dst0 + (slot) + 2048); } while (0)
    { const char* qsrc = c.ws + WS_Q + ((size_t)hh * 64 + 8 * qb + wid) * 1536;
      dma16<true>(qsrc, voff, c.lds0 + P2_Q + wid * 1536); dma16m<true>(qsrc + 1024, voff, c.lds0 + P2_Q + wid * 1536 + 1024, 0xffffffffull); }
    P2_ISSUE(0, 0); P2_ISSUE(1, P2_SLOT);
    const int vr = r < 24 ? r : 21 + (r & 1);
    const unsigned ka = 4 * kh * 3072 + lane * 16, kb = 4 * kh * 3072 + 1024 + lane * 8, va = 4 * kh * 3072 + 1536 + h * 384 + vr * 16;
#define LDK0(slot, t) ld128((lcptr)lds + (slot) + ka + (t) * 3072)
#define LDK1(slot, t) ld64((lcptr)lds + (slot) + kb + (t) * 3072)
#define LDV0(slot, t) ld128((lcptr)lds + (slot) + va + (t) * 3072)
#define LDV1(slot, t) ld128((lcptr)lds + (slot) + va + (t) * 3072 + 768)
    WAIT_BAR(3);
    if (pub_pk && threadIdx.x == 0) st32(c.rs, flag_off(FL_PK, c.b), c.tag);
    STAMP(c.ws, 0, 3);
    P2_ISSUE(2, 2 * P2_SLOT);
    s16x8 q0[2]; s16x4 q1z[2], q1[2];
#pragma unroll
    for (int i = 0; i < 2; ++i) { q0[i] = ld128((lcptr)lds + P2_Q + (2 * qg + i) * 1536 + lane * 16); q1z[i] = ld64((lcptr)lds + P2_Q + (2 * qg + i) * 1536 + 1024 + lane * 8); }
    { const s16x8 k0 = ld128((lcptr)lds + lane * 16); const s16x4 k1 = ld64((lcptr)lds + 1024 + lane * 8);
#pragma unroll
      for (int i = 0; i < 2; ++i) { f32x16 s = MFMA32(k0, q0[i], (f32x16){}); s = MFMA32K8(k1, q1z[i], s);
          float m = s[0];
#pragma unroll
          for (int e = 1; e < 16; ++e) m = fmaxf(m, s[e]);
          m = swap32_max(m);
          q1[i] = q1z[i]; if (h) q1[i][0] = (short)(cvtpk(-m, 0.f) & 0xffffu); } }
    s16x8 kA0 = LDK0(0, 0), kB0, vA0 = LDV0(0, 0), vA1 = LDV1(0, 0), vB0, vB1 = vA1; s16x4 kA1 = LDK1(0, 0), kB1;
    f32x16 sA, sB, o[2] = {(f32x16){}, (f32x16){}};
    sA = MFMA32(kA0, q0[0], (f32x16){}); sA = MFMA32K8(kA1, q1[0], sA);
    u32x4 p0, p1 = {0u, 0u, 0u, 0u};
    int cur = 0, nxt = P2_SLOT, fre = (P2_R - 1) * P2_SLOT;
    STG_DECL(); STG(0);
#define EX(v) __builtin_amdgcn_exp2f(v)
#define P2_STEP(C, N, K0, K1, NQ, OC, V0, OP, VP, LOADS, LAST) do { \
        OP = MFMA32(VP, __builtin_bit_cast(s16x8, p1), OP); SBAR(); \
        LOADS; SBAR(); \
        C[0] = EX(C[0]); C[1] = EX(C[1]); C[2] = EX(C[2]); C[3] = EX(C[3]); p0[0] = cvtpk(C[0], C[1]); p0[1] = cvtpk(C[2], C[3]); PIN(p0); SBAR(); \
        if (!(LAST)) N = MFMA32(K0, q0[NQ], (f32x16){}); SBAR(); \
        C[4] = EX(C[4]); C[5] = EX(C[5]); C[6] = EX(C[6]); C[7] = EX(C[7]); p0[2] = cvtpk(C[4], C[5]); p0[3] = cvtpk(C[6], C[7]); PIN(p0); SBAR(); \
        if (!(LAST)) N = MFMA32K8(K1, q1[NQ], N); SBAR(); \
        C[8] = EX(C[8]); C[9] = EX(C[9]); C[10] = EX(C[10]); C[11] = EX(C[11]); p1[0] = cvtpk(C[8], C[9]); p1[1] = cvtpk(C[10], C[11]); PIN(p1); SBAR(); \
        OC = MFMA32(V0, __builtin_bit_cast(s16x8, p0), OC); SBAR(); \
        C[12] = EX(C[12]); C[13] = EX(C[13]); C[14] = EX(C[14]); C[15] = EX(C[15]); p1[2] = cvtpk(C[12], C[13]); p1[3] = cvtpk(C[14], C[15]); PIN(p1); SBAR(); \
    } while (0)
#define P2_PIECE(st, k) do { if ((st) >= 0) dma16<true>(kvsrc + (size_t)(st) * 24576 + (k) * 1024, voff, dst0 + fre + (k) * 1024); } while (0)
#define P2_PAIR(T, NS_, NT, LAST, ST, KA, KB) do { \
        P2_STEP(sA, sB, kA0, kA1, 1, o[0], vA0, o[1], vB1, { kB0 = LDK0(cur, (T) + 1); kB1 = LDK1(cur, (T) + 1); vB0 = LDV0(cur, (T) + 1); vB1 = LDV1(cur, (T) + 1); }, false); \
        P2_STEP(sB, sA, kB0, kB1, 0, o[1], vA0, o[0], vA1, { P2_PIECE(ST, KA); }, false); \
        P2_STEP(sA, sB, kB0, kB1, 1, o[0], vB0, o[1], vA1, { if (!(LAST)) { kA0 = LDK0(NS_, NT); kA1 = LDK1(NS_, NT); vA0 = LDV0(NS_, NT); vA1 = LDV1(NS_, NT); } }, false); \
        P2_STEP(sB, sA, kA0, kA1, 0, o[1], vB0, o[0], vB1, { if ((KB) >= 0) P2_PIECE(ST, KB); }, LAST); } while (0)
#define P2_STAGE(LAST, ST) do { P2_PAIR(0, cur, 2, false, ST, 0, 1); P2_PAIR(2, nxt, 0, LAST, ST, 2, -1); \
        fre = cur; cur = nxt; nxt = (nxt == (P2_R - 1) * P2_SLOT) ? 0 : nxt + P2_SLOT; } while (0)
    for (int s = 0; s < P2_NS - 3; ++s) { WAIT_BAR(3); STGD(1 + s); P2_STAGE(false, s + P2_R - 1); }
    WAIT_BAR(3);
    STG(6);
    unsigned pk[4];
    if (wid == 0) {
#pragma unroll
        for (int i = 0; i < 4; ++i) pk[i] = lg32(c.rs, flag_off(FL_PK, 64 * i + lane));
    }
    P2_STAGE(false, -1);
    if (wid == 0) {
        for (unsigned spins = 0; spins < SPIN_CAP; ++spins) {
            if (__all(pk[0] == c.tag && pk[1] == c.tag && pk[2] == c.tag && pk[3] == c.tag)) break;
            __builtin_amdgcn_s_sleep(1);
#pragma unroll
            for (int i = 0; i < 4; ++i) pk[i] = lg32(c.rs, flag_off(FL_PK, 64 * i + lane));
        }
    }
    WAIT_BAR(0);
    STG(7);
    const unsigned wsc = c.lds0 + P2_Q + 12288 + wid * 1024;
    unsigned wu[6];
#pragma unroll
    for (int i = 0; i < 6; ++i) { const int L = imin((idx * 8 + wid) * 6 + i, 1519);
        wu[i] = (L < 400 ? WS_WOP + (g & 1) * 409600 + L * 1024 : WS_W13P + imin((g >> 1) * 1146880 + (L - 400) * 1024, 108 * 40960 - 1024)) + lane * 16; }
#define P2_WARM(i) do { if (WARM2) dma16<true>(c.ws, wu[i], wsc); } while (0)
    P2_PAIR(0, cur, 2, false, -1, 0, 1); P2_WARM(0); P2_WARM(1); P2_PAIR(2, nxt, 0, false, -1, 2, -1); P2_WARM(2);
    fre = cur; cur = nxt; nxt = (nxt == (P2_R - 1) * P2_SLOT) ? 0 : nxt + P2_SLOT;
    P2_WARM(3); P2_PAIR(0, cur, 2, false, -1, 0, 1); P2_WARM(4); P2_WARM(5); P2_PAIR(2, nxt, 0, true, -1, 2, -1);
#undef P2_WARM
    o[1] = MFMA32(vB1, __builtin_bit_cast(s16x8, p1), o[1]);
#undef P2_STEP
#undef P2_PAIR
#undef P2_PIECE
#undef P2_STAGE
#undef P2_ISSUE
    STG(8); STG_OUT(c.ws, 2, 9);
    STAMP(c.ws, 0, 4);
    LAS f32x4* ex = (LAS f32x4*)(lds + P2_EX);
    { const unsigned m = kh ? 0xffffffffu : 0u;
#pragma unroll
      for (int i = 0; i < 3; ++i) { const f32x4 a = {o[0][4 * i], o[0][4 * i + 1], o[0][4 * i + 2], o[0][4 * i + 3]}, e = {o[1][4 * i], o[1][4 * i + 1], o[1][4 * i + 2], o[1][4 * i + 3]};
          ex[(wid * 3 + i) * 64 + lane] = sel4(m, a, e); }
      LDS_BAR();
      STAMP(c.ws, 3, 3);
      f32x16 of = {};
#pragma unroll
      for (int i = 0; i < 3; ++i) { const f32x4 a = {o[0][4 * i], o[0][4 * i + 1], o[0][4 * i + 2], o[0][4 * i + 3]}, e = {o[1][4 * i], o[1][4 * i + 1], o[1][4 * i + 2], o[1][4 * i + 3]};
          const f32x4 t = sel4(m, e, a) + ex[((wid ^ 4) * 3 + i) * 64 + lane];
          of[4 * i] = t[0]; of[4 * i + 1] = t[1]; of[4 * i + 2] = t[2]; of[4 * i + 3] = t[3]; }
      float l = upper_half(of[8]);
      bool bad = !(l < 1.8446744e19f);
#pragma unroll
      for (int i = 0; i < 12; ++i) bad |= !(fabsf(of[i]) < 3.0e38f);
      if (__any(bad)) {
          const s16x8 qq0 = ld128((lcptr)lds + P2_Q + (2 * qg + kh) * 1536 + lane * 16); const s16x4 qq1 = ld64((lcptr)lds + P2_Q + (2 * qg + kh) * 1536 + 1024 + lane * 8);
          attn_safe(c, WS_KV + (unsigned)hh * 64 * 3072, qq0, qq1, of); l = upper_half(of[8]); }
      const float il = __builtin_amdgcn_rcpf(l);
      const unsigned a0 = cvtpk(of[0] * il, of[1] * il), a1 = cvtpk(of[2] * il, of[3] * il), b0 = cvtpk(of[4] * il, of[5] * il), b1 = cvtpk(of[6] * il, of[7] * il);
      auto x0 = __builtin_amdgcn_permlane32_swap(a0, b0, false, false), x1 = __builtin_amdgcn_permlane32_swap(a1, b1, false, false);
      const unsigned t16 = 2 * (8 * qb + 2 * qg + kh) + (r >> 4);
      st128(c.rs, WS_O + (t16 * 80 + 2 * hh + h) * 256 + (r & 15) * 16, (u32x4){x0[0], x1[0], x0[1], x1[1]});
      if (h == 0) st64(c.rs, WS_O + (t16 * 80 + 64 + (hh >> 1)) * 256 + (r & 15) * 16 + (hh & 1) * 8, (u32x2){cvtpk(of[8] * il, of[9] * il), cvtpk(of[10] * il, of[11] * il)});
    }
#undef LDK0
#undef LDK1
#undef LDV0
#undef LDV1
#undef EX
}

constexpr int PJ_R = 5;
template <int NKS, int KSS> struct PJ {
    static constexpr int NS = NKS / KSS, SLOT = 5 * KSS * 1024, NWI = (5 * KSS + 7) / 8, NA = NKS / 2, PRE = imin(PJ_R, NS);
};
template <int NKS, int KSS> DI int pj_rho(int b) { return ROTJ ? (((b >> 3) & 7) + 2 * (b >> 6)) % (NKS / KSS) : 0; }
template <int NKS, int KSS> DI void pj_issue_w(const Ctx& c, const unsigned (&voff)[(5 * KSS + 7) / 8], int st) {
    using P = PJ<NKS, KSS>;
    const char* base = c.ws + rot(st, pj_rho<NKS, KSS>(c.b), P::NS) * (KSS * 1024);
#pragma unroll
    for (int i = 0; i < P::NWI; ++i) dma16<true>(base, voff[i], c.lds0 + (st % PJ_R) * P::SLOT + imin(c.wid + 8 * i, 5 * KSS - 1) * 1024);
}
template <int NKS, int KSS> DI void pj_voff(const Ctx& c, unsigned wbase, int cc, unsigned (&voff)[(5 * KSS + 7) / 8]) {
#pragma unroll
    for (int i = 0; i < PJ<NKS, KSS>::NWI; ++i) { const int f = imin(c.wid + 8 * i, 5 * KSS - 1); voff[i] = wbase + (unsigned)((5 * cc + f / KSS) * NKS + f % KSS) * 1024 + c.lane * 16; }
}
DI int pj_pp(int b) { return 8 * ((b & 7) >> 1) + ((b >> 3) & 7); }
DI int pj_cc(int b) { return 4 * (b & 1) + (b >> 6); }
template <int NKS, int KSS> DI void pj_prefetch(const Ctx& c, unsigned wbase) {
    unsigned voff[PJ<NKS, KSS>::NWI]; pj_voff<NKS, KSS>(c, wbase, pj_cc(c.b), voff);
#pragma unroll
    for (int s = 0; s < PJ<NKS, KSS>::PRE; ++s) pj_issue_w<NKS, KSS>(c, voff, s);
}

template <int NA, int WIN, int HK, int NS, int NWI, int NRES> constexpr int pj_younger(int j) {
    int n = imin(NA - 1, j + WIN) - j;
    if (j < WIN) n += NRES;
    for (int s = 1; s + PJ_R - 1 < NS; ++s)
        if (HK * s <= j && (j < WIN || HK * s > j - WIN)) n += NWI;
    return n;
}
template <int NA, int WIN, int HK, int NS, int NWI> constexpr int pj_bar_younger(int s) {
    const int s0 = s + 2 - PJ_R;
    int n = 0;
    for (int t = s0 + 1; t < s; ++t) if (t + PJ_R - 1 < NS) n += NWI;
    for (int j = HK * s0; j < HK * s; ++j) if (j + WIN < NA) ++n;
    return n;
}
template <int NKS, int KSS, bool FIRST, int KH>
DI void pj_body(const Ctx& c, unsigned abase, unsigned wbase, int fc, u32x2 (&res)[3]) {
    using P = PJ<NKS, KSS>;
    constexpr int NS = P::NS, NG = NKS * 4, NA = P::NA, HK = KSS / 2, NCT = KH ? 2 : 3, CT0 = KH ? 3 : 0;
    const int lane = c.lane, wid = c.wid, t16l = wid & 3, r = lane & 15, gq = lane >> 4, pp = pj_pp(c.b), cc = pj_cc(c.b);
    lptr lds = c.lds;
    const unsigned t16 = 4 * pp + t16l;
    const int tok = 16 * t16 + r;
    constexpr int WIN = imin(NA, FIRST ? 4 : 9);
    s16x8 a[NA];
    const unsigned aoff = abase + (t16 * NG + 4 * KH) * 256 + lane * 16;
    const int rho = pj_rho<NKS, KSS>(c.b);
#define PJ_ABASE(j) (c.ws + rot((j) / HK, rho, NS) * (KSS * 1024) + ((j) % HK) * 2048)
#pragma unroll
    for (int j = 0; j < WIN; ++j) ldg_async(a[j], PJ_ABASE(j), aoff);
    if (FIRST) {
#pragma unroll
        for (int i = 0; i < NCT; ++i) res[i] = *(const GAS u32x2*)(c.x + (size_t)tok * 1280 + cc * 160 + (CT0 + i) * 32 + gq * 8);
    }
    constexpr int NCS = FIRST && KH ? 20 : 0;
    unsigned csv[NCS ? NCS : 1];
    const int cst = wid * 64 + lane - 256;
    if (NCS) { const int mat = cst >= 112, col = imin(fc * 112 + imin(cst, 223) - 112 * mat, F - 1);
#pragma unroll
        for (int i = 0; i < NCS; ++i) csv[i] = lg32(c.rs, WS_CSP + (unsigned)((mat * 20 + i) * F + col) * 4); }
    unsigned voff[P::NWI]; pj_voff<NKS, KSS>(c, wbase, cc, voff);
    constexpr int NRES = (FIRST ? NCT : 0) + NCS;
    WAIT_BARV(WIN + NRES);
    const unsigned wa = KH * 1024 + lane * 16;
#define PJ_READ(W, st, i) do { lcptr b_ = (lcptr)lds + ((st) % PJ_R) * P::SLOT + wa + (i) * 2048; \
        _Pragma("unroll") for (int ct = 0; ct < 5; ++ct) W[ct] = ld128(b_ + ct * KSS * 1024); } while (0)
    f32x4 acc[5];
#pragma unroll
    for (int ct = 0; ct < 5; ++ct) acc[ct] = (f32x4){};
    STAMP(c.ws, 3, FIRST ? 0 : 4);
    s16x8 w[2][5];
    PJ_READ(w[0], 0, 0);
#pragma unroll
    for (int s = 0; s < NS; ++s) {
        if (NS > PJ_R && s >= 1) {
            if (s + 1 >= PJ_R && s + 1 < NS) WAIT_BAR((pj_bar_younger<NA, WIN, HK, NS, P::NWI>(s))); else LDS_BAR();
            SBAR();
            if (s + PJ_R - 1 < NS) pj_issue_w<NKS, KSS>(c, voff, s + PJ_R - 1);
        }
#pragma unroll
        for (int i = 0; i < HK; ++i) {
            const int j = s * HK + i;
            if (i + 1 < HK) PJ_READ(w[(j + 1) & 1], s, i + 1); else if (s + 1 < NS) PJ_READ(w[(j + 1) & 1], s + 1, 0);
            if (j + WIN < NA) ldg_async(a[j + WIN], PJ_ABASE(j + WIN), aoff);
            WAIT_USE((pj_younger<NA, WIN, HK, NS, P::NWI, NRES>(j)), a[j]);
            SBAR();
#pragma unroll
            for (int ct = 0; ct < 5; ++ct) acc[ct] = MFMA16(w[j & 1][ct], a[j], acc[ct]);
            SBAR();
        }
    }
#undef PJ_READ
#undef PJ_ABASE
    if (NCS) { float s = 0.f;
#pragma unroll
        for (int i = 0; i < NCS; ++i) s += __uint_as_float(csv[i]);
        if (cst < 224) *(LAS float*)(lds + P4_CS + ((cst >= 112) * 128 + cst - 112 * (cst >= 112)) * 4) = s; }
    STAMP(c.ws, 3, FIRST ? 1 : 5);
    LDS_BAR();
    LAS f32x4* red = (LAS f32x4*)(lds + XCH);
#pragma unroll
    for (int i = 0; i < 5 - NCT; ++i) red[(wid * 3 + i) * 64 + lane] = acc[(KH ? 0 : 3) + i];
    LDS_BAR();
    STAMP(c.ws, 3, FIRST ? 2 : 6);
    float s1 = 0.f, s2 = 0.f;
#pragma unroll
    for (int i = 0; i < NCT; ++i) {
        const f32x4 o = red[((wid ^ 4) * 3 + i) * 64 + lane], m = acc[CT0 + i];
        const unsigned w0 = cvtpk(bflo(res[i].x) + m[0] + o[0], bfhi(res[i].x) + m[1] + o[1]), w1 = cvtpk(bflo(res[i].y) + m[2] + o[2], bfhi(res[i].y) + m[3] + o[3]);
        if (FIRST) {
            res[i] = (u32x2){w0, w1};
            st64(c.rs, WS_H + (t16 * 80 + 10 * cc + 2 * (CT0 + i) + (gq >> 1)) * 256 + r * 16 + (gq & 1) * 8, res[i]);
            const float a_ = bflo(w0), b_ = bfhi(w0), c_ = bflo(w1), d = bfhi(w1); s1 += (a_ + b_) + (c_ + d); s2 += (a_ * a_ + b_ * b_) + (c_ * c_ + d * d);
        } else {
            *(GAS u32x2*)(c.out + (size_t)tok * 1280 + cc * 160 + (CT0 + i) * 32 + gq * 8) = (u32x2){w0, w1};
        }
    }
    if (FIRST) {
        s1 += __shfl_xor(s1, 16); s2 += __shfl_xor(s2, 16); s1 = swap32_add(s1); s2 = swap32_add(s2);
        LAS f32x2* ex = (LAS f32x2*)(lds + XCH + 2048);
        if (KH == 1 && gq == 0) ex[t16l * 16 + r] = (f32x2){s1, s2};
        LDS_BAR();
        if (KH == 0 && gq == 0) { const f32x2 e = ex[t16l * 16 + r];
            st64(c.rs, WS_HST + ((t16 * 8 + cc) * 16 + r) * 8, (u32x2){__float_as_uint(s1 + e.x), __float_as_uint(s2 + e.y)}); }
    }
}
template <int NKS, int KSS, bool FIRST>
DI void phase_proj(const Ctx& c, unsigned abase, unsigned wbase, int fc, u32x2 (&res)[3]) {
    if (c.wid < 4) pj_body<NKS, KSS, FIRST, 0>(c, abase, wbase, fc, res);
    else pj_body<NKS, KSS, FIRST, 1>(c, abase, wbase, fc, res);
}

constexpr int P4_R = 7, P4_SLOT = 22528, P4_NS = 20;

DI void p4_wvoff(const Ctx& c, int fc, unsigned& woff0, unsigned& woff1) {
    const unsigned o0 = c.wid * 1792 + c.lane * 16, o1 = c.wid * 1792 + 1024 + (c.lane < 48 ? c.lane : 47) * 16;
    woff0 = WS_W13P + (unsigned)imin(7 * fc + (o0 >> 11), 107) * 40960 + (o0 & 2047); woff1 = WS_W13P + (unsigned)imin(7 * fc + (o1 >> 11), 107) * 40960 + (o1 & 2047);
}
DI int p4_rho(int b) { return ROT4 == 1 ? (((b >> 3) & 7) + 5 * (b >> 6)) % P4_NS : ROT4 == 2 ? 5 * (b >> 6) : ROT4 == 3 ? ((b >> 3) & 7) : 0; }
DI void p4_issue_w(const Ctx& c, unsigned woff0, unsigned woff1, int st) {
    const char* w_ = c.ws + rot(st, p4_rho(c.b), P4_NS) * 2048; const unsigned wdst = c.lds0 + 8192 + c.wid * 1792 + (st % P4_R) * P4_SLOT;
    dma16<true>(w_, woff0, wdst); dma16m<true>(w_, woff1, wdst + 1024, 0xffffffffffffull);
}
DI void p4_prefetch(const Ctx& c, int fc) {
    unsigned woff0, woff1; p4_wvoff(c, fc, woff0, woff1);
#pragma unroll
    for (int s = 0; s < P4_R - 1; ++s) p4_issue_w(c, woff0, woff1, s);
}
constexpr int p4_extra(int s) { int n = 0; for (int t = s - 5 < 0 ? 0 : s - 5; t < s; ++t) if (WARM4 && t <= 14 && t % 3 == 2) ++n; return n; }
template <int UG>
DI void p4_body(const Ctx& c, int qb, int fc) {
    constexpr int NU = UG ? 3 : 4, U0 = UG ? 4 : 0;
    const int lane = c.lane, wid = c.wid, tt = wid & 3, r = lane & 31, h = lane >> 5;
    lptr lds = c.lds;
    const unsigned adst = c.lds0 + wid * 1024;
    const unsigned aoff = WS_H + (unsigned)(8 * qb + wid) * 80 * 256 + lane * 16;
    unsigned woff0, woff1; p4_wvoff(c, fc, woff0, woff1);
    const int rho = p4_rho(c.b);
#pragma unroll
    for (int s = 0; s < P4_R - 1; ++s) dma16<true>(c.ws + rot(s, rho, P4_NS) * 1024, aoff, adst + s * P4_SLOT);
    const unsigned xa = ((2 * tt + (r >> 4)) * 4 + h) * 256 + (r & 15) * 16, wa = 8192 + U0 * 2048 + lane * 16;
    const unsigned t16 = 8 * qb + 2 * tt + (r >> 4);
    u32x2 hs[4];
#pragma unroll
    for (int i = 0; i < 4; ++i) hs[i] = lg64(c.rs, WS_HST + ((t16 * 8 + 4 * h + i) * 16 + (r & 15)) * 8);
#define P4_PIECE(st, k) do { const int p_ = rot(st, rho, P4_NS); const unsigned sl_ = ((st) % P4_R) * P4_SLOT; \
        if ((k) == 0) dma16<true>(c.ws + p_ * 1024, aoff, adst + sl_); \
        if ((k) == 1) dma16<true>(c.ws + p_ * 2048, woff0, c.lds0 + 8192 + wid * 1792 + sl_); \
        if ((k) == 2) dma16m<true>(c.ws + p_ * 2048, woff1, c.lds0 + 8192 + wid * 1792 + sl_ + 1024, 0xffffffffffffull); } while (0)
#define P4_READ(n) do { if ((ABL & 32) && (n) > 2) break; lcptr b_ = (lcptr)lds + (((n) >> 1) % P4_R) * P4_SLOT; X[(n) % 3] = ld128(b_ + xa + ((n) & 1) * 512); \
        _Pragma("unroll") for (int u = 0; u < NU; ++u) W[(n) % 3][u] = ld128(b_ + wa + u * 2048 + ((n) & 1) * 1024); } while (0)
    f32x16 acc[NU];
#pragma unroll
    for (int u = 0; u < NU; ++u) acc[u] = (f32x16){};
    WAIT_BAR((P4_R - 2) + 4);
    s16x8 X[3], W[3][NU];
    STG_DECL(); STG(0);
    P4_READ(0); P4_READ(1);
#pragma unroll
    for (int s = 0; s < P4_NS; ++s) {
        if (!(ABL & 16)) WAIT_BARV(fly2(s, P4_R, P4_NS, 1, 2) + (s + 1 < P4_R - 1 ? 4 : 0) + p4_extra(s));
        STG(1 + s);
        SBAR();
#define P4_NEXT(k) do { if (s + P4_R - 1 < P4_NS && !(ABL & 8)) P4_PIECE(s + P4_R - 1, k); SBAR(); } while (0)
#pragma unroll
        for (int ks = 0; ks < 2; ++ks) {
            const int n = 2 * s + ks;
            if (n + 2 < 2 * P4_NS) P4_READ(n + 2);
            SBAR();
            if (ABL & 64) { PIN(X[n % 3]); _Pragma("unroll") for (int u = 0; u < NU; ++u) PIN(W[n % 3][u]); P4_NEXT(ks); if (ks) P4_NEXT(2); }
            else {
                acc[0] = MFMA32(W[n % 3][0], X[n % 3], acc[0]); SBAR(); P4_NEXT(ks);
                acc[1] = MFMA32(W[n % 3][1], X[n % 3], acc[1]); acc[2] = MFMA32(W[n % 3][2], X[n % 3], acc[2]); SBAR();
                if (ks) P4_NEXT(2);
                if (NU > 3) acc[NU - 1] = MFMA32(W[n % 3][NU - 1], X[n % 3], acc[NU - 1]);
            }
            SBAR();
        }
#undef P4_NEXT
        if (WARM4 && s <= 14 && s % 3 == 2) {
            const int L = imin(((c.b >> 3) * 8 + wid) * 5 + s / 3, 1079);
            dma16<true>(c.ws, WS_W2P + (c.b & 1) * 1105920 + L * 1024 + lane * 16, c.lds0 + P4_R * P4_SLOT + 1024); SBAR(); }
    }
    STG(21); STG_OUT(c.ws, 1, 22);
#undef P4_READ
    LDS_BAR();
    STAMP(c.ws, 1, 2);
    float s1 = (__uint_as_float(hs[0].x) + __uint_as_float(hs[1].x)) + (__uint_as_float(hs[2].x) + __uint_as_float(hs[3].x));
    float s2 = (__uint_as_float(hs[0].y) + __uint_as_float(hs[1].y)) + (__uint_as_float(hs[2].y) + __uint_as_float(hs[3].y));
    s1 = swap32_add(s1); s2 = swap32_add(s2);
    const float mean = s1 * (1.0f / D), var = fmaxf(s2 * (1.0f / D) - mean * mean, 0.f), rstd = __builtin_amdgcn_rsqf(var + LN_EPS);
    const f32x2 nm = {-mean, -mean}, k1 = {-1.4426950408889634f * rstd, -1.4426950408889634f * rstd}, r2 = {rstd * rstd, rstd * rstd}, one = {1.0f, 1.0f};
    const LAS float* csl = (const LAS float*)(lds + P4_CS);
#pragma unroll
    for (int u = 0; u < NU; ++u) {
        if (7 * fc + U0 + u < 108) {
            u32x2 w[2];
#pragma unroll
            for (int gq = 0; gq < 2; ++gq) {
                const f32x4 cg_ = *(const LAS f32x4*)(csl + (U0 + u) * 16 + 8 * gq + 4 * h), cu = *(const LAS f32x4*)(csl + 128 + (U0 + u) * 16 + 8 * gq + 4 * h);
                f32x2 o[2];
#pragma unroll
                for (int e = 0; e < 2; ++e) {
                    const f32x2 a = __builtin_elementwise_fma(nm, (f32x2){cg_[2 * e], cg_[2 * e + 1]}, (f32x2){acc[u][4 * gq + 2 * e], acc[u][4 * gq + 2 * e + 1]});
                    const f32x2 b2 = __builtin_elementwise_fma(nm, (f32x2){cu[2 * e], cu[2 * e + 1]}, (f32x2){acc[u][8 + 4 * gq + 2 * e], acc[u][8 + 4 * gq + 2 * e + 1]});
                    const f32x2 t = a * k1, d = (f32x2){__builtin_amdgcn_exp2f(t.x), __builtin_amdgcn_exp2f(t.y)} + one;
                    const f32x2 rc = {__builtin_amdgcn_rcpf(d.x), __builtin_amdgcn_rcpf(d.y)};
                    o[e] = (a * b2) * (rc * r2);
                }
                w[gq] = (u32x2){cvtpk(o[0].x, o[0].y), cvtpk(o[1].x, o[1].y)};
            }
            auto x0 = __builtin_amdgcn_permlane32_swap(w[0].x, w[1].x, false, false), x1 = __builtin_amdgcn_permlane32_swap(w[0].y, w[1].y, false, false);
            st128(c.rs, WS_HM + (t16 * 216 + 14 * fc + 2 * (U0 + u) + h) * 256 + (r & 15) * 16, (u32x4){x0[0], x1[0], x0[1], x1[1]});
        }
    }
}
DI void phase4(const Ctx& c, int qb, int fc) {
    if (c.wid < 4) p4_body<0>(c, qb, fc); else p4_body<1>(c, qb, fc);
}
DI void p4_colsums(const Ctx& c, int fc) {
    const int tid = (int)threadIdx.x - 256;
    if (tid >= 0 && tid < 224) { const int mat = tid >= 112, cl = tid - 112 * mat, col = imin(fc * 112 + cl, F - 1);
        float v[20];
#pragma unroll
        for (int i = 0; i < 20; ++i) v[i] = __uint_as_float(lg32(c.rs, WS_CSP + (unsigned)((mat * 20 + i) * F + col) * 4));
        float s = 0.f;
#pragma unroll
        for (int i = 0; i < 20; ++i) s += v[i];
        *(LAS float*)(c.lds + P4_CS + (mat * 128 + cl) * 4) = s; }
}

template <int PH0, int PH1>
__global__ __launch_bounds__(512, 2) void block_fwd(const char* __restrict__ x, const char* __restrict__ wqkv, const char* __restrict__ wo, const char* __restrict__ w1,
                                                    const char* __restrict__ w3, const char* __restrict__ w2, char* __restrict__ out, char* __restrict__ ws) {
    extern __shared__ __attribute__((aligned(16))) char smem[];
    Ctx c; c.x = x; c.wqkv = wqkv; c.wo = wo; c.w1 = w1; c.w3 = w3; c.w2 = w2; c.out = out; c.ws = ws;
    c.rs = __builtin_amdgcn_make_buffer_rsrc(ws, 0, WS_END, 0x00020000);
    c.lds = (lptr)smem; c.lds0 = (unsigned)(uintptr_t)smem; c.b = blockIdx.x; c.lane = threadIdx.x & 63; c.wid = __builtin_amdgcn_readfirstlane(threadIdx.x >> 6);
    const int lane = c.lane, g = c.b & 7, idx = c.b >> 3;
    STAMP(ws, 0, 0);
    c.tag = *(const __attribute__((address_space(4))) unsigned*)(ws + WS_E) + 1u;
    if (PH0 > 0) asm volatile("" : "+s"(c.tag));
    u32x2 res[3] = {(u32x2){0u, 0u}, (u32x2){0u, 0u}, (u32x2){0u, 0u}};
    if (PH0 <= 0) {
        phase1(c);
        STAMP(c.ws, 2, 5);
        STAMP(ws, 0, 1);
        WAIT_VM(0);
        publish(c, FL_KV);
        repack_store(c);
        if (PH1 == 0) { WAIT_VM(0); publish(c, FL_PK); }
        STAMP(ws, 0, 2);
    }
    const int qb4 = 8 * (g & 1) + (idx & 7), fc = 4 * (g >> 1) + (idx >> 3), pp = pj_pp(c.b);
    if (PH0 <= 1 && PH1 >= 1) {
        { const int hh = 4 * g + (idx & 3), qb = idx >> 2;
          const int wg = lane < 16 ? wg_p1(lane, (32 + hh) / 6) : lane < 32 ? wg_p1(lane - 16, (64 + hh) / 6) : wg_p1(2 * qb + (lane & 1), hh / 6);
          const unsigned off[1] = {flag_off(FL_KV, wg)}; wait_flags(c, off); }
        phase2(c, PH0 == 0);
        STAMP(ws, 2, 6);
        WAIT_VM(0);
        STAMP(ws, 3, 7);
        publish(c, FL_O);
        if (PH1 >= 2) pj_prefetch<20, 4>(c, WS_WOP);
        STAMP(ws, 0, 5);
    }
    if (PH0 <= 2 && PH1 >= 2) {
        if (PH0 == 2) pj_prefetch<20, 4>(c, WS_WOP);
        { const unsigned off[1] = {flag_off(FL_O, wg_p2(lane & 31, pp >> 2))}; wait_flags(c, off); }
        STAMP(ws, 0, 6);
        phase_proj<20, 4, true>(c, WS_O, WS_WOP, fc, res);
        WAIT_VM(0);
        publish(c, FL_H);
        if (PH1 >= 3) p4_prefetch(c, fc);
        STAMP(ws, 0, 7);
    }
    if (PH0 <= 3 && PH1 >= 3) {
        if (PH0 == 3) { p4_colsums(c, fc); p4_prefetch(c, fc); }
        { const unsigned off[1] = {flag_off(FL_H, wg_p3(2 * qb4 + (lane & 1), (lane >> 1) & 7))}; wait_flags(c, off); }
        STAMP(ws, 1, 1);
        phase4(c, qb4, fc);
        STAMP(ws, 2, 7);
        WAIT_VM(0);
        STAMP(ws, 1, 6);
        publish(c, FL_M);
        if (PH1 >= 4) pj_prefetch<54, 6>(c, WS_W2P);
        STAMP(ws, 1, 3);
    }
    if (PH0 <= 4 && PH1 >= 4) {
        if (PH0 == 4) pj_prefetch<54, 6>(c, WS_W2P);
        const unsigned fm = flag_off(FL_M, wg_p4(pp >> 1, lane & 15));
        if (c.b == 0) {
            const unsigned off[5] = {fm, flag_off(FL_O, lane), flag_off(FL_O, 64 + lane), flag_off(FL_O, 128 + lane), flag_off(FL_O, 192 + lane)}; wait_flags(c, off);
        } else { const unsigned off[1] = {fm}; wait_flags(c, off); }
        STAMP(ws, 1, 4);
        if (PH0 > 2) {
            const int cc = pj_cc(c.b), ct0 = c.wid >> 2 ? 3 : 0, gq = lane >> 4;
#pragma unroll
            for (int i = 0; i < 3; ++i) res[i] = lg64(c.rs, WS_H + ((4 * pp + (c.wid & 3)) * 80 + 10 * cc + 2 * imin(ct0 + i, 4) + (gq >> 1)) * 256 + (lane & 15) * 16 + (gq & 1) * 8);
            WAIT_VM(0); PIN(res[0]); PIN(res[1]); PIN(res[2]);
        }
        phase_proj<54, 6, false>(c, WS_HM, WS_W2P, fc, res);
        STAMP(ws, 1, 5);
        if (c.b == 0 && threadIdx.x == 0) st32(c.rs, WS_E, c.tag);
    }
}

}

extern "C" void kernel_launch(void* const* d_in, const int* in_sizes, int n_in, void* d_out, int out_size, void* d_ws, size_t ws_size, hipStream_t stream) {
    using namespace blk;
    const char* x = (const char*)d_in[0]; const char* wqkv = (const char*)d_in[1]; const char* wo = (const char*)d_in[2];
    const char* w1 = (const char*)d_in[4]; const char* w3 = (const char*)d_in[5]; const char* w2 = (const char*)d_in[6];
    char* ws = (char*)d_ws; char* out = (char*)d_out;
#define LAUNCH(A, B) do { hipFuncSetAttribute((const void*)block_fwd<A, B>, hipFuncAttributeMaxDynamicSharedMemorySize, LDS_BYTES); \
        block_fwd<A, B><<<256, 512, LDS_BYTES, stream>>>(x, wqkv, wo, w1, w3, w2, out, ws); } while (0)
    LAUNCH(0, 4);
#undef LAUNCH
}
```

```cpp
#include <hip/hip_runtime.h>
#include <hip/hip_bf16.h>
#include <stdint.h>

namespace blk {

constexpr int S = 2048, D = 640, H = 32, F = 1728;
constexpr float LN_EPS = 1e-5f;
constexpr float QSCALE = 0.22360679774997896f * 1.4426950408889634f;

#define LAS __attribute__((address_space(3)))
#define GAS __attribute__((address_space(1)))
#define DI __device__ __forceinline__
typedef short s16x8 __attribute__((ext_vector_type(8)));
typedef short s16x4 __attribute__((ext_vector_type(4)));
typedef float f32x16 __attribute__((ext_vector_type(16)));
typedef float f32x4 __attribute__((ext_vector_type(4)));
typedef float f32x2 __attribute__((ext_vector_type(2)));
typedef unsigned u32x4 __attribute__((ext_vector_type(4)));
typedef unsigned u32x2 __attribute__((ext_vector_type(2)));
typedef __bf16 bf16x2 __attribute__((ext_vector_type(2)));
typedef LAS char* lptr;
typedef LAS const char* lcptr;
typedef __amdgpu_buffer_rsrc_t rsrc_t;

constexpr unsigned WS_E = 0;
constexpr unsigned WS_FLAG = 4096;
constexpr unsigned WS_ROWP = 256u << 10;
constexpr unsigned WS_COLP = 512u << 10;
constexpr unsigned WS_Q = 1u << 20;
constexpr unsigned WS_KV = 4u << 20;
constexpr unsigned WS_O = 12u << 20;
constexpr unsigned WS_H = 16u << 20;
constexpr unsigned WS_HST = 20u << 20;
constexpr unsigned WS_CSP = 21u << 20;
constexpr unsigned WS_HM = 24u << 20;
constexpr unsigned WS_W13P = 32u << 20;
constexpr unsigned WS_W2P = 40u << 20;
constexpr unsigned WS_WOP = 44u << 20;
constexpr unsigned WS_END = 48u << 20;
enum { FL_KV = 0, FL_PK = 1, FL_O = 2, FL_H = 3, FL_M = 4 };
constexpr int LDS_BYTES = 163840;
constexpr unsigned SPIN_CAP = 1u << 16;

#define SBAR() __builtin_amdgcn_sched_barrier(0)
#define PIN(x) asm volatile("" : "+v"(x))
#define MFMA32(a, b, c) __builtin_amdgcn_mfma_f32_32x32x16_bf16(a, b, c, 0, 0, 0)
#define MFMA32K8(a, b, c) __builtin_amdgcn_mfma_f32_32x32x8bf16_1k(a, b, c, 0, 0, 0)
#define MFMA16(a, b, c) __builtin_amdgcn_mfma_f32_16x16x32_bf16(a, b, c, 0, 0, 0)
#define WAIT_BAR(N) asm volatile("s_waitcnt vmcnt(%0) lgkmcnt(0)\n\ts_barrier" :: "i"(N) : "memory")
#define WAIT_BARV(N) asm volatile("s_waitcnt vmcnt(%0)\n\ts_barrier" :: "i"(N) : "memory")
#define WAIT_VM(N) asm volatile("s_waitcnt vmcnt(%0)" :: "i"(N) : "memory")
#define LDS_BAR() asm volatile("s_waitcnt lgkmcnt(0)\n\ts_barrier" ::: "memory")

template <bool SC> DI void dma16(const void* sbase, unsigned voff, unsigned ldsaddr) {
    if (SC) asm volatile("s_mov_b32 m0, %2\n\ts_nop 0\n\tglobal_load_lds_dwordx4 %0, %1 sc1" :: "v"(voff), "s"(sbase), "s"(ldsaddr) : "memory");
    else asm volatile("s_mov_b32 m0, %2\n\ts_nop 0\n\tglobal_load_lds_dwordx4 %0, %1" :: "v"(voff), "s"(sbase), "s"(ldsaddr) : "memory");
}
template <bool SC> DI void dma16m(const void* sbase, unsigned voff, unsigned ldsaddr, unsigned long long mask) {
    if (SC) asm volatile("s_mov_b32 m0, %2\n\ts_mov_b64 exec, %3\n\tglobal_load_lds_dwordx4 %0, %1 sc1\n\ts_mov_b64 exec, -1" :: "v"(voff), "s"(sbase), "s"(ldsaddr), "s"(mask) : "memory");
    else asm volatile("s_mov_b32 m0, %2\n\ts_mov_b64 exec, %3\n\tglobal_load_lds_dwordx4 %0, %1\n\ts_mov_b64 exec, -1" :: "v"(voff), "s"(sbase), "s"(ldsaddr), "s"(mask) : "memory");
}
constexpr int imin(int a, int b) { return a < b ? a : b; }
constexpr int imax(int a, int b) { return a > b ? a : b; }
constexpr int fly(int s, int R, int NS) { return imax(0, imin(R - 3, NS - 2 - s)); }
constexpr int fly2(int s, int R, int NS, int na, int nw) {
    const int need = imin(s + 1, NS - 1);
    const int pro = imin(R - 1, NS);
    const int issued = pro * na + imax(0, imin(s, NS - R + 1)) * (na + nw);
    const int endpos = need < pro ? (need + 1) * na : pro * na + (need - pro + 1) * (na + nw);
    return imax(0, issued - endpos);
}
DI int rot(int i, int rho, int ns) { const int t = i + rho; return t >= ns ? t - ns : t; }
DI unsigned cvtpk(float lo, float hi) { f32x2 v = {lo, hi}; return __builtin_bit_cast(unsigned, __builtin_convertvector(v, bf16x2)); }
DI float bflo(unsigned w) { return __uint_as_float(w << 16); }
DI float bfhi(unsigned w) { return __uint_as_float(w & 0xffff0000u); }
DI s16x8 ld128(lcptr p) { return *(const LAS s16x8*)p; }
DI s16x4 ld64(lcptr p) { return *(const LAS s16x4*)p; }
DI s16x4 ldtr(lcptr p) { return __builtin_bit_cast(s16x4, __builtin_amdgcn_ds_read_tr16_b64_v4i16((LAS s16x4*)p)); }
DI s16x8 cat(s16x4 a, s16x4 b) { return __builtin_shufflevector(a, b, 0, 1, 2, 3, 4, 5, 6, 7); }
DI f32x4 sel4(unsigned m, f32x4 a, f32x4 b) { const u32x4 x = __builtin_bit_cast(u32x4, a), y = __builtin_bit_cast(u32x4, b); return __builtin_bit_cast(f32x4, (x & m) | (y & ~m)); }
DI float swap32_add(float v) { auto r = __builtin_amdgcn_permlane32_swap(__float_as_uint(v), __float_as_uint(v), false, false); return __uint_as_float(r[0]) + __uint_as_float(r[1]); }
DI float swap32_max(float v) { auto r = __builtin_amdgcn_permlane32_swap(__float_as_uint(v), __float_as_uint(v), false, false); return fmaxf(__uint_as_float(r[0]), __uint_as_float(r[1])); }
DI float upper_half(float v) { auto r = __builtin_amdgcn_permlane32_swap(__float_as_uint(v), __float_as_uint(v), false, false); return __uint_as_float(r[1]); }
DI void st128(rsrc_t rs, unsigned off, u32x4 v) { __builtin_amdgcn_raw_buffer_store_b128(v, rs, off, 0, 16); }
DI void st64(rsrc_t rs, unsigned off, u32x2 v) { __builtin_amdgcn_raw_buffer_store_b64(v, rs, off, 0, 16); }
DI void st32(rsrc_t rs, unsigned off, unsigned v) { __builtin_amdgcn_raw_buffer_store_b32(v, rs, off, 0, 16); }
DI u32x4 lg128(rsrc_t rs, unsigned off) { return __builtin_amdgcn_raw_buffer_load_b128(rs, off, 0, 16); }
DI u32x2 lg64(rsrc_t rs, unsigned off) { return __builtin_amdgcn_raw_buffer_load_b64(rs, off, 0, 16); }
DI unsigned lg32(rsrc_t rs, unsigned off) { return __builtin_amdgcn_raw_buffer_load_b32(rs, off, 0, 16); }

DI void ldg_async(s16x8& dst, const void* sbase, unsigned voff) { asm volatile("global_load_dwordx4 %0, %1, %2 sc1" : "=v"(dst) : "v"(voff), "s"(sbase) : "memory"); }
#define WAIT_USE(N, x) asm volatile("s_waitcnt vmcnt(%1)" : "+v"(x) : "i"(N) : "memory")
struct Ctx { const char *x, *wqkv, *wo, *w1, *w3, *w2; char *out, *ws; rsrc_t rs; lptr lds; unsigned lds0, tag; int b, lane, wid; };

DI unsigned flag_off(int kind, int wg) { return WS_FLAG + (unsigned)(kind * 256 + wg) * 128u; }
DI void publish(const Ctx& c, int kind) {
    asm volatile("s_barrier" ::: "memory");
    if (threadIdx.x == 0) st32(c.rs, flag_off(kind, c.b), c.tag);
}
template <int N> DI void wait_flags(const Ctx& c, const unsigned (&off)[N]) {
    if (c.wid == 0) {
        for (unsigned spins = 0; spins < SPIN_CAP; ++spins) {
            unsigned v[N];
#pragma unroll
            for (int i = 0; i < N; ++i) v[i] = lg32(c.rs, off[i]);
            bool ok = true;
#pragma unroll
            for (int i = 0; i < N; ++i) ok &= v[i] == c.tag;
            if (__all(ok)) break;
            __builtin_amdgcn_s_sleep(1);
        }
    }
    asm volatile("s_waitcnt lgkmcnt(0)\n\ts_barrier" ::: "memory");
}
DI int wg_p1(int pb, int nb) { return ((pb >> 3) | ((nb >> 2) << 1)) + 8 * ((pb & 7) + 8 * (nb & 3)); }
DI int wg_p2(int hh, int qb) { return (hh >> 2) + 8 * ((hh & 3) + 4 * qb); }
DI int wg_p3(int pp, int cc) { return (((pp >> 3) << 1) | (cc >> 2)) + 8 * ((pp & 7) + 8 * (cc & 3)); }
DI int wg_p4(int qb, int fc) { return ((qb >> 3) | ((fc >> 2) << 1)) + 8 * ((qb & 7) + 8 * (fc & 3)); }

constexpr int P1_R = 5, P1_SLOT = 32768, P1_NS = 10;
constexpr int P1_STG = 264;
constexpr int P1_OUT = 65536;
constexpr int P1_ST = P1_OUT + 40960;
constexpr int P1_CS = P1_ST + 1024;

DI void repack_issue_one(const Ctx& c, int i);
template <int UP>
DI void p1_loop(Ctx& c, unsigned xoff, unsigned woff, int tt, int rho, unsigned put, unsigned get, f32x16& acc0, f32x16& acc1, u32x2 (&part)[10]) {
    f32x16 accx = {};
    const int lane = c.lane, wid = c.wid, r = lane & 31, h = lane >> 5, q = (lane >> 2) & 3, p = lane & 3, cg = (lane >> 4) & 1;
    constexpr unsigned long long WMASK = ~((1ull << 15) | (1ull << 27) | (1ull << 39) | (1ull << 51));
    const unsigned xdst = c.lds0 + wid * 2048, wdst = c.lds0 + 16384 + wid * 2048;
    const int xrow = 32 * tt + r;
    const unsigned xa = xrow * 128 + ((h ^ ((xrow >> 1) & 7)) * 16);
    const unsigned wk = 16384 + (8 * h + q) * 256 + (p & 1) * 8;
    const unsigned wa0 = wk + (((4 * (2 * UP) + 2 * cg + (p >> 1)) ^ (4 * q)) * 16);
    const unsigned wa1 = wk + (((4 * (2 * UP + 1) + 2 * cg + (p >> 1)) ^ (4 * q)) * 16);
    const unsigned waz = wk + (((4 * tt + 2 * cg + (p >> 1)) ^ (4 * q)) * 16);
    const s16x8 ones = {0x3f80, 0x3f80, 0x3f80, 0x3f80, 0x3f80, 0x3f80, 0x3f80, 0x3f80};
#define P1_PIECE(st, slot, k) do { const int p_ = rot(st, rho, P1_NS); \
        if ((k) == 0) dma16<false>(c.x + p_ * 128, xoff, xdst + (slot)); \
        if ((k) == 1) dma16<false>(c.x + p_ * 128 + 8 * 1280, xoff ^ 64, xdst + (slot) + 1024); \
        if ((k) == 2) dma16m<false>(c.wqkv + (size_t)p_ * (64 * 3840), woff, wdst + (slot), WMASK); \
        if ((k) == 3) dma16m<false>(c.wqkv + (size_t)p_ * (64 * 3840) + 4 * 3840, woff, wdst + (slot) + 1024, WMASK); } while (0)
#define P1_ISSUE(st, slot) do { P1_PIECE(st, slot, 0); P1_PIECE(st, slot, 1); P1_PIECE(st, slot, 2); P1_PIECE(st, slot, 3); } while (0)
#define P1_READ(n) do { lcptr b_ = (lcptr)c.lds + (((n) >> 2) % P1_R) * P1_SLOT; \
        X[(n) % 3] = ld128(b_ + (xa ^ (((n) & 3) * 32))); \
        W0[(n) % 3] = cat(ldtr(b_ + wa0 + ((n) & 3) * 4096), ldtr(b_ + wa0 + ((n) & 3) * 4096 + 1024)); \
        W1[(n) % 3] = cat(ldtr(b_ + wa1 + ((n) & 3) * 4096), ldtr(b_ + wa1 + ((n) & 3) * 4096 + 1024)); \
        if (UP && (n) < 4) WZ[(n) & 3] = cat(ldtr(b_ + waz + ((n) & 3) * 4096), ldtr(b_ + waz + ((n) & 3) * 4096 + 1024)); } while (0)
#define P1_MMA(n) do { acc0 = MFMA32(W0[(n) % 3], X[(n) % 3], acc0); acc1 = MFMA32(W1[(n) % 3], X[(n) % 3], acc1); \
        if ((n) < 4) { if (UP) accx = MFMA32(WZ[(n) & 3], ones, accx); else accx = MFMA32(X[(n) % 3], X[(n) % 3], accx); } } while (0)
    P1_ISSUE(0, 0); P1_ISSUE(1, P1_SLOT); P1_ISSUE(2, 2 * P1_SLOT);
    WAIT_BAR(8);
    asm volatile("" : "+s"(c.tag));
    c.tag += 1u;
    P1_ISSUE(3, 3 * P1_SLOT);
    s16x8 X[3] = {}, W0[3] = {}, W1[3] = {}, WZ[4] = {};
    P1_READ(0); P1_READ(1);
#pragma unroll
    for (int s = 0; s < P1_NS; ++s) {
        constexpr int NX = P1_R - 1, GS = P1_NS - P1_R;
        WAIT_BARV(4 * fly(s, P1_R, P1_NS) + (s >= 1 && s + 1 <= P1_R - 1 ? 1 : 0) + (s > GS && !UP ? 10 : 0) + imax(0, s - GS - 1));
        SBAR();
#pragma unroll
        for (int ks = 0; ks < 4; ++ks) {
            const int n = 4 * s + ks;
            if (n + 2 < 4 * P1_NS) P1_READ(n + 2);
            SBAR(); P1_MMA(n); SBAR();
            if (s + NX < P1_NS) P1_PIECE(s + NX, ((s + NX) % P1_R) * P1_SLOT, ks);
            if (s > GS && ks == 1) repack_issue_one(c, s - GS - 1);
            SBAR();
        }
        if (s == 0) {
            const int tr = (r & 3) + 4 * (r >> 3); float v = 0.f;
#pragma unroll
            for (int i = 0; i < 16; ++i) v = (tr == i) ? accx[i] : v;
            st64(c.rs, put + (h == ((r >> 2) & 1) ? (tt * 32 + r) * 8 : 0x7fffff00u), (u32x2){__float_as_uint(v), c.tag});
            SBAR();
        }
        if (s == GS && !UP) {
#pragma unroll
            for (int i = 0; i < 10; ++i) part[i] = lg64(c.rs, get + i * 1024);
            SBAR();
        }
    }
#undef P1_PIECE
#undef P1_ISSUE
#undef P1_READ
#undef P1_MMA
}

DI void phase1(Ctx& c) {
    const int lane = c.lane, wid = c.wid, tid = threadIdx.x, g = c.b & 7, idx = c.b >> 3;
    const int pb = 8 * (g & 1) + (idx & 7), nb = 4 * (g >> 1) + (idx >> 3);
    const int tt = wid & 3, up = wid >> 2, r = lane & 31, h = lane >> 5;
    lptr lds = c.lds;
    if (tid < P1_R * 64) { const int sl = tid >> 6, k = tid & 63;
        *(LAS u32x4*)(lds + sl * P1_SLOT + 16384 + k * 256 + ((15 ^ (4 * (k & 3))) * 16)) = (u32x4){0x3f803f80u, 0x3f803f80u, 0x3f803f80u, 0x3f803f80u}; }
    const int xr = 16 * wid + (lane >> 3), xc = (lane & 7) ^ ((xr >> 1) & 7);
    const unsigned xoff = (pb * 128 + xr) * 1280 + xc * 16;
    const int wkk = 8 * wid + (lane >> 4), wp = (lane & 15) ^ (4 * (wkk & 3)), wpc = wp < 15 ? wp : 14;
    const unsigned woff = wkk * 3840 + nb * 240 + wpc * 16;
    f32x16 acc0 = {}, acc1 = {};
    const int rho = (pb + 3 * nb) % P1_NS;
    const unsigned put = up ? WS_COLP + (unsigned)(nb * 16 + pb) * 1024 : WS_ROWP + (unsigned)(pb * 16 + nb) * 1024;
    const int gi = (wid & 1) * 64 + lane;
    const unsigned get = ((wid & 2) ? WS_COLP + (unsigned)nb * 16384 : WS_ROWP + (unsigned)pb * 16384) + gi * 8;
    u32x2 part[10];
    if (up == 0) p1_loop<0>(c, xoff, woff, tt, rho, put, get, acc0, acc1, part);
    else p1_loop<1>(c, xoff, woff, tt, rho, put, get, acc0, acc1, part);
    float psum = 0.f;
    if (up == 0) {
        for (unsigned spins = 0; spins < SPIN_CAP; ++spins) {
            bool ok = true;
#pragma unroll
            for (int i = 0; i < 10; ++i) ok &= part[i].y == c.tag;
            if (__all(ok)) break;
            __builtin_amdgcn_s_sleep(1);
#pragma unroll
            for (int i = 0; i < 10; ++i) part[i] = lg64(c.rs, get + i * 1024);
        }
#pragma unroll
        for (int i = 0; i < 10; ++i) psum += __uint_as_float(part[i].x);
    }
    LDS_BAR();
    LAS float* st = (LAS float*)(lds + P1_ST); LAS float* cs = (LAS float*)(lds + P1_CS);
    if (wid < 2) st[gi * 2 + 1] = psum; else if (wid < 4) cs[gi] = psum;
    if (up == 1 && h == 0) st[(tt * 32 + r) * 2] = acc1[12];
    LDS_BAR();
    { const f32x2 sv = *(const LAS f32x2*)(st + (tt * 32 + r) * 2);
      const float mean = sv.x * (1.0f / D), var = fmaxf(sv.y * (1.0f / D) - mean * mean, 0.f), rstd = __builtin_amdgcn_rsqf(var + LN_EPS), rm = rstd * mean;
      lptr stg = lds + P1_OUT + (32 * tt + r) * P1_STG;
#pragma unroll
      for (int u = 0; u < 2; ++u)
#pragma unroll
        for (int gq = 0; gq < 4; ++gq) {
            const int col = (2 * up + u) * 32 + 8 * gq + 4 * h;
            const f32x4 c4 = *(const LAS f32x4*)(cs + col);
            const float sc = (nb * 120 + col < D) ? QSCALE : 1.0f, a = rstd * sc, bb = rm * sc;
            float v[4];
#pragma unroll
            for (int e = 0; e < 4; ++e) v[e] = a * (u ? acc1[4 * gq + e] : acc0[4 * gq + e]) - bb * c4[e];
            *(LAS u32x2*)(stg + col * 2) = (u32x2){cvtpk(v[0], v[1]), cvtpk(v[2], v[3])};
        }
    }
    LDS_BAR();
#pragma unroll
    for (int j = 0; j < 3; ++j) {
        const int slot = up + 2 * j, gidx = 6 * nb + slot, type = gidx >> 5, head = gidx & 31, tile = 4 * pb + tt;
        if (type < 2) {
            lcptr row = (lcptr)lds + P1_OUT + (32 * tt + r) * P1_STG + slot * 40;
            const u32x2 a0 = *(const LAS u32x2*)(row + 16 * h), a1 = *(const LAS u32x2*)(row + 16 * h + 8), a2 = *(const LAS u32x2*)(row + 32);
            const unsigned dst = type ? WS_KV + (unsigned)(head * 64 + tile) * 3072 : WS_Q + (unsigned)(head * 64 + tile) * 1536;
            st128(c.rs, dst + lane * 16, (u32x4){a0.x, a0.y, a1.x, a1.y});
            st64(c.rs, dst + 1024 + lane * 8, h ? (u32x2){type ? 0x00003f80u : 0u, 0u} : a2);
        } else {
            const int q = (lane >> 2) & 3, p = lane & 3, cg = (lane >> 4) & 1;
            lcptr vb = (lcptr)lds + P1_OUT + (32 * tt + 4 * h + q) * P1_STG + slot * 40 + (cg * 4 + p < 5 ? (cg * 4 + p) * 8 : 0);
            const unsigned dst = WS_KV + (unsigned)(head * 64 + tile) * 3072 + 1536;
#pragma unroll
            for (int s = 0; s < 2; ++s) {
                const u32x2 a = __builtin_bit_cast(u32x2, ldtr(vb + (16 * s) * P1_STG)), e = __builtin_bit_cast(u32x2, ldtr(vb + (16 * s + 8) * P1_STG));
                u32x4 v = {a.x, a.y, e.x, e.y};
                if (r == 20) v = (u32x4){0x3f803f80u, 0x3f803f80u, 0x3f803f80u, 0x3f803f80u};
                if (r > 20) v = (u32x4){0u, 0u, 0u, 0u};
                if (r < 24) st128(c.rs, dst + s * 768 + h * 384 + r * 16, v);
            }
        }
    }
}

constexpr int PK_OFF = 0;
DI int pk_job(int b, int i) { const int j = b + 256 * i; return j < 910 ? j : j - 256; }
DI void repack_issue_one(const Ctx& c, int i) {
    const int row = 8 * c.wid + (c.lane >> 3), pc = c.lane & 7;
    const int j = pk_job(c.b, i);
    const char* src; unsigned off;
    if (j < 540) { const int jj = j < 270 ? j : j - 270, kb = jj / 27, cb = jj % 27; src = j < 270 ? c.w1 : c.w3; off = (64 * kb + row) * 3456 + cb * 128 + pc * 16; }
    else if (j < 810) { const int jj = j - 540, kb = jj / 10, cb = jj % 10; src = c.w2; off = (64 * kb + row) * 1280 + cb * 128 + pc * 16; }
    else { const int jj = j - 810, kb = jj / 10, cb = jj % 10, kp = 64 * kb + row, G = kp >> 3, e = kp & 7;
        const int k = G < 64 ? 20 * (G >> 1) + 8 * (G & 1) + e : 40 * (G - 64) + 16 + (e < 4 ? e : e + 16);
        src = c.wo; off = k * 1280 + cb * 128 + pc * 16; }
    dma16<false>(src, off, c.lds0 + PK_OFF + i * 8192 + c.wid * 1024);
}
DI void repack_store(const Ctx& c) {
    const int lane = c.lane, w = c.wid, g = lane >> 4, i16 = lane & 15, q = i16 >> 2, p = i16 & 3;
#pragma unroll
    for (int i = 0; i < 4; ++i) {
        const int j = pk_job(c.b, i);
        lcptr blk = (lcptr)c.lds + PK_OFF + i * 8192 + (w >> 1) * 32 + p * 8;
        if (j < 540) {
            const int mat = j >= 270, jj = mat ? j - 270 : j, kb = jj / 27, cb = jj % 27, ks = 2 * (w & 1) + (g >> 1), hh = g & 1;
            lcptr a = blk + (16 * ks + 8 * hh + q) * 128;
            const u32x2 v0 = __builtin_bit_cast(u32x2, ldtr(a)), v1 = __builtin_bit_cast(u32x2, ldtr(a + 512));
            st128(c.rs, WS_W13P + (unsigned)((4 * cb + (w >> 1)) * 40 + 4 * kb + ks) * 1024 + (mat * 16 + i16 + 32 * hh) * 16, (u32x4){v0.x, v0.y, v1.x, v1.y});
            float s = ((bflo(v0.x) + bfhi(v0.x)) + (bflo(v0.y) + bfhi(v0.y))) + ((bflo(v1.x) + bfhi(v1.x)) + (bflo(v1.y) + bfhi(v1.y)));
            s += __shfl_xor(s, 16); s += __shfl_xor(s, 32);
            if (lane < 16) st32(c.rs, WS_CSP + (unsigned)((mat * 20 + 2 * kb + (w & 1)) * F + 64 * cb + 16 * (w >> 1) + i16) * 4, __float_as_uint(s));
        } else {
            const bool isw2 = j < 810; const int jj = isw2 ? j - 540 : j - 810, kb = jj / 10, cb = jj % 10, ks = w & 1;
            lcptr a = blk + (32 * ks + 8 * g + q) * 128;
            const u32x2 v0 = __builtin_bit_cast(u32x2, ldtr(a)), v1 = __builtin_bit_cast(u32x2, ldtr(a + 512));
            const unsigned dst = isw2 ? WS_W2P + (unsigned)((4 * cb + (w >> 1)) * 54 + 2 * kb + ks) * 1024 : WS_WOP + (unsigned)((4 * cb + (w >> 1)) * 20 + 2 * kb + ks) * 1024;
            st128(c.rs, dst + lane * 16, (u32x4){v0.x, v0.y, v1.x, v1.y});
        }
    }
}

constexpr int P2_R = 4, P2_SLOT = 24576, P2_NS = 8;
constexpr int P2_Q = 110592;
constexpr int XCH = 135168;
constexpr int P2_EX = XCH;
constexpr int P4_CS = XCH + 24576;

DI void attn_safe(const Ctx& c, unsigned kv, s16x8 q0, s16x4 q1z, f32x16& o) {
    const int lane = c.lane, r = lane & 31, h = lane >> 5, vr = r < 24 ? r : r - 16;
    float m = -INFINITY; o = (f32x16){};
    for (int t = 0; t < 64; ++t) {
        const unsigned p = kv + t * 3072;
        const u32x4 k0 = lg128(c.rs, p + lane * 16); const u32x2 k1 = lg64(c.rs, p + 1024 + lane * 8);
        const u32x4 v0 = lg128(c.rs, p + 1536 + h * 384 + vr * 16), v1 = lg128(c.rs, p + 1536 + 768 + h * 384 + vr * 16);
        f32x16 s = MFMA32(__builtin_bit_cast(s16x8, k0), q0, (f32x16){}); s = MFMA32K8(__builtin_bit_cast(s16x4, k1), q1z, s);
        float tm = s[0];
#pragma unroll
        for (int i = 1; i < 16; ++i) tm = fmaxf(tm, s[i]);
        tm = swap32_max(tm);
        const float mn = fmaxf(m, tm), al = __builtin_amdgcn_exp2f(m - mn); m = mn;
#pragma unroll
        for (int i = 0; i < 16; ++i) { o[i] *= al; s[i] = __builtin_amdgcn_exp2f(s[i] - mn); }
        const u32x4 p0 = {cvtpk(s[0], s[1]), cvtpk(s[2], s[3]), cvtpk(s[4], s[5]), cvtpk(s[6], s[7])};
        const u32x4 p1 = {cvtpk(s[8], s[9]), cvtpk(s[10], s[11]), cvtpk(s[12], s[13]), cvtpk(s[14], s[15])};
        o = MFMA32(__builtin_bit_cast(s16x8, v0), __builtin_bit_cast(s16x8, p0), o); o = MFMA32(__builtin_bit_cast(s16x8, v1), __builtin_bit_cast(s16x8, p1), o);
    }
}

DI void phase2(const Ctx& c) {
    const int lane = c.lane, wid = c.wid, g = c.b & 7, idx = c.b >> 3, hh = 4 * g + (idx & 3), qb = idx >> 2;
    const int qg = wid & 3, kh = wid >> 2, r = lane & 31, h = lane >> 5;
    lptr lds = c.lds;
    const char* kvsrc = c.ws + WS_KV + (size_t)hh * 64 * 3072 + wid * 3072;
    const unsigned dst0 = c.lds0 + wid * 3072, voff = lane * 16;
#define P2_ISSUE(st, slot) do { const char* s_ = kvsrc + (size_t)(st) * 24576; dma16<true>(s_, voff, dst0 + (slot)); dma16<true>(s_ + 1024, voff, dst0 + (slot) + 1024); dma16<true>(s_ + 2048, voff, dst0 + (slot) + 2048); } while (0)
    { const char* qsrc = c.ws + WS_Q + ((size_t)hh * 64 + 8 * qb + wid) * 1536;
      dma16<true>(qsrc, voff, c.lds0 + P2_Q + wid * 1536); dma16m<true>(qsrc + 1024, voff, c.lds0 + P2_Q + wid * 1536 + 1024, 0xffffffffull); }
    P2_ISSUE(0, 0); P2_ISSUE(1, P2_SLOT);
    const int vr = r < 24 ? r : 21 + (r & 1);
    const unsigned ka = 4 * kh * 3072 + lane * 16, kb = 4 * kh * 3072 + 1024 + lane * 8, va = 4 * kh * 3072 + 1536 + h * 384 + vr * 16;
#define LDK0(slot, t) ld128((lcptr)lds + (slot) + ka + (t) * 3072)
#define LDK1(slot, t) ld64((lcptr)lds + (slot) + kb + (t) * 3072)
#define LDV0(slot, t) ld128((lcptr)lds + (slot) + va + (t) * 3072)
#define LDV1(slot, t) ld128((lcptr)lds + (slot) + va + (t) * 3072 + 768)
    WAIT_BAR(3);
    if (threadIdx.x == 0) st32(c.rs, flag_off(FL_PK, c.b), c.tag);
    P2_ISSUE(2, 2 * P2_SLOT);
    s16x8 q0[2]; s16x4 q1z[2], q1[2];
#pragma unroll
    for (int i = 0; i < 2; ++i) { q0[i] = ld128((lcptr)lds + P2_Q + (2 * qg + i) * 1536 + lane * 16); q1z[i] = ld64((lcptr)lds + P2_Q + (2 * qg + i) * 1536 + 1024 + lane * 8); }
    { const s16x8 k0 = ld128((lcptr)lds + lane * 16); const s16x4 k1 = ld64((lcptr)lds + 1024 + lane * 8);
#pragma unroll
      for (int i = 0; i < 2; ++i) { f32x16 s = MFMA32(k0, q0[i], (f32x16){}); s = MFMA32K8(k1, q1z[i], s);
          float m = s[0];
#pragma unroll
          for (int e = 1; e < 16; ++e) m = fmaxf(m, s[e]);
          m = swap32_max(m);
          q1[i] = q1z[i]; if (h) q1[i][0] = (short)(cvtpk(-m, 0.f) & 0xffffu); } }
    s16x8 kA0 = LDK0(0, 0), kB0, vA0 = LDV0(0, 0), vA1 = LDV1(0, 0), vB0, vB1 = vA1; s16x4 kA1 = LDK1(0, 0), kB1;
    f32x16 sA, sB, o[2] = {(f32x16){}, (f32x16){}};
    sA = MFMA32(kA0, q0[0], (f32x16){}); sA = MFMA32K8(kA1, q1[0], sA);
    u32x4 p0, p1 = {0u, 0u, 0u, 0u};
    int cur = 0, nxt = P2_SLOT, fre = (P2_R - 1) * P2_SLOT;
#define EX(v) __builtin_amdgcn_exp2f(v)
#define P2_STEP(C, N, K0, K1, NQ, OC, V0, OP, VP, LOADS, LAST) do { \
        OP = MFMA32(VP, __builtin_bit_cast(s16x8, p1), OP); SBAR(); \
        LOADS; SBAR(); \
        C[0] = EX(C[0]); C[1] = EX(C[1]); C[2] = EX(C[2]); C[3] = EX(C[3]); p0[0] = cvtpk(C[0], C[1]); p0[1] = cvtpk(C[2], C[3]); PIN(p0); SBAR(); \
        if (!(LAST)) N = MFMA32(K0, q0[NQ], (f32x16){}); SBAR(); \
        C[4] = EX(C[4]); C[5] = EX(C[5]); C[6] = EX(C[6]); C[7] = EX(C[7]); p0[2] = cvtpk(C[4], C[5]); p0[3] = cvtpk(C[6], C[7]); PIN(p0); SBAR(); \
        if (!(LAST)) N = MFMA32K8(K1, q1[NQ], N); SBAR(); \
        C[8] = EX(C[8]); C[9] = EX(C[9]); C[10] = EX(C[10]); C[11] = EX(C[11]); p1[0] = cvtpk(C[8], C[9]); p1[1] = cvtpk(C[10], C[11]); PIN(p1); SBAR(); \
        OC = MFMA32(V0, __builtin_bit_cast(s16x8, p0), OC); SBAR(); \
        C[12] = EX(C[12]); C[13] = EX(C[13]); C[14] = EX(C[14]); C[15] = EX(C[15]); p1[2] = cvtpk(C[12], C[13]); p1[3] = cvtpk(C[14], C[15]); PIN(p1); SBAR(); \
    } while (0)
#define P2_PIECE(st, k) do { if ((st) >= 0) dma16<true>(kvsrc + (size_t)(st) * 24576 + (k) * 1024, voff, dst0 + fre + (k) * 1024); } while (0)
#define P2_PAIR(T, NS_, NT, LAST, ST, KA, KB) do { \
        if (((T) == 0) == (kh != 0)) __builtin_amdgcn_s_setprio(1); else __builtin_amdgcn_s_setprio(0); \
        P2_STEP(sA, sB, kA0, kA1, 1, o[0], vA0, o[1], vB1, { kB0 = LDK0(cur, (T) + 1); kB1 = LDK1(cur, (T) + 1); vB0 = LDV0(cur, (T) + 1); vB1 = LDV1(cur, (T) + 1); }, false); \
        P2_STEP(sB, sA, kB0, kB1, 0, o[1], vA0, o[0], vA1, { P2_PIECE(ST, KA); }, false); \
        P2_STEP(sA, sB, kB0, kB1, 1, o[0], vB0, o[1], vA1, { if (!(LAST)) { kA0 = LDK0(NS_, NT); kA1 = LDK1(NS_, NT); vA0 = LDV0(NS_, NT); vA1 = LDV1(NS_, NT); } }, false); \
        P2_STEP(sB, sA, kA0, kA1, 0, o[1], vB0, o[0], vB1, { if ((KB) >= 0) P2_PIECE(ST, KB); }, LAST); } while (0)
#define P2_STAGE(LAST, ST) do { P2_PAIR(0, cur, 2, false, ST, 0, 1); P2_PAIR(2, nxt, 0, LAST, ST, 2, -1); \
        fre = cur; cur = nxt; nxt = (nxt == (P2_R - 1) * P2_SLOT) ? 0 : nxt + P2_SLOT; } while (0)
#pragma unroll
    for (int s = 0; s < P2_NS - 3; ++s) { WAIT_BAR(3); P2_STAGE(false, s + P2_R - 1); }
    WAIT_BAR(3);
    unsigned pk[4];
    if (wid == 0) {
#pragma unroll
        for (int i = 0; i < 4; ++i) pk[i] = lg32(c.rs, flag_off(FL_PK, 64 * i + lane));
    }
    P2_STAGE(false, -1);
    if (wid == 0) {
        for (unsigned spins = 0; spins < SPIN_CAP; ++spins) {
            if (__all(pk[0] == c.tag && pk[1] == c.tag && pk[2] == c.tag && pk[3] == c.tag)) break;
            __builtin_amdgcn_s_sleep(1);
#pragma unroll
            for (int i = 0; i < 4; ++i) pk[i] = lg32(c.rs, flag_off(FL_PK, 64 * i + lane));
        }
    }
    WAIT_BAR(0);
    const unsigned wsc = c.lds0 + P2_Q + 12288 + wid * 1024;
    unsigned wu[6];
#pragma unroll
    for (int i = 0; i < 6; ++i) { const int L = imin((idx * 8 + wid) * 6 + i, 1519);
        wu[i] = (L < 400 ? WS_WOP + (g & 1) * 409600 + L * 1024 : WS_W13P + imin((g >> 1) * 1146880 + (L - 400) * 1024, 108 * 40960 - 1024)) + lane * 16; }
#define P2_WARM(i) dma16<true>(c.ws, wu[i], wsc)
    P2_PAIR(0, cur, 2, false, -1, 0, 1); P2_WARM(0); P2_WARM(1); P2_PAIR(2, nxt, 0, false, -1, 2, -1); P2_WARM(2);
    fre = cur; cur = nxt; nxt = (nxt == (P2_R - 1) * P2_SLOT) ? 0 : nxt + P2_SLOT;
    P2_WARM(3); P2_PAIR(0, cur, 2, false, -1, 0, 1); P2_WARM(4); P2_WARM(5); P2_PAIR(2, nxt, 0, true, -1, 2, -1);
#undef P2_WARM
    o[1] = MFMA32(vB1, __builtin_bit_cast(s16x8, p1), o[1]);
#undef P2_STEP
#undef P2_PAIR
#undef P2_PIECE
#undef P2_STAGE
#undef P2_ISSUE
    __builtin_amdgcn_s_setprio(0);
    LAS f32x4* ex = (LAS f32x4*)(lds + P2_EX);
    { const unsigned m = kh ? 0xffffffffu : 0u;
#pragma unroll
      for (int i = 0; i < 3; ++i) { const f32x4 a = {o[0][4 * i], o[0][4 * i + 1], o[0][4 * i + 2], o[0][4 * i + 3]}, e = {o[1][4 * i], o[1][4 * i + 1], o[1][4 * i + 2], o[1][4 * i + 3]};
          ex[(wid * 3 + i) * 64 + lane] = sel4(m, a, e); }
      LDS_BAR();
      f32x16 of = {};
#pragma unroll
      for (int i = 0; i < 3; ++i) { const f32x4 a = {o[0][4 * i], o[0][4 * i + 1], o[0][4 * i + 2], o[0][4 * i + 3]}, e = {o[1][4 * i], o[1][4 * i + 1], o[1][4 * i + 2], o[1][4 * i + 3]};
          const f32x4 t = sel4(m, e, a) + ex[((wid ^ 4) * 3 + i) * 64 + lane];
          of[4 * i] = t[0]; of[4 * i + 1] = t[1]; of[4 * i + 2] = t[2]; of[4 * i + 3] = t[3]; }
      float l = upper_half(of[8]);
      bool bad = !(l < 1.2676506e30f && l > 8.6736174e-19f);
#pragma unroll
      for (int i = 0; i < 12; ++i) bad |= !(fabsf(of[i]) < 3.0e38f);
      if (__any(bad)) {
          const s16x8 qq0 = ld128((lcptr)lds + P2_Q + (2 * qg + kh) * 1536 + lane * 16); const s16x4 qq1 = ld64((lcptr)lds + P2_Q + (2 * qg + kh) * 1536 + 1024 + lane * 8);
          attn_safe(c, WS_KV + (unsigned)hh * 64 * 3072, qq0, qq1, of); l = upper_half(of[8]); }
      const float il = __builtin_amdgcn_rcpf(l);
      const unsigned a0 = cvtpk(of[0] * il, of[1] * il), a1 = cvtpk(of[2] * il, of[3] * il), b0 = cvtpk(of[4] * il, of[5] * il), b1 = cvtpk(of[6] * il, of[7] * il);
      auto x0 = __builtin_amdgcn_permlane32_swap(a0, b0, false, false), x1 = __builtin_amdgcn_permlane32_swap(a1, b1, false, false);
      const unsigned t16 = 2 * (8 * qb + 2 * qg + kh) + (r >> 4);
      st128(c.rs, WS_O + (t16 * 80 + 2 * hh + h) * 256 + (r & 15) * 16, (u32x4){x0[0], x1[0], x0[1], x1[1]});
      if (h == 0) st64(c.rs, WS_O + (t16 * 80 + 64 + (hh >> 1)) * 256 + (r & 15) * 16 + (hh & 1) * 8, (u32x2){cvtpk(of[8] * il, of[9] * il), cvtpk(of[10] * il, of[11] * il)});
    }
#undef LDK0
#undef LDK1
#undef LDV0
#undef LDV1
#undef EX
}

constexpr int PJ_R = 5;
template <int NKS, int KSS> struct PJ {
    static constexpr int NS = NKS / KSS, SLOT = 5 * KSS * 1024, NWI = (5 * KSS + 7) / 8, NA = NKS / 2, PRE = imin(PJ_R, NS);
};
template <int NKS, int KSS> DI void pj_issue_w(const Ctx& c, const unsigned (&voff)[(5 * KSS + 7) / 8], int st) {
    using P = PJ<NKS, KSS>;
    const char* base = c.ws + st * (KSS * 1024);
#pragma unroll
    for (int i = 0; i < P::NWI; ++i) dma16<true>(base, voff[i], c.lds0 + (st % PJ_R) * P::SLOT + imin(c.wid + 8 * i, 5 * KSS - 1) * 1024);
}
template <int NKS, int KSS> DI void pj_voff(const Ctx& c, unsigned wbase, int cc, unsigned (&voff)[(5 * KSS + 7) / 8]) {
#pragma unroll
    for (int i = 0; i < PJ<NKS, KSS>::NWI; ++i) { const int f = imin(c.wid + 8 * i, 5 * KSS - 1); voff[i] = wbase + (unsigned)((5 * cc + f / KSS) * NKS + f % KSS) * 1024 + c.lane * 16; }
}
DI int pj_pp(int b) { return 8 * ((b & 7) >> 1) + ((b >> 3) & 7); }
DI int pj_cc(int b) { return 4 * (b & 1) + (b >> 6); }
template <int NKS, int KSS> DI void pj_prefetch(const Ctx& c, unsigned wbase) {
    unsigned voff[PJ<NKS, KSS>::NWI]; pj_voff<NKS, KSS>(c, wbase, pj_cc(c.b), voff);
#pragma unroll
    for (int s = 0; s < PJ<NKS, KSS>::PRE; ++s) pj_issue_w<NKS, KSS>(c, voff, s);
}

template <int NA, int WIN, int HK, int NS, int NWI, int NRES> constexpr int pj_younger(int j) {
    int n = imin(NA - 1, j + WIN) - j;
    if (j < WIN) n += NRES;
    for (int s = 1; s + PJ_R - 1 < NS; ++s)
        if (HK * s <= j && (j < WIN || HK * s > j - WIN)) n += NWI;
    return n;
}
template <int NA, int WIN, int HK, int NS, int NWI> constexpr int pj_bar_younger(int s) {
    const int s0 = s + 2 - PJ_R;
    int n = 0;
    for (int t = s0 + 1; t < s; ++t) if (t + PJ_R - 1 < NS) n += NWI;
    for (int j = HK * s0; j < HK * s; ++j) if (j + WIN < NA) ++n;
    return n;
}
template <int NKS, int KSS, bool FIRST, int KH>
DI void pj_body(const Ctx& c, unsigned abase, unsigned wbase, int fc, u32x2 (&res)[3]) {
    using P = PJ<NKS, KSS>;
    constexpr int NS = P::NS, NG = NKS * 4, NA = P::NA, HK = KSS / 2, NCT = KH ? 2 : 3, CT0 = KH ? 3 : 0;
    const int lane = c.lane, wid = c.wid, t16l = wid & 3, r = lane & 15, gq = lane >> 4, pp = pj_pp(c.b), cc = pj_cc(c.b);
    lptr lds = c.lds;
    const unsigned t16 = 4 * pp + t16l;
    const int tok = 16 * t16 + r;
    constexpr int WIN = imin(NA, FIRST ? 4 : 9);
    s16x8 a[NA];
    const unsigned aoff = abase + (t16 * NG + 4 * KH) * 256 + lane * 16;
#define PJ_ABASE(j) (c.ws + (j) * 2048)
#pragma unroll
    for (int j = 0; j < WIN; ++j) ldg_async(a[j], PJ_ABASE(j), aoff);
    if (FIRST) {
#pragma unroll
        for (int i = 0; i < NCT; ++i) res[i] = *(const GAS u32x2*)(c.x + (size_t)tok * 1280 + cc * 160 + (CT0 + i) * 32 + gq * 8);
    }
    constexpr int NCS = FIRST && KH ? 20 : 0;
    unsigned csv[NCS ? NCS : 1];
    const int cst = wid * 64 + lane - 256;
    if (NCS) { const int mat = cst >= 112, col = imin(fc * 112 + imin(cst, 223) - 112 * mat, F - 1);
#pragma unroll
        for (int i = 0; i < NCS; ++i) csv[i] = lg32(c.rs, WS_CSP + (unsigned)((mat * 20 + i) * F + col) * 4); }
    unsigned voff[P::NWI]; pj_voff<NKS, KSS>(c, wbase, cc, voff);
    constexpr int NRES = (FIRST ? NCT : 0) + NCS;
    WAIT_BARV(WIN + NRES);
    const unsigned wa = KH * 1024 + lane * 16;
#define PJ_READ(W, st, i) do { lcptr b_ = (lcptr)lds + ((st) % PJ_R) * P::SLOT + wa + (i) * 2048; \
        _Pragma("unroll") for (int ct = 0; ct < 5; ++ct) W[ct] = ld128(b_ + ct * KSS * 1024); } while (0)
    f32x4 acc[5];
#pragma unroll
    for (int ct = 0; ct < 5; ++ct) acc[ct] = (f32x4){};
    s16x8 w[2][5];
    PJ_READ(w[0], 0, 0);
#pragma unroll
    for (int s = 0; s < NS; ++s) {
        if (NS > PJ_R && s >= 1) {
            if (s + 1 >= PJ_R && s + 1 < NS) WAIT_BAR((pj_bar_younger<NA, WIN, HK, NS, P::NWI>(s))); else LDS_BAR();
            SBAR();
            if (s + PJ_R - 1 < NS) pj_issue_w<NKS, KSS>(c, voff, s + PJ_R - 1);
        }
#pragma unroll
        for (int i = 0; i < HK; ++i) {
            const int j = s * HK + i;
            if (i + 1 < HK) PJ_READ(w[(j + 1) & 1], s, i + 1); else if (s + 1 < NS) PJ_READ(w[(j + 1) & 1], s + 1, 0);
            if (j + WIN < NA) ldg_async(a[j + WIN], PJ_ABASE(j + WIN), aoff);
            WAIT_USE((pj_younger<NA, WIN, HK, NS, P::NWI, NRES>(j)), a[j]);
            SBAR();
#pragma unroll
            for (int ct = 0; ct < 5; ++ct) acc[ct] = MFMA16(w[j & 1][ct], a[j], acc[ct]);
            SBAR();
        }
    }
#undef PJ_READ
#undef PJ_ABASE
    if (NCS) { float s = 0.f;
#pragma unroll
        for (int i = 0; i < NCS; ++i) s += __uint_as_float(csv[i]);
        if (cst < 224) *(LAS float*)(lds + P4_CS + ((cst >= 112) * 128 + cst - 112 * (cst >= 112)) * 4) = s; }
    LDS_BAR();
    LAS f32x4* red = (LAS f32x4*)(lds + XCH);
#pragma unroll
    for (int i = 0; i < 5 - NCT; ++i) red[(wid * 3 + i) * 64 + lane] = acc[(KH ? 0 : 3) + i];
    LDS_BAR();
    float s1 = 0.f, s2 = 0.f;
#pragma unroll
    for (int i = 0; i < NCT; ++i) {
        const f32x4 o = red[((wid ^ 4) * 3 + i) * 64 + lane], m = acc[CT0 + i];
        const unsigned w0 = cvtpk(bflo(res[i].x) + m[0] + o[0], bfhi(res[i].x) + m[1] + o[1]), w1 = cvtpk(bflo(res[i].y) + m[2] + o[2], bfhi(res[i].y) + m[3] + o[3]);
        if (FIRST) {
            res[i] = (u32x2){w0, w1};
            st64(c.rs, WS_H + (t16 * 80 + 10 * cc + 2 * (CT0 + i) + (gq >> 1)) * 256 + r * 16 + (gq & 1) * 8, res[i]);
            const float a_ = bflo(w0), b_ = bfhi(w0), c_ = bflo(w1), d = bfhi(w1); s1 += (a_ + b_) + (c_ + d); s2 += (a_ * a_ + b_ * b_) + (c_ * c_ + d * d);
        } else {
            *(GAS u32x2*)(c.out + (size_t)tok * 1280 + cc * 160 + (CT0 + i) * 32 + gq * 8) = (u32x2){w0, w1};
        }
    }
    if (FIRST) {
        s1 += __shfl_xor(s1, 16); s2 += __shfl_xor(s2, 16); s1 = swap32_add(s1); s2 = swap32_add(s2);
        LAS f32x2* ex = (LAS f32x2*)(lds + XCH + 2048);
        if (KH == 1 && gq == 0) ex[t16l * 16 + r] = (f32x2){s1, s2};
        LDS_BAR();
        if (KH == 0 && gq == 0) { const f32x2 e = ex[t16l * 16 + r];
            st64(c.rs, WS_HST + ((t16 * 8 + cc) * 16 + r) * 8, (u32x2){__float_as_uint(s1 + e.x), __float_as_uint(s2 + e.y)}); }
    }
}
template <int NKS, int KSS, bool FIRST>
DI void phase_proj(const Ctx& c, unsigned abase, unsigned wbase, int fc, u32x2 (&res)[3]) {
    if (c.wid < 4) pj_body<NKS, KSS, FIRST, 0>(c, abase, wbase, fc, res);
    else pj_body<NKS, KSS, FIRST, 1>(c, abase, wbase, fc, res);
}

constexpr int P4_R = 7, P4_SLOT = 22528, P4_NS = 20;

DI void p4_wvoff(const Ctx& c, int fc, unsigned& woff0, unsigned& woff1) {
    const unsigned o0 = c.wid * 1792 + c.lane * 16, o1 = c.wid * 1792 + 1024 + (c.lane < 48 ? c.lane : 47) * 16;
    woff0 = WS_W13P + (unsigned)imin(7 * fc + (o0 >> 11), 107) * 40960 + (o0 & 2047); woff1 = WS_W13P + (unsigned)imin(7 * fc + (o1 >> 11), 107) * 40960 + (o1 & 2047);
}
DI void p4_issue_w(const Ctx& c, unsigned woff0, unsigned woff1, int st) {
    const char* w_ = c.ws + st * 2048; const unsigned wdst = c.lds0 + 8192 + c.wid * 1792 + (st % P4_R) * P4_SLOT;
    dma16<true>(w_, woff0, wdst); dma16m<true>(w_, woff1, wdst + 1024, 0xffffffffffffull);
}
DI void p4_prefetch(const Ctx& c, int fc) {
    unsigned woff0, woff1; p4_wvoff(c, fc, woff0, woff1);
#pragma unroll
    for (int s = 0; s < P4_R - 1; ++s) p4_issue_w(c, woff0, woff1, s);
}
constexpr int p4_extra(int s) { int n = 0; for (int t = s - 5 < 0 ? 0 : s - 5; t < s; ++t) if (t <= 14 && t % 3 == 2) ++n; return n; }
template <int UG>
DI void p4_body(const Ctx& c, int qb, int fc) {
    constexpr int NU = UG ? 3 : 4, U0 = UG ? 4 : 0;
    const int lane = c.lane, wid = c.wid, tt = wid & 3, r = lane & 31, h = lane >> 5;
    lptr lds = c.lds;
    const unsigned adst = c.lds0 + wid * 1024;
    const unsigned aoff = WS_H + (unsigned)(8 * qb + wid) * 80 * 256 + lane * 16;
    unsigned woff0, woff1; p4_wvoff(c, fc, woff0, woff1);
#pragma unroll
    for (int s = 0; s < P4_R - 1; ++s) dma16<true>(c.ws + s * 1024, aoff, adst + s * P4_SLOT);
    const unsigned xa = ((2 * tt + (r >> 4)) * 4 + h) * 256 + (r & 15) * 16, wa = 8192 + U0 * 2048 + lane * 16;
    const unsigned t16 = 8 * qb + 2 * tt + (r >> 4);
    u32x2 hs[4];
#pragma unroll
    for (int i = 0; i < 4; ++i) hs[i] = lg64(c.rs, WS_HST + ((t16 * 8 + 4 * h + i) * 16 + (r & 15)) * 8);
#define P4_PIECE(st, k) do { const unsigned sl_ = ((st) % P4_R) * P4_SLOT; \
        if ((k) == 0) dma16<true>(c.ws + (st) * 1024, aoff, adst + sl_); \
        if ((k) == 1) dma16<true>(c.ws + (st) * 2048, woff0, c.lds0 + 8192 + wid * 1792 + sl_); \
        if ((k) == 2) dma16m<true>(c.ws + (st) * 2048, woff1, c.lds0 + 8192 + wid * 1792 + sl_ + 1024, 0xffffffffffffull); } while (0)
#define P4_READ(n) do { lcptr b_ = (lcptr)lds + (((n) >> 1) % P4_R) * P4_SLOT; X[(n) % 3] = ld128(b_ + xa + ((n) & 1) * 512); \
        _Pragma("unroll") for (int u = 0; u < NU; ++u) W[(n) % 3][u] = ld128(b_ + wa + u * 2048 + ((n) & 1) * 1024); } while (0)
    f32x16 acc[NU];
#pragma unroll
    for (int u = 0; u < NU; ++u) acc[u] = (f32x16){};
    WAIT_BAR((P4_R - 2) + 4);
    s16x8 X[3], W[3][NU];
    P4_READ(0); P4_READ(1);
#pragma unroll
    for (int s = 0; s < P4_NS; ++s) {
        WAIT_BARV(fly2(s, P4_R, P4_NS, 1, 2) + (s + 1 < P4_R - 1 ? 4 : 0) + p4_extra(s));
        SBAR();
#define P4_NEXT(k) do { if (s + P4_R - 1 < P4_NS) P4_PIECE(s + P4_R - 1, k); SBAR(); } while (0)
#pragma unroll
        for (int ks = 0; ks < 2; ++ks) {
            const int n = 2 * s + ks;
            if (n + 2 < 2 * P4_NS) P4_READ(n + 2);
            SBAR();
            acc[0] = MFMA32(W[n % 3][0], X[n % 3], acc[0]); SBAR(); P4_NEXT(ks);
            acc[1] = MFMA32(W[n % 3][1], X[n % 3], acc[1]); acc[2] = MFMA32(W[n % 3][2], X[n % 3], acc[2]); SBAR();
            if (ks) P4_NEXT(2);
            if (NU > 3) acc[NU - 1] = MFMA32(W[n % 3][NU - 1], X[n % 3], acc[NU - 1]);
            SBAR();
        }
#undef P4_NEXT
        if (s <= 14 && s % 3 == 2) {
            const int L = imin(((c.b >> 3) * 8 + wid) * 5 + s / 3, 1079);
            dma16<true>(c.ws, WS_W2P + (c.b & 1) * 1105920 + L * 1024 + lane * 16, c.lds0 + P4_R * P4_SLOT + 1024); SBAR(); }
    }
#undef P4_READ
    LDS_BAR();
    float s1 = (__uint_as_float(hs[0].x) + __uint_as_float(hs[1].x)) + (__uint_as_float(hs[2].x) + __uint_as_float(hs[3].x));
    float s2 = (__uint_as_float(hs[0].y) + __uint_as_float(hs[1].y)) + (__uint_as_float(hs[2].y) + __uint_as_float(hs[3].y));
    s1 = swap32_add(s1); s2 = swap32_add(s2);
    const float mean = s1 * (1.0f / D), var = fmaxf(s2 * (1.0f / D) - mean * mean, 0.f), rstd = __builtin_amdgcn_rsqf(var + LN_EPS);
    const f32x2 nm = {-mean, -mean}, k1 = {-1.4426950408889634f * rstd, -1.4426950408889634f * rstd}, r2 = {rstd * rstd, rstd * rstd}, one = {1.0f, 1.0f};
    const LAS float* csl = (const LAS float*)(lds + P4_CS);
#pragma unroll
    for (int u = 0; u < NU; ++u) {
        if (7 * fc + U0 + u < 108) {
            u32x2 w[2];
#pragma unroll
            for (int gq = 0; gq < 2; ++gq) {
                const f32x4 cg_ = *(const LAS f32x4*)(csl + (U0 + u) * 16 + 8 * gq + 4 * h), cu = *(const LAS f32x4*)(csl + 128 + (U0 + u) * 16 + 8 * gq + 4 * h);
                f32x2 o[2];
#pragma unroll
                for (int e = 0; e < 2; ++e) {
                    const f32x2 a = __builtin_elementwise_fma(nm, (f32x2){cg_[2 * e], cg_[2 * e + 1]}, (f32x2){acc[u][4 * gq + 2 * e], acc[u][4 * gq + 2 * e + 1]});
                    const f32x2 b2 = __builtin_elementwise_fma(nm, (f32x2){cu[2 * e], cu[2 * e + 1]}, (f32x2){acc[u][8 + 4 * gq + 2 * e], acc[u][8 + 4 * gq + 2 * e + 1]});
                    const f32x2 t = a * k1, d = (f32x2){__builtin_amdgcn_exp2f(t.x), __builtin_amdgcn_exp2f(t.y)} + one;
                    const f32x2 rc = {__builtin_amdgcn_rcpf(d.x), __builtin_amdgcn_rcpf(d.y)};
                    o[e] = (a * b2) * (rc * r2);
                }
                w[gq] = (u32x2){cvtpk(o[0].x, o[0].y), cvtpk(o[1].x, o[1].y)};
            }
            auto x0 = __builtin_amdgcn_permlane32_swap(w[0].x, w[1].x, false, false), x1 = __builtin_amdgcn_permlane32_swap(w[0].y, w[1].y, false, false);
            st128(c.rs, WS_HM + (t16 * 216 + 14 * fc + 2 * (U0 + u) + h) * 256 + (r & 15) * 16, (u32x4){x0[0], x1[0], x0[1], x1[1]});
        }
    }
}
DI void phase4(const Ctx& c, int qb, int fc) {
    if (c.wid < 4) p4_body<0>(c, qb, fc); else p4_body<1>(c, qb, fc);
}
__global__ __launch_bounds__(512, 2) void block_fwd(const char* __restrict__ x, const char* __restrict__ wqkv, const char* __restrict__ wo, const char* __restrict__ w1,
                                                    const char* __restrict__ w3, const char* __restrict__ w2, char* __restrict__ out, char* __restrict__ ws) {
    extern __shared__ __attribute__((aligned(16))) char smem[];
    Ctx c; c.x = x; c.wqkv = wqkv; c.wo = wo; c.w1 = w1; c.w3 = w3; c.w2 = w2; c.out = out; c.ws = ws;
    c.rs = __builtin_amdgcn_make_buffer_rsrc(ws, 0, WS_END, 0x00020000);
    c.lds = (lptr)smem; c.lds0 = (unsigned)(uintptr_t)smem; c.b = blockIdx.x; c.lane = threadIdx.x & 63; c.wid = __builtin_amdgcn_readfirstlane(threadIdx.x >> 6);
    const int lane = c.lane, g = c.b & 7, idx = c.b >> 3;
    c.tag = *(const __attribute__((address_space(4))) unsigned*)(ws + WS_E);
    u32x2 res[3] = {(u32x2){0u, 0u}, (u32x2){0u, 0u}, (u32x2){0u, 0u}};
    const int qb4 = 8 * (g & 1) + (idx & 7), fc = 4 * (g >> 1) + (idx >> 3), pp = pj_pp(c.b);

    phase1(c);
    WAIT_VM(0);
    publish(c, FL_KV);
    repack_store(c);

    { const int hh = 4 * g + (idx & 3), qb = idx >> 2;
      const int wg = lane < 16 ? wg_p1(lane, (32 + hh) / 6) : lane < 32 ? wg_p1(lane - 16, (64 + hh) / 6) : wg_p1(2 * qb + (lane & 1), hh / 6);
      const unsigned off[1] = {flag_off(FL_KV, wg)}; wait_flags(c, off); }
    phase2(c);
    WAIT_VM(0);
    publish(c, FL_O);
    pj_prefetch<20, 4>(c, WS_WOP);

    { const unsigned off[1] = {flag_off(FL_O, wg_p2(lane & 31, pp >> 2))}; wait_flags(c, off); }
    phase_proj<20, 4, true>(c, WS_O, WS_WOP, fc, res);
    WAIT_VM(0);
    publish(c, FL_H);
    p4_prefetch(c, fc);

    { const unsigned off[1] = {flag_off(FL_H, wg_p3(2 * qb4 + (lane & 1), (lane >> 1) & 7))}; wait_flags(c, off); }
    phase4(c, qb4, fc);
    WAIT_VM(0);
    publish(c, FL_M);
    pj_prefetch<54, 6>(c, WS_W2P);

    { const unsigned fm = flag_off(FL_M, wg_p4(pp >> 1, lane & 15));
      if (c.b == 0) {
          const unsigned off[5] = {fm, flag_off(FL_O, lane), flag_off(FL_O, 64 + lane), flag_off(FL_O, 128 + lane), flag_off(FL_O, 192 + lane)}; wait_flags(c, off);
      } else { const unsigned off[1] = {fm}; wait_flags(c, off); } }
    phase_proj<54, 6, false>(c, WS_HM, WS_W2P, fc, res);
    if (c.b == 0 && threadIdx.x == 0) st32(c.rs, WS_E, c.tag);
}

}

extern "C" void kernel_launch(void* const* d_in, const int* in_sizes, int n_in, void* d_out, int out_size, void* d_ws, size_t ws_size, hipStream_t stream) {
    using namespace blk;
    const char* x = (const char*)d_in[0]; const char* wqkv = (const char*)d_in[1]; const char* wo = (const char*)d_in[2];
    const char* w1 = (const char*)d_in[4]; const char* w3 = (const char*)d_in[5]; const char* w2 = (const char*)d_in[6];
    char* ws = (char*)d_ws; char* out = (char*)d_out;
    hipFuncSetAttribute((const void*)block_fwd, hipFuncAttributeMaxDynamicSharedMemorySize, LDS_BYTES);
    block_fwd<<<256, 512, LDS_BYTES, stream>>>(x, wqkv, wo, w1, w3, w2, out, ws);
}
```

```cpp
#include <hip/hip_runtime.h>
#include <hip/hip_bf16.h>
#include <stdint.h>

namespace blk {

constexpr int S = 2048, D = 640, H = 32, F = 1728;
constexpr float LN_EPS = 1e-5f;
constexpr float QSCALE = 0.22360679774997896f * 1.4426950408889634f;

#define LAS __attribute__((address_space(3)))
#define GAS __attribute__((address_space(1)))
#define DI __device__ __forceinline__
typedef short s16x8 __attribute__((ext_vector_type(8)));
typedef short s16x4 __attribute__((ext_vector_type(4)));
typedef float f32x16 __attribute__((ext_vector_type(16)));
typedef float f32x4 __attribute__((ext_vector_type(4)));
typedef float f32x2 __attribute__((ext_vector_type(2)));
typedef unsigned u32x4 __attribute__((ext_vector_type(4)));
typedef unsigned u32x2 __attribute__((ext_vector_type(2)));
typedef __bf16 bf16x2 __attribute__((ext_vector_type(2)));
typedef LAS char* lptr;
typedef LAS const char* lcptr;
typedef __amdgpu_buffer_rsrc_t rsrc_t;

constexpr unsigned WS_E = 0;
constexpr unsigned WS_FLAG = 4096;
constexpr unsigned WS_ROWP = 256u << 10;
constexpr unsigned WS_COLP = 512u << 10;
constexpr unsigned WS_Q = 1u << 20;
constexpr unsigned WS_KV = 4u << 20;
constexpr unsigned WS_O = 12u << 20;
constexpr unsigned WS_H = 16u << 20;
constexpr unsigned WS_HST = 20u << 20;
constexpr unsigned WS_CSP = 21u << 20;
constexpr unsigned WS_HM = 24u << 20;
constexpr unsigned WS_W13P = 32u << 20;
constexpr unsigned WS_W2P = 40u << 20;
constexpr unsigned WS_WOP = 44u << 20;
constexpr unsigned WS_END = 48u << 20;
enum { FL_KV = 0, FL_PK = 1, FL_O = 2, FL_H = 3, FL_M = 4 };
constexpr int LDS_BYTES = 163840;
constexpr unsigned SPIN_CAP = 1u << 16;
constexpr unsigned CODE_BYTES = 64 * 1024;

#define SBAR() __builtin_amdgcn_sched_barrier(0)
#define PIN(x) asm volatile("" : "+v"(x))
#define MFMA32(a, b, c) __builtin_amdgcn_mfma_f32_32x32x16_bf16(a, b, c, 0, 0, 0)
#define MFMA32K8(a, b, c) __builtin_amdgcn_mfma_f32_32x32x8bf16_1k(a, b, c, 0, 0, 0)
#define MFMA16(a, b, c) __builtin_amdgcn_mfma_f32_16x16x32_bf16(a, b, c, 0, 0, 0)
#define WAIT_BAR(N) asm volatile("s_waitcnt vmcnt(%0) lgkmcnt(0)\n\ts_barrier" :: "i"(N) : "memory")
#define WAIT_BARV(N) asm volatile("s_waitcnt vmcnt(%0)\n\ts_barrier" :: "i"(N) : "memory")
#define WAIT_VM(N) asm volatile("s_waitcnt vmcnt(%0)" :: "i"(N) : "memory")
#define LDS_BAR() asm volatile("s_waitcnt lgkmcnt(0)\n\ts_barrier" ::: "memory")

template <bool SC> DI void dma16(const void* sbase, unsigned voff, unsigned ldsaddr) {
    if (SC) asm volatile("s_mov_b32 m0, %2\n\ts_nop 0\n\tglobal_load_lds_dwordx4 %0, %1 sc1" :: "v"(voff), "s"(sbase), "s"(ldsaddr) : "memory");
    else asm volatile("s_mov_b32 m0, %2\n\ts_nop 0\n\tglobal_load_lds_dwordx4 %0, %1" :: "v"(voff), "s"(sbase), "s"(ldsaddr) : "memory");
}
template <bool SC> DI void dma16m(const void* sbase, unsigned voff, unsigned ldsaddr, unsigned long long mask) {
    if (SC) asm volatile("s_mov_b32 m0, %2\n\ts_mov_b64 exec, %3\n\tglobal_load_lds_dwordx4 %0, %1 sc1\n\ts_mov_b64 exec, -1" :: "v"(voff), "s"(sbase), "s"(ldsaddr), "s"(mask) : "memory");
    else asm volatile("s_mov_b32 m0, %2\n\ts_mov_b64 exec, %3\n\tglobal_load_lds_dwordx4 %0, %1\n\ts_mov_b64 exec, -1" :: "v"(voff), "s"(sbase), "s"(ldsaddr), "s"(mask) : "memory");
}
constexpr int imin(int a, int b) { return a < b ? a : b; }
constexpr int imax(int a, int b) { return a > b ? a : b; }
constexpr int fly(int s, int R, int NS) { return imax(0, imin(R - 3, NS - 2 - s)); }
constexpr int fly2(int s, int R, int NS, int na, int nw) {
    const int need = imin(s + 1, NS - 1);
    const int pro = imin(R - 1, NS);
    const int issued = pro * na + imax(0, imin(s, NS - R + 1)) * (na + nw);
    const int endpos = need < pro ? (need + 1) * na : pro * na + (need - pro + 1) * (na + nw);
    return imax(0, issued - endpos);
}
DI int rot(int i, int rho, int ns) { const int t = i + rho; return t >= ns ? t - ns : t; }
DI unsigned cvtpk(float lo, float hi) { f32x2 v = {lo, hi}; return __builtin_bit_cast(unsigned, __builtin_convertvector(v, bf16x2)); }
DI float bflo(unsigned w) { return __uint_as_float(w << 16); }
DI float bfhi(unsigned w) { return __uint_as_float(w & 0xffff0000u); }
DI s16x8 ld128(lcptr p) { return *(const LAS s16x8*)p; }
DI s16x4 ld64(lcptr p) { return *(const LAS s16x4*)p; }
DI s16x4 ldtr(lcptr p) { return __builtin_bit_cast(s16x4, __builtin_amdgcn_ds_read_tr16_b64_v4i16((LAS s16x4*)p)); }
DI s16x8 cat(s16x4 a, s16x4 b) { return __builtin_shufflevector(a, b, 0, 1, 2, 3, 4, 5, 6, 7); }
DI f32x4 sel4(unsigned m, f32x4 a, f32x4 b) { const u32x4 x = __builtin_bit_cast(u32x4, a), y = __builtin_bit_cast(u32x4, b); return __builtin_bit_cast(f32x4, (x & m) | (y & ~m)); }
DI float swap32_add(float v) { auto r = __builtin_amdgcn_permlane32_swap(__float_as_uint(v), __float_as_uint(v), false, false); return __uint_as_float(r[0]) + __uint_as_float(r[1]); }
DI float swap32_max(float v) { auto r = __builtin_amdgcn_permlane32_swap(__float_as_uint(v), __float_as_uint(v), false, false); return fmaxf(__uint_as_float(r[0]), __uint_as_float(r[1])); }
DI float upper_half(float v) { auto r = __builtin_amdgcn_permlane32_swap(__float_as_uint(v), __float_as_uint(v), false, false); return __uint_as_float(r[1]); }
DI void st128(rsrc_t rs, unsigned off, u32x4 v) { __builtin_amdgcn_raw_buffer_store_b128(v, rs, off, 0, 16); }
DI void st64(rsrc_t rs, unsigned off, u32x2 v) { __builtin_amdgcn_raw_buffer_store_b64(v, rs, off, 0, 16); }
DI void st32(rsrc_t rs, unsigned off, unsigned v) { __builtin_amdgcn_raw_buffer_store_b32(v, rs, off, 0, 16); }
DI u32x4 lg128(rsrc_t rs, unsigned off) { return __builtin_amdgcn_raw_buffer_load_b128(rs, off, 0, 16); }
DI u32x2 lg64(rsrc_t rs, unsigned off) { return __builtin_amdgcn_raw_buffer_load_b64(rs, off, 0, 16); }
DI unsigned lg32(rsrc_t rs, unsigned off) { return __builtin_amdgcn_raw_buffer_load_b32(rs, off, 0, 16); }

DI void ldg_async(s16x8& dst, const void* sbase, unsigned voff) { asm volatile("global_load_dwordx4 %0, %1, %2 sc1" : "=v"(dst) : "v"(voff), "s"(sbase) : "memory"); }
#define WAIT_USE(N, x) asm volatile("s_waitcnt vmcnt(%1)" : "+v"(x) : "i"(N) : "memory")
struct Ctx { const char *code, *x, *wqkv, *wo, *w1, *w3, *w2; char *out, *ws; rsrc_t rs; lptr lds; unsigned lds0, tag; int b, lane, wid; };

DI unsigned flag_off(int kind, int wg) { return WS_FLAG + (unsigned)(kind * 256 + wg) * 128u; }
DI void publish(const Ctx& c, int kind) {
    asm volatile("s_barrier" ::: "memory");
    if (threadIdx.x == 0) st32(c.rs, flag_off(kind, c.b), c.tag);
}
template <int N> DI void wait_flags(const Ctx& c, const unsigned (&off)[N]) {
    if (c.wid == 0) {
        for (unsigned spins = 0; spins < SPIN_CAP; ++spins) {
            unsigned v[N];
#pragma unroll
            for (int i = 0; i < N; ++i) v[i] = lg32(c.rs, off[i]);
            bool ok = true;
#pragma unroll
            for (int i = 0; i < N; ++i) ok &= v[i] == c.tag;
            if (__all(ok)) break;
            __builtin_amdgcn_s_sleep(1);
        }
    }
    asm volatile("s_waitcnt lgkmcnt(0)\n\ts_barrier" ::: "memory");
}
DI int wg_p1(int pb, int nb) { return ((pb >> 3) | ((nb >> 2) << 1)) + 8 * ((pb & 7) + 8 * (nb & 3)); }
DI int wg_p2(int hh, int qb) { return (hh >> 2) + 8 * ((hh & 3) + 4 * qb); }
DI int wg_p3(int pp, int cc) { return (((pp >> 3) << 1) | (cc >> 2)) + 8 * ((pp & 7) + 8 * (cc & 3)); }
DI int wg_p4(int qb, int fc) { return ((qb >> 3) | ((fc >> 2) << 1)) + 8 * ((qb & 7) + 8 * (fc & 3)); }

constexpr int P1_R = 5, P1_SLOT = 32768, P1_NS = 10;
constexpr int P1_STG = 264;
constexpr int P1_OUT = 65536;
constexpr int P1_ST = P1_OUT + 40960;
constexpr int P1_CS = P1_ST + 1024;

DI void repack_issue_one(const Ctx& c, int i);
template <int UP>
DI void p1_loop(Ctx& c, unsigned xoff, unsigned woff, int tt, int rho, unsigned put, unsigned get, f32x16& acc0, f32x16& acc1, u32x2 (&part)[10]) {
    f32x16 accx = {};
    const int lane = c.lane, wid = c.wid, r = lane & 31, h = lane >> 5, q = (lane >> 2) & 3, p = lane & 3, cg = (lane >> 4) & 1;
    constexpr unsigned long long WMASK = ~((1ull << 15) | (1ull << 27) | (1ull << 39) | (1ull << 51));
    const unsigned xdst = c.lds0 + wid * 2048, wdst = c.lds0 + 16384 + wid * 2048;
    const int xrow = 32 * tt + r;
    const unsigned xa = xrow * 128 + ((h ^ ((xrow >> 1) & 7)) * 16);
    const unsigned wk = 16384 + (8 * h + q) * 256 + (p & 1) * 8;
    const unsigned wa0 = wk + (((4 * (2 * UP) + 2 * cg + (p >> 1)) ^ (4 * q)) * 16);
    const unsigned wa1 = wk + (((4 * (2 * UP + 1) + 2 * cg + (p >> 1)) ^ (4 * q)) * 16);
    const unsigned waz = wk + (((4 * tt + 2 * cg + (p >> 1)) ^ (4 * q)) * 16);
    const s16x8 ones = {0x3f80, 0x3f80, 0x3f80, 0x3f80, 0x3f80, 0x3f80, 0x3f80, 0x3f80};
#define P1_PIECE(st, slot, k) do { const int p_ = rot(st, rho, P1_NS); \
        if ((k) == 0) dma16<false>(c.x + p_ * 128, xoff, xdst + (slot)); \
        if ((k) == 1) dma16<false>(c.x + p_ * 128 + 8 * 1280, xoff ^ 64, xdst + (slot) + 1024); \
        if ((k) == 2) dma16m<false>(c.wqkv + (size_t)p_ * (64 * 3840), woff, wdst + (slot), WMASK); \
        if ((k) == 3) dma16m<false>(c.wqkv + (size_t)p_ * (64 * 3840) + 4 * 3840, woff, wdst + (slot) + 1024, WMASK); } while (0)
#define P1_ISSUE(st, slot) do { P1_PIECE(st, slot, 0); P1_PIECE(st, slot, 1); P1_PIECE(st, slot, 2); P1_PIECE(st, slot, 3); } while (0)
#define P1_READ(n) do { lcptr b_ = (lcptr)c.lds + (((n) >> 2) % P1_R) * P1_SLOT; \
        X[(n) % 3] = ld128(b_ + (xa ^ (((n) & 3) * 32))); \
        W0[(n) % 3] = cat(ldtr(b_ + wa0 + ((n) & 3) * 4096), ldtr(b_ + wa0 + ((n) & 3) * 4096 + 1024)); \
        W1[(n) % 3] = cat(ldtr(b_ + wa1 + ((n) & 3) * 4096), ldtr(b_ + wa1 + ((n) & 3) * 4096 + 1024)); \
        if (UP && (n) < 4) WZ[(n) & 3] = cat(ldtr(b_ + waz + ((n) & 3) * 4096), ldtr(b_ + waz + ((n) & 3) * 4096 + 1024)); } while (0)
#define P1_MMA(n) do { acc0 = MFMA32(W0[(n) % 3], X[(n) % 3], acc0); acc1 = MFMA32(W1[(n) % 3], X[(n) % 3], acc1); \
        if ((n) < 4) { if (UP) accx = MFMA32(WZ[(n) & 3], ones, accx); else accx = MFMA32(X[(n) % 3], X[(n) % 3], accx); } } while (0)
    P1_ISSUE(0, 0); P1_ISSUE(1, P1_SLOT); P1_ISSUE(2, 2 * P1_SLOT);
    WAIT_BAR(8);
    asm volatile("" : "+s"(c.tag));
    c.tag += 1u;
    P1_ISSUE(3, 3 * P1_SLOT);
    {
        const unsigned piece = ((unsigned)(c.b >> 3) * 8 + wid) * 1024;
        dma16<false>(c.code, (piece < CODE_BYTES ? piece : CODE_BYTES - 1024) + lane * 16, xdst + 4 * P1_SLOT); }
    s16x8 X[3] = {}, W0[3] = {}, W1[3] = {}, WZ[4] = {};
    P1_READ(0); P1_READ(1);
#pragma unroll
    for (int s = 0; s < P1_NS; ++s) {
        constexpr int NX = P1_R - 1, GS = P1_NS - P1_R;
        WAIT_BARV(4 * fly(s, P1_R, P1_NS) + (s + 1 <= P1_R - 2 ? 1 : 0) + (s >= 1 && s + 1 <= P1_R - 1 ? 1 : 0) + (s > GS && !UP ? 10 : 0) + imax(0, s - GS - 1));
        SBAR();
#pragma unroll
        for (int ks = 0; ks < 4; ++ks) {
            const int n = 4 * s + ks;
            if (n + 2 < 4 * P1_NS) P1_READ(n + 2);
            SBAR(); P1_MMA(n); SBAR();
            if (s + NX < P1_NS) P1_PIECE(s + NX, ((s + NX) % P1_R) * P1_SLOT, ks);
            if (s > GS && ks == 1) repack_issue_one(c, s - GS - 1);
            SBAR();
        }
        if (s == 0) {
            const int tr = (r & 3) + 4 * (r >> 3); float v = 0.f;
#pragma unroll
            for (int i = 0; i < 16; ++i) v = (tr == i) ? accx[i] : v;
            st64(c.rs, put + (h == ((r >> 2) & 1) ? (tt * 32 + r) * 8 : 0x7fffff00u), (u32x2){__float_as_uint(v), c.tag});
            SBAR();
        }
        if (s == GS && !UP) {
#pragma unroll
            for (int i = 0; i < 10; ++i) part[i] = lg64(c.rs, get + i * 1024);
            SBAR();
        }
    }
#undef P1_PIECE
#undef P1_ISSUE
#undef P1_READ
#undef P1_MMA
}

DI void phase1(Ctx& c) {
    const int lane = c.lane, wid = c.wid, tid = threadIdx.x, g = c.b & 7, idx = c.b >> 3;
    const int pb = 8 * (g & 1) + (idx & 7), nb = 4 * (g >> 1) + (idx >> 3);
    const int tt = wid & 3, up = wid >> 2, r = lane & 31, h = lane >> 5;
    lptr lds = c.lds;
    if (tid < P1_R * 64) { const int sl = tid >> 6, k = tid & 63;
        *(LAS u32x4*)(lds + sl * P1_SLOT + 16384 + k * 256 + ((15 ^ (4 * (k & 3))) * 16)) = (u32x4){0x3f803f80u, 0x3f803f80u, 0x3f803f80u, 0x3f803f80u}; }
    const int xr = 16 * wid + (lane >> 3), xc = (lane & 7) ^ ((xr >> 1) & 7);
    const unsigned xoff = (pb * 128 + xr) * 1280 + xc * 16;
    const int wkk = 8 * wid + (lane >> 4), wp = (lane & 15) ^ (4 * (wkk & 3)), wpc = wp < 15 ? wp : 14;
    const unsigned woff = wkk * 3840 + nb * 240 + wpc * 16;
    f32x16 acc0 = {}, acc1 = {};
    const int rho = (pb + 3 * nb) % P1_NS;
    const unsigned put = up ? WS_COLP + (unsigned)(nb * 16 + pb) * 1024 : WS_ROWP + (unsigned)(pb * 16 + nb) * 1024;
    const int gi = (wid & 1) * 64 + lane;
    const unsigned get = ((wid & 2) ? WS_COLP + (unsigned)nb * 16384 : WS_ROWP + (unsigned)pb * 16384) + gi * 8;
    u32x2 part[10];
    if (up == 0) p1_loop<0>(c, xoff, woff, tt, rho, put, get, acc0, acc1, part);
    else p1_loop<1>(c, xoff, woff, tt, rho, put, get, acc0, acc1, part);
    float psum = 0.f;
    if (up == 0) {
        for (unsigned spins = 0; spins < SPIN_CAP; ++spins) {
            bool ok = true;
#pragma unroll
            for (int i = 0; i < 10; ++i) ok &= part[i].y == c.tag;
            if (__all(ok)) break;
            __builtin_amdgcn_s_sleep(1);
#pragma unroll
            for (int i = 0; i < 10; ++i) part[i] = lg64(c.rs, get + i * 1024);
        }
#pragma unroll
        for (int i = 0; i < 10; ++i) psum += __uint_as_float(part[i].x);
    }
    LDS_BAR();
    LAS float* st = (LAS float*)(lds + P1_ST); LAS float* cs = (LAS float*)(lds + P1_CS);
    if (wid < 2) st[gi * 2 + 1] = psum; else if (wid < 4) cs[gi] = psum;
    if (up == 1 && h == 0) st[(tt * 32 + r) * 2] = acc1[12];
    LDS_BAR();
    { const f32x2 sv = *(const LAS f32x2*)(st + (tt * 32 + r) * 2);
      const float mean = sv.x * (1.0f / D), var = fmaxf(sv.y * (1.0f / D) - mean * mean, 0.f), rstd = __builtin_amdgcn_rsqf(var + LN_EPS), rm = rstd * mean;
      lptr stg = lds + P1_OUT + (32 * tt + r) * P1_STG;
#pragma unroll
      for (int u = 0; u < 2; ++u)
#pragma unroll
        for (int gq = 0; gq < 4; ++gq) {
            const int col = (2 * up + u) * 32 + 8 * gq + 4 * h;
            const f32x4 c4 = *(const LAS f32x4*)(cs + col);
            const float sc = (nb * 120 + col < D) ? QSCALE : 1.0f, a = rstd * sc, bb = rm * sc;
            float v[4];
#pragma unroll
            for (int e = 0; e < 4; ++e) v[e] = a * (u ? acc1[4 * gq + e] : acc0[4 * gq + e]) - bb * c4[e];
            *(LAS u32x2*)(stg + col * 2) = (u32x2){cvtpk(v[0], v[1]), cvtpk(v[2], v[3])};
        }
    }
    LDS_BAR();
#pragma unroll
    for (int j = 0; j < 3; ++j) {
        const int slot = up + 2 * j, gidx = 6 * nb + slot, type = gidx >> 5, head = gidx & 31, tile = 4 * pb + tt;
        if (type < 2) {
            lcptr row = (lcptr)lds + P1_OUT + (32 * tt + r) * P1_STG + slot * 40;
            const u32x2 a0 = *(const LAS u32x2*)(row + 16 * h), a1 = *(const LAS u32x2*)(row + 16 * h + 8), a2 = *(const LAS u32x2*)(row + 32);
            const unsigned dst = type ? WS_KV + (unsigned)(head * 64 + tile) * 3072 : WS_Q + (unsigned)(head * 64 + tile) * 1536;
            st128(c.rs, dst + lane * 16, (u32x4){a0.x, a0.y, a1.x, a1.y});
            st64(c.rs, dst + 1024 + lane * 8, h ? (u32x2){type ? 0x00003f80u : 0u, 0u} : a2);
        } else {
            const int q = (lane >> 2) & 3, p = lane & 3, cg = (lane >> 4) & 1;
            lcptr vb = (lcptr)lds + P1_OUT + (32 * tt + 4 * h + q) * P1_STG + slot * 40 + (cg * 4 + p < 5 ? (cg * 4 + p) * 8 : 0);
            const unsigned dst = WS_KV + (unsigned)(head * 64 + tile) * 3072 + 1536;
#pragma unroll
            for (int s = 0; s < 2; ++s) {
                const u32x2 a = __builtin_bit_cast(u32x2, ldtr(vb + (16 * s) * P1_STG)), e = __builtin_bit_cast(u32x2, ldtr(vb + (16 * s + 8) * P1_STG));
                u32x4 v = {a.x, a.y, e.x, e.y};
                if (r == 20) v = (u32x4){0x3f803f80u, 0x3f803f80u, 0x3f803f80u, 0x3f803f80u};
                if (r > 20) v = (u32x4){0u, 0u, 0u, 0u};
                if (r < 24) st128(c.rs, dst + s * 768 + h * 384 + r * 16, v);
            }
        }
    }
}

constexpr int PK_OFF = 0;
DI int pk_job(int b, int i) { const int j = b + 256 * i; return j < 910 ? j : j - 256; }
DI void repack_issue_one(const Ctx& c, int i) {
    const int row = 8 * c.wid + (c.lane >> 3), pc = c.lane & 7;
    const int j = pk_job(c.b, i);
    const char* src; unsigned off;
    if (j < 540) { const int jj = j < 270 ? j : j - 270, kb = jj / 27, cb = jj % 27; src = j < 270 ? c.w1 : c.w3; off = (64 * kb + row) * 3456 + cb * 128 + pc * 16; }
    else if (j < 810) { const int jj = j - 540, kb = jj / 10, cb = jj % 10; src = c.w2; off = (64 * kb + row) * 1280 + cb * 128 + pc * 16; }
    else { const int jj = j - 810, kb = jj / 10, cb = jj % 10, kp = 64 * kb + row, G = kp >> 3, e = kp & 7;
        const int k = G < 64 ? 20 * (G >> 1) + 8 * (G & 1) + e : 40 * (G - 64) + 16 + (e < 4 ? e : e + 16);
        src = c.wo; off = k * 1280 + cb * 128 + pc * 16; }
    dma16<false>(src, off, c.lds0 + PK_OFF + i * 8192 + c.wid * 1024);
}
DI void repack_store(const Ctx& c) {
    const int lane = c.lane, w = c.wid, g = lane >> 4, i16 = lane & 15, q = i16 >> 2, p = i16 & 3;
#pragma unroll
    for (int i = 0; i < 4; ++i) {
        const int j = pk_job(c.b, i);
        lcptr blk = (lcptr)c.lds + PK_OFF + i * 8192 + (w >> 1) * 32 + p * 8;
        if (j < 540) {
            const int mat = j >= 270, jj = mat ? j - 270 : j, kb = jj / 27, cb = jj % 27, ks = 2 * (w & 1) + (g >> 1), hh = g & 1;
            lcptr a = blk + (16 * ks + 8 * hh + q) * 128;
            const u32x2 v0 = __builtin_bit_cast(u32x2, ldtr(a)), v1 = __builtin_bit_cast(u32x2, ldtr(a + 512));
            st128(c.rs, WS_W13P + (unsigned)((4 * cb + (w >> 1)) * 40 + 4 * kb + ks) * 1024 + (mat * 16 + i16 + 32 * hh) * 16, (u32x4){v0.x, v0.y, v1.x, v1.y});
            float s = ((bflo(v0.x) + bfhi(v0.x)) + (bflo(v0.y) + bfhi(v0.y))) + ((bflo(v1.x) + bfhi(v1.x)) + (bflo(v1.y) + bfhi(v1.y)));
            s += __shfl_xor(s, 16); s += __shfl_xor(s, 32);
            if (lane < 16) st32(c.rs, WS_CSP + (unsigned)((mat * 20 + 2 * kb + (w & 1)) * F + 64 * cb + 16 * (w >> 1) + i16) * 4, __float_as_uint(s));
        } else {
            const bool isw2 = j < 810; const int jj = isw2 ? j - 540 : j - 810, kb = jj / 10, cb = jj % 10, ks = w & 1;
            lcptr a = blk + (32 * ks + 8 * g + q) * 128;
            const u32x2 v0 = __builtin_bit_cast(u32x2, ldtr(a)), v1 = __builtin_bit_cast(u32x2, ldtr(a + 512));
            const unsigned dst = isw2 ? WS_W2P + (unsigned)((4 * cb + (w >> 1)) * 54 + 2 * kb + ks) * 1024 : WS_WOP + (unsigned)((4 * cb + (w >> 1)) * 20 + 2 * kb + ks) * 1024;
            st128(c.rs, dst + lane * 16, (u32x4){v0.x, v0.y, v1.x, v1.y});
        }
    }
}

constexpr int P2_R = 4, P2_SLOT = 24576, P2_NS = 8;
constexpr int P2_Q = 110592;
constexpr int XCH = 135168;
constexpr int P2_EX = XCH;
constexpr int P4_CS = XCH + 24576;

DI void attn_safe(const Ctx& c, unsigned kv, s16x8 q0, s16x4 q1z, f32x16& o) {
    const int lane = c.lane, r = lane & 31, h = lane >> 5, vr = r < 24 ? r : r - 16;
    float m = -INFINITY; o = (f32x16){};
    for (int t = 0; t < 64; ++t) {
        const unsigned p = kv + t * 3072;
        const u32x4 k0 = lg128(c.rs, p + lane * 16); const u32x2 k1 = lg64(c.rs, p + 1024 + lane * 8);
        const u32x4 v0 = lg128(c.rs, p + 1536 + h * 384 + vr * 16), v1 = lg128(c.rs, p + 1536 + 768 + h * 384 + vr * 16);
        f32x16 s = MFMA32(__builtin_bit_cast(s16x8, k0), q0, (f32x16){}); s = MFMA32K8(__builtin_bit_cast(s16x4, k1), q1z, s);
        float tm = s[0];
#pragma unroll
        for (int i = 1; i < 16; ++i) tm = fmaxf(tm, s[i]);
        tm = swap32_max(tm);
        const float mn = fmaxf(m, tm), al = __builtin_amdgcn_exp2f(m - mn); m = mn;
#pragma unroll
        for (int i = 0; i < 16; ++i) { o[i] *= al; s[i] = __builtin_amdgcn_exp2f(s[i] - mn); }
        const u32x4 p0 = {cvtpk(s[0], s[1]), cvtpk(s[2], s[3]), cvtpk(s[4], s[5]), cvtpk(s[6], s[7])};
        const u32x4 p1 = {cvtpk(s[8], s[9]), cvtpk(s[10], s[11]), cvtpk(s[12], s[13]), cvtpk(s[14], s[15])};
        o = MFMA32(__builtin_bit_cast(s16x8, v0), __builtin_bit_cast(s16x8, p0), o); o = MFMA32(__builtin_bit_cast(s16x8, v1), __builtin_bit_cast(s16x8, p1), o);
    }
}

DI void phase2(const Ctx& c) {
    const int lane = c.lane, wid = c.wid, g = c.b & 7, idx = c.b >> 3, hh = 4 * g + (idx & 3), qb = idx >> 2;
    const int qg = wid & 3, kh = wid >> 2, r = lane & 31, h = lane >> 5;
    lptr lds = c.lds;
    const char* kvsrc = c.ws + WS_KV + (size_t)hh * 64 * 3072 + wid * 3072;
    const unsigned dst0 = c.lds0 + wid * 3072, voff = lane * 16;
#define P2_ISSUE(st, slot) do { const char* s_ = kvsrc + (size_t)(st) * 24576; dma16<true>(s_, voff, dst0 + (slot)); dma16<true>(s_ + 1024, voff, dst0 + (slot) + 1024); dma16<true>(s_ + 2048, voff, dst0 + (slot) + 2048); } while (0)
    { const char* qsrc = c.ws + WS_Q + ((size_t)hh * 64 + 8 * qb + wid) * 1536;
      dma16<true>(qsrc, voff, c.lds0 + P2_Q + wid * 1536); dma16m<true>(qsrc + 1024, voff, c.lds0 + P2_Q + wid * 1536 + 1024, 0xffffffffull); }
    P2_ISSUE(0, 0); P2_ISSUE(1, P2_SLOT);
    const int vr = r < 24 ? r : 21 + (r & 1);
    const unsigned ka = 4 * kh * 3072 + lane * 16, kb = 4 * kh * 3072 + 1024 + lane * 8, va = 4 * kh * 3072 + 1536 + h * 384 + vr * 16;
#define LDK0(slot, t) ld128((lcptr)lds + (slot) + ka + (t) * 3072)
#define LDK1(slot, t) ld64((lcptr)lds + (slot) + kb + (t) * 3072)
#define LDV0(slot, t) ld128((lcptr)lds + (slot) + va + (t) * 3072)
#define LDV1(slot, t) ld128((lcptr)lds + (slot) + va + (t) * 3072 + 768)
    WAIT_BAR(3);
    if (threadIdx.x == 0) st32(c.rs, flag_off(FL_PK, c.b), c.tag);
    P2_ISSUE(2, 2 * P2_SLOT);
    s16x8 q0[2]; s16x4 q1z[2], q1[2];
#pragma unroll
    for (int i = 0; i < 2; ++i) { q0[i] = ld128((lcptr)lds + P2_Q + (2 * qg + i) * 1536 + lane * 16); q1z[i] = ld64((lcptr)lds + P2_Q + (2 * qg + i) * 1536 + 1024 + lane * 8); }
    { const s16x8 k0 = ld128((lcptr)lds + lane * 16); const s16x4 k1 = ld64((lcptr)lds + 1024 + lane * 8);
#pragma unroll
      for (int i = 0; i < 2; ++i) { f32x16 s = MFMA32(k0, q0[i], (f32x16){}); s = MFMA32K8(k1, q1z[i], s);
          float m = s[0];
#pragma unroll
          for (int e = 1; e < 16; ++e) m = fmaxf(m, s[e]);
          m = swap32_max(m);
          q1[i] = q1z[i]; if (h) q1[i][0] = (short)(cvtpk(-m, 0.f) & 0xffffu); } }
    s16x8 kA0 = LDK0(0, 0), kB0, vA0 = LDV0(0, 0), vA1 = LDV1(0, 0), vB0, vB1 = vA1; s16x4 kA1 = LDK1(0, 0), kB1;
    f32x16 sA, sB, o[2] = {(f32x16){}, (f32x16){}};
    sA = MFMA32(kA0, q0[0], (f32x16){}); sA = MFMA32K8(kA1, q1[0], sA);
    u32x4 p0, p1 = {0u, 0u, 0u, 0u};
    int cur = 0, nxt = P2_SLOT, fre = (P2_R - 1) * P2_SLOT;
#define EX(v) __builtin_amdgcn_exp2f(v)
#define P2_STEP(C, N, K0, K1, NQ, OC, V0, OP, VP, LOADS, LAST) do { \
        OP = MFMA32(VP, __builtin_bit_cast(s16x8, p1), OP); SBAR(); \
        LOADS; SBAR(); \
        C[0] = EX(C[0]); C[1] = EX(C[1]); C[2] = EX(C[2]); C[3] = EX(C[3]); p0[0] = cvtpk(C[0], C[1]); p0[1] = cvtpk(C[2], C[3]); PIN(p0); SBAR(); \
        if (!(LAST)) N = MFMA32(K0, q0[NQ], (f32x16){}); SBAR(); \
        C[4] = EX(C[4]); C[5] = EX(C[5]); C[6] = EX(C[6]); C[7] = EX(C[7]); p0[2] = cvtpk(C[4], C[5]); p0[3] = cvtpk(C[6], C[7]); PIN(p0); SBAR(); \
        if (!(LAST)) N = MFMA32K8(K1, q1[NQ], N); SBAR(); \
        C[8] = EX(C[8]); C[9] = EX(C[9]); C[10] = EX(C[10]); C[11] = EX(C[11]); p1[0] = cvtpk(C[8], C[9]); p1[1] = cvtpk(C[10], C[11]); PIN(p1); SBAR(); \
        OC = MFMA32(V0, __builtin_bit_cast(s16x8, p0), OC); SBAR(); \
        C[12] = EX(C[12]); C[13] = EX(C[13]); C[14] = EX(C[14]); C[15] = EX(C[15]); p1[2] = cvtpk(C[12], C[13]); p1[3] = cvtpk(C[14], C[15]); PIN(p1); SBAR(); \
    } while (0)
#define P2_PIECE(st, k) do { if ((st) >= 0) dma16<true>(kvsrc + (size_t)(st) * 24576 + (k) * 1024, voff, dst0 + fre + (k) * 1024); } while (0)
#define P2_PAIR(T, NS_, NT, LAST, ST, KA, KB) do { \
        if (((T) == 0) == (kh != 0)) __builtin_amdgcn_s_setprio(1); else __builtin_amdgcn_s_setprio(0); \
        P2_STEP(sA, sB, kA0, kA1, 1, o[0], vA0, o[1], vB1, { kB0 = LDK0(cur, (T) + 1); kB1 = LDK1(cur, (T) + 1); vB0 = LDV0(cur, (T) + 1); vB1 = LDV1(cur, (T) + 1); }, false); \
        P2_STEP(sB, sA, kB0, kB1, 0, o[1], vA0, o[0], vA1, { P2_PIECE(ST, KA); }, false); \
        P2_STEP(sA, sB, kB0, kB1, 1, o[0], vB0, o[1], vA1, { if (!(LAST)) { kA0 = LDK0(NS_, NT); kA1 = LDK1(NS_, NT); vA0 = LDV0(NS_, NT); vA1 = LDV1(NS_, NT); } }, false); \
        P2_STEP(sB, sA, kA0, kA1, 0, o[1], vB0, o[0], vB1, { if ((KB) >= 0) P2_PIECE(ST, KB); }, LAST); } while (0)
#define P2_STAGE(LAST, ST) do { P2_PAIR(0, cur, 2, false, ST, 0, 1); P2_PAIR(2, nxt, 0, LAST, ST, 2, -1); \
        fre = cur; cur = nxt; nxt = (nxt == (P2_R - 1) * P2_SLOT) ? 0 : nxt + P2_SLOT; } while (0)
#pragma unroll
    for (int s = 0; s < P2_NS - 3; ++s) { WAIT_BAR(3); P2_STAGE(false, s + P2_R - 1); }
    WAIT_BAR(3);
    unsigned pk[4];
    if (wid == 0) {
#pragma unroll
        for (int i = 0; i < 4; ++i) pk[i] = lg32(c.rs, flag_off(FL_PK, 64 * i + lane));
    }
    P2_STAGE(false, -1);
    if (wid == 0) {
        for (unsigned spins = 0; spins < SPIN_CAP; ++spins) {
            if (__all(pk[0] == c.tag && pk[1] == c.tag && pk[2] == c.tag && pk[3] == c.tag)) break;
            __builtin_amdgcn_s_sleep(1);
#pragma unroll
            for (int i = 0; i < 4; ++i) pk[i] = lg32(c.rs, flag_off(FL_PK, 64 * i + lane));
        }
    }
    WAIT_BAR(0);
    const unsigned wsc = c.lds0 + P2_Q + 12288 + wid * 1024;
    unsigned wu[6];
#pragma unroll
    for (int i = 0; i < 6; ++i) { const int L = imin((idx * 8 + wid) * 6 + i, 1519);
        wu[i] = (L < 400 ? WS_WOP + (g & 1) * 409600 + L * 1024 : WS_W13P + imin((g >> 1) * 1146880 + (L - 400) * 1024, 108 * 40960 - 1024)) + lane * 16; }
#define P2_WARM(i) dma16<true>(c.ws, wu[i], wsc)
    P2_PAIR(0, cur, 2, false, -1, 0, 1); P2_WARM(0); P2_WARM(1); P2_PAIR(2, nxt, 0, false, -1, 2, -1); P2_WARM(2);
    fre = cur; cur = nxt; nxt = (nxt == (P2_R - 1) * P2_SLOT) ? 0 : nxt + P2_SLOT;
    P2_WARM(3); P2_PAIR(0, cur, 2, false, -1, 0, 1); P2_WARM(4); P2_WARM(5); P2_PAIR(2, nxt, 0, true, -1, 2, -1);
#undef P2_WARM
    o[1] = MFMA32(vB1, __builtin_bit_cast(s16x8, p1), o[1]);
#undef P2_STEP
#undef P2_PAIR
#undef P2_PIECE
#undef P2_STAGE
#undef P2_ISSUE
    __builtin_amdgcn_s_setprio(0);
    LAS f32x4* ex = (LAS f32x4*)(lds + P2_EX);
    { const unsigned m = kh ? 0xffffffffu : 0u;
#pragma unroll
      for (int i = 0; i < 3; ++i) { const f32x4 a = {o[0][4 * i], o[0][4 * i + 1], o[0][4 * i + 2], o[0][4 * i + 3]}, e = {o[1][4 * i], o[1][4 * i + 1], o[1][4 * i + 2], o[1][4 * i + 3]};
          ex[(wid * 3 + i) * 64 + lane] = sel4(m, a, e); }
      LDS_BAR();
      f32x16 of = {};
#pragma unroll
      for (int i = 0; i < 3; ++i) { const f32x4 a = {o[0][4 * i], o[0][4 * i + 1], o[0][4 * i + 2], o[0][4 * i + 3]}, e = {o[1][4 * i], o[1][4 * i + 1], o[1][4 * i + 2], o[1][4 * i + 3]};
          const f32x4 t = sel4(m, e, a) + ex[((wid ^ 4) * 3 + i) * 64 + lane];
          of[4 * i] = t[0]; of[4 * i + 1] = t[1]; of[4 * i + 2] = t[2]; of[4 * i + 3] = t[3]; }
      float l = upper_half(of[8]);
      bool bad = !(l < 1.2676506e30f && l > 8.6736174e-19f);
#pragma unroll
      for (int i = 0; i < 12; ++i) bad |= !(fabsf(of[i]) < 3.0e38f);
      if (__any(bad)) {
          const s16x8 qq0 = ld128((lcptr)lds + P2_Q + (2 * qg + kh) * 1536 + lane * 16); const s16x4 qq1 = ld64((lcptr)lds + P2_Q + (2 * qg + kh) * 1536 + 1024 + lane * 8);
          attn_safe(c, WS_KV + (unsigned)hh * 64 * 3072, qq0, qq1, of); l = upper_half(of[8]); }
      const float il = __builtin_amdgcn_rcpf(l);
      const unsigned a0 = cvtpk(of[0] * il, of[1] * il), a1 = cvtpk(of[2] * il, of[3] * il), b0 = cvtpk(of[4] * il, of[5] * il), b1 = cvtpk(of[6] * il, of[7] * il);
      auto x0 = __builtin_amdgcn_permlane32_swap(a0, b0, false, false), x1 = __builtin_amdgcn_permlane32_swap(a1, b1, false, false);
      const unsigned t16 = 2 * (8 * qb + 2 * qg + kh) + (r >> 4);
      st128(c.rs, WS_O + (t16 * 80 + 2 * hh + h) * 256 + (r & 15) * 16, (u32x4){x0[0], x1[0], x0[1], x1[1]});
      if (h == 0) st64(c.rs, WS_O + (t16 * 80 + 64 + (hh >> 1)) * 256 + (r & 15) * 16 + (hh & 1) * 8, (u32x2){cvtpk(of[8] * il, of[9] * il), cvtpk(of[10] * il, of[11] * il)});
    }
#undef LDK0
#undef LDK1
#undef LDV0
#undef LDV1
#undef EX
}

constexpr int PJ_R = 5;
template <int NKS, int KSS> struct PJ {
    static constexpr int NS = NKS / KSS, SLOT = 5 * KSS * 1024, NWI = (5 * KSS + 7) / 8, NA = NKS / 2, PRE = imin(PJ_R, NS);
};
template <int NKS, int KSS> DI void pj_issue_w(const Ctx& c, const unsigned (&voff)[(5 * KSS + 7) / 8], int st) {
    using P = PJ<NKS, KSS>;
    const char* base = c.ws + st * (KSS * 1024);
#pragma unroll
    for (int i = 0; i < P::NWI; ++i) dma16<true>(base, voff[i], c.lds0 + (st % PJ_R) * P::SLOT + imin(c.wid + 8 * i, 5 * KSS - 1) * 1024);
}
template <int NKS, int KSS> DI void pj_voff(const Ctx& c, unsigned wbase, int cc, unsigned (&voff)[(5 * KSS + 7) / 8]) {
#pragma unroll
    for (int i = 0; i < PJ<NKS, KSS>::NWI; ++i) { const int f = imin(c.wid + 8 * i, 5 * KSS - 1); voff[i] = wbase + (unsigned)((5 * cc + f / KSS) * NKS + f % KSS) * 1024 + c.lane * 16; }
}
DI int pj_pp(int b) { return 8 * ((b & 7) >> 1) + ((b >> 3) & 7); }
DI int pj_cc(int b) { return 4 * (b & 1) + (b >> 6); }
template <int NKS, int KSS> DI void pj_prefetch(const Ctx& c, unsigned wbase) {
    unsigned voff[PJ<NKS, KSS>::NWI]; pj_voff<NKS, KSS>(c, wbase, pj_cc(c.b), voff);
#pragma unroll
    for (int s = 0; s < PJ<NKS, KSS>::PRE; ++s) pj_issue_w<NKS, KSS>(c, voff, s);
}

template <int NA, int WIN, int HK, int NS, int NWI, int NRES> constexpr int pj_younger(int j) {
    int n = imin(NA - 1, j + WIN) - j;
    if (j < WIN) n += NRES;
    for (int s = 1; s + PJ_R - 1 < NS; ++s)
        if (HK * s <= j && (j < WIN || HK * s > j - WIN)) n += NWI;
    return n;
}
template <int NA, int WIN, int HK, int NS, int NWI> constexpr int pj_bar_younger(int s) {
    const int s0 = s + 2 - PJ_R;
    int n = 0;
    for (int t = s0 + 1; t < s; ++t) if (t + PJ_R - 1 < NS) n += NWI;
    for (int j = HK * s0; j < HK * s; ++j) if (j + WIN < NA) ++n;
    return n;
}
template <int NKS, int KSS, bool FIRST, int KH>
DI void pj_body(const Ctx& c, unsigned abase, unsigned wbase, int fc, u32x2 (&res)[3]) {
    using P = PJ<NKS, KSS>;
    constexpr int NS = P::NS, NG = NKS * 4, NA = P::NA, HK = KSS / 2, NCT = KH ? 2 : 3, CT0 = KH ? 3 : 0;
    const int lane = c.lane, wid = c.wid, t16l = wid & 3, r = lane & 15, gq = lane >> 4, pp = pj_pp(c.b), cc = pj_cc(c.b);
    lptr lds = c.lds;
    const unsigned t16 = 4 * pp + t16l;
    const int tok = 16 * t16 + r;
    constexpr int WIN = imin(NA, FIRST ? 4 : 9);
    s16x8 a[NA];
    const unsigned aoff = abase + (t16 * NG + 4 * KH) * 256 + lane * 16;
#define PJ_ABASE(j) (c.ws + (j) * 2048)
#pragma unroll
    for (int j = 0; j < WIN; ++j) ldg_async(a[j], PJ_ABASE(j), aoff);
    if (FIRST) {
#pragma unroll
        for (int i = 0; i < NCT; ++i) res[i] = *(const GAS u32x2*)(c.x + (size_t)tok * 1280 + cc * 160 + (CT0 + i) * 32 + gq * 8);
    }
    constexpr int NCS = FIRST && KH ? 20 : 0;
    unsigned csv[NCS ? NCS : 1];
    const int cst = wid * 64 + lane - 256;
    if (NCS) { const int mat = cst >= 112, col = imin(fc * 112 + imin(cst, 223) - 112 * mat, F - 1);
#pragma unroll
        for (int i = 0; i < NCS; ++i) csv[i] = lg32(c.rs, WS_CSP + (unsigned)((mat * 20 + i) * F + col) * 4); }
    unsigned voff[P::NWI]; pj_voff<NKS, KSS>(c, wbase, cc, voff);
    constexpr int NRES = (FIRST ? NCT : 0) + NCS;
    WAIT_BARV(WIN + NRES);
    const unsigned wa = KH * 1024 + lane * 16;
#define PJ_READ(W, st, i) do { lcptr b_ = (lcptr)lds + ((st) % PJ_R) * P::SLOT + wa + (i) * 2048; \
        _Pragma("unroll") for (int ct = 0; ct < 5; ++ct) W[ct] = ld128(b_ + ct * KSS * 1024); } while (0)
    f32x4 acc[5];
#pragma unroll
    for (int ct = 0; ct < 5; ++ct) acc[ct] = (f32x4){};
    s16x8 w[2][5];
    PJ_READ(w[0], 0, 0);
#pragma unroll
    for (int s = 0; s < NS; ++s) {
        if (NS > PJ_R && s >= 1) {
            if (s + 1 >= PJ_R && s + 1 < NS) WAIT_BAR((pj_bar_younger<NA, WIN, HK, NS, P::NWI>(s))); else LDS_BAR();
            SBAR();
            if (s + PJ_R - 1 < NS) pj_issue_w<NKS, KSS>(c, voff, s + PJ_R - 1);
        }
#pragma unroll
        for (int i = 0; i < HK; ++i) {
            const int j = s * HK + i;
            if (i + 1 < HK) PJ_READ(w[(j + 1) & 1], s, i + 1); else if (s + 1 < NS) PJ_READ(w[(j + 1) & 1], s + 1, 0);
            if (j + WIN < NA) ldg_async(a[j + WIN], PJ_ABASE(j + WIN), aoff);
            WAIT_USE((pj_younger<NA, WIN, HK, NS, P::NWI, NRES>(j)), a[j]);
            SBAR();
#pragma unroll
            for (int ct = 0; ct < 5; ++ct) acc[ct] = MFMA16(w[j & 1][ct], a[j], acc[ct]);
            SBAR();
        }
    }
#undef PJ_READ
#undef PJ_ABASE
    if (NCS) { float s = 0.f;
#pragma unroll
        for (int i = 0; i < NCS; ++i) s += __uint_as_float(csv[i]);
        if (cst < 224) *(LAS float*)(lds + P4_CS + ((cst >= 112) * 128 + cst - 112 * (cst >= 112)) * 4) = s; }
    LDS_BAR();
    LAS f32x4* red = (LAS f32x4*)(lds + XCH);
#pragma unroll
    for (int i = 0; i < 5 - NCT; ++i) red[(wid * 3 + i) * 64 + lane] = acc[(KH ? 0 : 3) + i];
    LDS_BAR();
    float s1 = 0.f, s2 = 0.f;
#pragma unroll
    for (int i = 0; i < NCT; ++i) {
        const f32x4 o = red[((wid ^ 4) * 3 + i) * 64 + lane], m = acc[CT0 + i];
        const unsigned w0 = cvtpk(bflo(res[i].x) + m[0] + o[0], bfhi(res[i].x) + m[1] + o[1]), w1 = cvtpk(bflo(res[i].y) + m[2] + o[2], bfhi(res[i].y) + m[3] + o[3]);
        if (FIRST) {
            res[i] = (u32x2){w0, w1};
            st64(c.rs, WS_H + (t16 * 80 + 10 * cc + 2 * (CT0 + i) + (gq >> 1)) * 256 + r * 16 + (gq & 1) * 8, res[i]);
            const float a_ = bflo(w0), b_ = bfhi(w0), c_ = bflo(w1), d = bfhi(w1); s1 += (a_ + b_) + (c_ + d); s2 += (a_ * a_ + b_ * b_) + (c_ * c_ + d * d);
        } else {
            *(GAS u32x2*)(c.out + (size_t)tok * 1280 + cc * 160 + (CT0 + i) * 32 + gq * 8) = (u32x2){w0, w1};
        }
    }
    if (FIRST) {
        s1 += __shfl_xor(s1, 16); s2 += __shfl_xor(s2, 16); s1 = swap32_add(s1); s2 = swap32_add(s2);
        LAS f32x2* ex = (LAS f32x2*)(lds + XCH + 2048);
        if (KH == 1 && gq == 0) ex[t16l * 16 + r] = (f32x2){s1, s2};
        LDS_BAR();
        if (KH == 0 && gq == 0) { const f32x2 e = ex[t16l * 16 + r];
            st64(c.rs, WS_HST + ((t16 * 8 + cc) * 16 + r) * 8, (u32x2){__float_as_uint(s1 + e.x), __float_as_uint(s2 + e.y)}); }
    }
}
template <int NKS, int KSS, bool FIRST>
DI void phase_proj(const Ctx& c, unsigned abase, unsigned wbase, int fc, u32x2 (&res)[3]) {
    if (c.wid < 4) pj_body<NKS, KSS, FIRST, 0>(c, abase, wbase, fc, res);
    else pj_body<NKS, KSS, FIRST, 1>(c, abase, wbase, fc, res);
}

constexpr int P4_R = 7, P4_SLOT = 22528, P4_NS = 20;

DI void p4_wvoff(const Ctx& c, int fc, unsigned& woff0, unsigned& woff1) {
    const unsigned o0 = c.wid * 1792 + c.lane * 16, o1 = c.wid * 1792 + 1024 + (c.lane < 48 ? c.lane : 47) * 16;
    woff0 = WS_W13P + (unsigned)imin(7 * fc + (o0 >> 11), 107) * 40960 + (o0 & 2047); woff1 = WS_W13P + (unsigned)imin(7 * fc + (o1 >> 11), 107) * 40960 + (o1 & 2047);
}
DI void p4_issue_w(const Ctx& c, unsigned woff0, unsigned woff1, int st) {
    const char* w_ = c.ws + st * 2048; const unsigned wdst = c.lds0 + 8192 + c.wid * 1792 + (st % P4_R) * P4_SLOT;
    dma16<true>(w_, woff0, wdst); dma16m<true>(w_, woff1, wdst + 1024, 0xffffffffffffull);
}
DI void p4_prefetch(const Ctx& c, int fc) {
    unsigned woff0, woff1; p4_wvoff(c, fc, woff0, woff1);
#pragma unroll
    for (int s = 0; s < P4_R - 1; ++s) p4_issue_w(c, woff0, woff1, s);
}
constexpr int p4_extra(int s) { int n = 0; for (int t = s - 5 < 0 ? 0 : s - 5; t < s; ++t) if (t <= 14 && t % 3 == 2) ++n; return n; }
template <int UG>
DI void p4_body(const Ctx& c, int qb, int fc) {
    constexpr int NU = UG ? 3 : 4, U0 = UG ? 4 : 0;
    const int lane = c.lane, wid = c.wid, tt = wid & 3, r = lane & 31, h = lane >> 5;
    lptr lds = c.lds;
    const unsigned adst = c.lds0 + wid * 1024;
    const unsigned aoff = WS_H + (unsigned)(8 * qb + wid) * 80 * 256 + lane * 16;
    unsigned woff0, woff1; p4_wvoff(c, fc, woff0, woff1);
#pragma unroll
    for (int s = 0; s < P4_R - 1; ++s) dma16<true>(c.ws + s * 1024, aoff, adst + s * P4_SLOT);
    const unsigned xa = ((2 * tt + (r >> 4)) * 4 + h) * 256 + (r & 15) * 16, wa = 8192 + U0 * 2048 + lane * 16;
    const unsigned t16 = 8 * qb + 2 * tt + (r >> 4);
    u32x2 hs[4];
#pragma unroll
    for (int i = 0; i < 4; ++i) hs[i] = lg64(c.rs, WS_HST + ((t16 * 8 + 4 * h + i) * 16 + (r & 15)) * 8);
#define P4_PIECE(st, k) do { const unsigned sl_ = ((st) % P4_R) * P4_SLOT; \
        if ((k) == 0) dma16<true>(c.ws + (st) * 1024, aoff, adst + sl_); \
        if ((k) == 1) dma16<true>(c.ws + (st) * 2048, woff0, c.lds0 + 8192 + wid * 1792 + sl_); \
        if ((k) == 2) dma16m<true>(c.ws + (st) * 2048, woff1, c.lds0 + 8192 + wid * 1792 + sl_ + 1024, 0xffffffffffffull); } while (0)
#define P4_READ(n) do { lcptr b_ = (lcptr)lds + (((n) >> 1) % P4_R) * P4_SLOT; X[(n) % 3] = ld128(b_ + xa + ((n) & 1) * 512); \
        _Pragma("unroll") for (int u = 0; u < NU; ++u) W[(n) % 3][u] = ld128(b_ + wa + u * 2048 + ((n) & 1) * 1024); } while (0)
    f32x16 acc[NU];
#pragma unroll
    for (int u = 0; u < NU; ++u) acc[u] = (f32x16){};
    WAIT_BAR((P4_R - 2) + 4);
    s16x8 X[3], W[3][NU];
    P4_READ(0); P4_READ(1);
#pragma unroll
    for (int s = 0; s < P4_NS; ++s) {
        WAIT_BARV(fly2(s, P4_R, P4_NS, 1, 2) + (s + 1 < P4_R - 1 ? 4 : 0) + p4_extra(s));
        SBAR();
#define P4_NEXT(k) do { if (s + P4_R - 1 < P4_NS) P4_PIECE(s + P4_R - 1, k); SBAR(); } while (0)
#pragma unroll
        for (int ks = 0; ks < 2; ++ks) {
            const int n = 2 * s + ks;
            if (n + 2 < 2 * P4_NS) P4_READ(n + 2);
            SBAR();
            acc[0] = MFMA32(W[n % 3][0], X[n % 3], acc[0]); SBAR(); P4_NEXT(ks);
            acc[1] = MFMA32(W[n % 3][1], X[n % 3], acc[1]); acc[2] = MFMA32(W[n % 3][2], X[n % 3], acc[2]); SBAR();
            if (ks) P4_NEXT(2);
            if (NU > 3) acc[NU - 1] = MFMA32(W[n % 3][NU - 1], X[n % 3], acc[NU - 1]);
            SBAR();
        }
#undef P4_NEXT
        if (s <= 14 && s % 3 == 2) {
            const int L = imin(((c.b >> 3) * 8 + wid) * 5 + s / 3, 1079);
            dma16<true>(c.ws, WS_W2P + (c.b & 1) * 1105920 + L * 1024 + lane * 16, c.lds0 + P4_R * P4_SLOT + 1024); SBAR(); }
    }
#undef P4_READ
    LDS_BAR();
    float s1 = (__uint_as_float(hs[0].x) + __uint_as_float(hs[1].x)) + (__uint_as_float(hs[2].x) + __uint_as_float(hs[3].x));
    float s2 = (__uint_as_float(hs[0].y) + __uint_as_float(hs[1].y)) + (__uint_as_float(hs[2].y) + __uint_as_float(hs[3].y));
    s1 = swap32_add(s1); s2 = swap32_add(s2);
    const float mean = s1 * (1.0f / D), var = fmaxf(s2 * (1.0f / D) - mean * mean, 0.f), rstd = __builtin_amdgcn_rsqf(var + LN_EPS);
    const f32x2 nm = {-mean, -mean}, k1 = {-1.4426950408889634f * rstd, -1.4426950408889634f * rstd}, r2 = {rstd * rstd, rstd * rstd}, one = {1.0f, 1.0f};
    const LAS float* csl = (const LAS float*)(lds + P4_CS);
#pragma unroll
    for (int u = 0; u < NU; ++u) {
        if (7 * fc + U0 + u < 108) {
            u32x2 w[2];
#pragma unroll
            for (int gq = 0; gq < 2; ++gq) {
                const f32x4 cg_ = *(const LAS f32x4*)(csl + (U0 + u) * 16 + 8 * gq + 4 * h), cu = *(const LAS f32x4*)(csl + 128 + (U0 + u) * 16 + 8 * gq + 4 * h);
                f32x2 o[2];
#pragma unroll
                for (int e = 0; e < 2; ++e) {
                    const f32x2 a = __builtin_elementwise_fma(nm, (f32x2){cg_[2 * e], cg_[2 * e + 1]}, (f32x2){acc[u][4 * gq + 2 * e], acc[u][4 * gq + 2 * e + 1]});
                    const f32x2 b2 = __builtin_elementwise_fma(nm, (f32x2){cu[2 * e], cu[2 * e + 1]}, (f32x2){acc[u][8 + 4 * gq + 2 * e], acc[u][8 + 4 * gq + 2 * e + 1]});
                    const f32x2 t = a * k1, d = (f32x2){__builtin_amdgcn_exp2f(t.x), __builtin_amdgcn_exp2f(t.y)} + one;
                    const f32x2 rc = {__builtin_amdgcn_rcpf(d.x), __builtin_amdgcn_rcpf(d.y)};
                    o[e] = (a * b2) * (rc * r2);
                }
                w[gq] = (u32x2){cvtpk(o[0].x, o[0].y), cvtpk(o[1].x, o[1].y)};
            }
            auto x0 = __builtin_amdgcn_permlane32_swap(w[0].x, w[1].x, false, false), x1 = __builtin_amdgcn_permlane32_swap(w[0].y, w[1].y, false, false);
            st128(c.rs, WS_HM + (t16 * 216 + 14 * fc + 2 * (U0 + u) + h) * 256 + (r & 15) * 16, (u32x4){x0[0], x1[0], x0[1], x1[1]});
        }
    }
}
DI void phase4(const Ctx& c, int qb, int fc) {
    if (c.wid < 4) p4_body<0>(c, qb, fc); else p4_body<1>(c, qb, fc);
}
__global__ __launch_bounds__(512, 2) void block_fwd(const char* __restrict__ x, const char* __restrict__ wqkv, const char* __restrict__ wo, const char* __restrict__ w1,
                                                    const char* __restrict__ w3, const char* __restrict__ w2, char* __restrict__ out, char* __restrict__ ws) {
    extern __shared__ __attribute__((aligned(16))) char smem[];
    Ctx c;
    { unsigned long long pc; asm volatile("s_getpc_b64 %0" : "=s"(pc)); c.code = (const char*)(pc & ~1023ull); }
    c.x = x; c.wqkv = wqkv; c.wo = wo; c.w1 = w1; c.w3 = w3; c.w2 = w2; c.out = out; c.ws = ws;
    c.rs = __builtin_amdgcn_make_buffer_rsrc(ws, 0, WS_END, 0x00020000);
    c.lds = (lptr)smem; c.lds0 = (unsigned)(uintptr_t)smem; c.b = blockIdx.x; c.lane = threadIdx.x & 63; c.wid = __builtin_amdgcn_readfirstlane(threadIdx.x >> 6);
    const int lane = c.lane, g = c.b & 7, idx = c.b >> 3;
    c.tag = *(const __attribute__((address_space(4))) unsigned*)(ws + WS_E);
    u32x2 res[3] = {(u32x2){0u, 0u}, (u32x2){0u, 0u}, (u32x2){0u, 0u}};
    const int qb4 = 8 * (g & 1) + (idx & 7), fc = 4 * (g >> 1) + (idx >> 3), pp = pj_pp(c.b);

    phase1(c);
    WAIT_VM(0);
    publish(c, FL_KV);
    repack_store(c);

    { const int hh = 4 * g + (idx & 3), qb = idx >> 2;
      const int wg = lane < 16 ? wg_p1(lane, (32 + hh) / 6) : lane < 32 ? wg_p1(lane - 16, (64 + hh) / 6) : wg_p1(2 * qb + (lane & 1), hh / 6);
      const unsigned off[1] = {flag_off(FL_KV, wg)}; wait_flags(c, off); }
    phase2(c);
    WAIT_VM(0);
    publish(c, FL_O);
    pj_prefetch<20, 4>(c, WS_WOP);

    { const unsigned off[1] = {flag_off(FL_O, wg_p2(lane & 31, pp >> 2))}; wait_flags(c, off); }
    phase_proj<20, 4, true>(c, WS_O, WS_WOP, fc, res);
    WAIT_VM(0);
    publish(c, FL_H);
    p4_prefetch(c, fc);

    { const unsigned off[1] = {flag_off(FL_H, wg_p3(2 * qb4 + (lane & 1), (lane >> 1) & 7))}; wait_flags(c, off); }
    phase4(c, qb4, fc);
    WAIT_VM(0);
    publish(c, FL_M);
    pj_prefetch<54, 6>(c, WS_W2P);

    { const unsigned fm = flag_off(FL_M, wg_p4(pp >> 1, lane & 15));
      if (c.b == 0) {
          const unsigned off[5] = {fm, flag_off(FL_O, lane), flag_off(FL_O, 64 + lane), flag_off(FL_O, 128 + lane), flag_off(FL_O, 192 + lane)}; wait_flags(c, off);
      } else { const unsigned off[1] = {fm}; wait_flags(c, off); } }
    phase_proj<54, 6, false>(c, WS_HM, WS_W2P, fc, res);
    if (c.b == 0 && threadIdx.x == 0) st32(c.rs, WS_E, c.tag);
}

}

extern "C" void kernel_launch(void* const* d_in, const int* in_sizes, int n_in, void* d_out, int out_size, void* d_ws, size_t ws_size, hipStream_t stream) {
    using namespace blk;
    const char* x = (const char*)d_in[0]; const char* wqkv = (const char*)d_in[1]; const char* wo = (const char*)d_in[2];
    const char* w1 = (const char*)d_in[4]; const char* w3 = (const char*)d_in[5]; const char* w2 = (const char*)d_in[6];
    char* ws = (char*)d_ws; char* out = (char*)d_out;
    hipFuncSetAttribute((const void*)block_fwd, hipFuncAttributeMaxDynamicSharedMemorySize, LDS_BYTES);
    block_fwd<<<256, 512, LDS_BYTES, stream>>>(x, wqkv, wo, w1, w3, w2, out, ws);
}
```

```cpp
#include <hip/hip_runtime.h>
#include <hip/hip_bf16.h>
#include <stdint.h>

namespace blk {

constexpr int S = 2048, D = 640, H = 32, F = 1728;
constexpr float LN_EPS = 1e-5f;
constexpr float QSCALE = 0.22360679774997896f * 1.4426950408889634f;

#define LAS __attribute__((address_space(3)))
#define GAS __attribute__((address_space(1)))
#define DI __device__ __forceinline__
typedef short s16x8 __attribute__((ext_vector_type(8)));
typedef short s16x4 __attribute__((ext_vector_type(4)));
typedef float f32x16 __attribute__((ext_vector_type(16)));
typedef float f32x4 __attribute__((ext_vector_type(4)));
typedef float f32x2 __attribute__((ext_vector_type(2)));
typedef unsigned u32x4 __attribute__((ext_vector_type(4)));
typedef unsigned u32x2 __attribute__((ext_vector_type(2)));
typedef __bf16 bf16x2 __attribute__((ext_vector_type(2)));
typedef LAS char* lptr;
typedef LAS const char* lcptr;
typedef __amdgpu_buffer_rsrc_t rsrc_t;

constexpr unsigned WS_E = 0;
constexpr unsigned WS_FLAG = 4096;
constexpr unsigned WS_ROWP = 256u << 10;
constexpr unsigned WS_COLP = 512u << 10;
constexpr unsigned WS_Q = 1u << 20;
constexpr unsigned WS_KV = 4u << 20;
constexpr unsigned WS_O = 12u << 20;
constexpr unsigned WS_H = 16u << 20;
constexpr unsigned WS_HST = 20u << 20;
constexpr unsigned WS_CSP = 21u << 20;
constexpr unsigned WS_HM = 24u << 20;
constexpr unsigned WS_W13P = 32u << 20;
constexpr unsigned WS_W2P = 40u << 20;
constexpr unsigned WS_WOP = 44u << 20;
constexpr unsigned WS_END = 48u << 20;
enum { FL_KV = 0, FL_PK = 1, FL_O = 2, FL_H = 3, FL_M = 4 };
constexpr int LDS_BYTES = 163840;
constexpr unsigned SPIN_CAP = 1u << 16;
constexpr unsigned CODE_BYTES = 64 * 1024;

#define SBAR() __builtin_amdgcn_sched_barrier(0)
#define PIN(x) asm volatile("" : "+v"(x))
#define MFMA32(a, b, c) __builtin_amdgcn_mfma_f32_32x32x16_bf16(a, b, c, 0, 0, 0)
#define MFMA32K8(a, b, c) __builtin_amdgcn_mfma_f32_32x32x8bf16_1k(a, b, c, 0, 0, 0)
#define MFMA16(a, b, c) __builtin_amdgcn_mfma_f32_16x16x32_bf16(a, b, c, 0, 0, 0)
#define WAIT_BAR(N) asm volatile("s_waitcnt vmcnt(%0) lgkmcnt(0)\n\ts_barrier" :: "i"(N) : "memory")
#define WAIT_BARV(N) asm volatile("s_waitcnt vmcnt(%0)\n\ts_barrier" :: "i"(N) : "memory")
#define WAIT_VM(N) asm volatile("s_waitcnt vmcnt(%0)" :: "i"(N) : "memory")
#define LDS_BAR() asm volatile("s_waitcnt lgkmcnt(0)\n\ts_barrier" ::: "memory")

template <bool SC> DI void dma16(const void* sbase, unsigned voff, unsigned ldsaddr) {
    if (SC) asm volatile("s_mov_b32 m0, %2\n\ts_nop 0\n\tglobal_load_lds_dwordx4 %0, %1 sc1" :: "v"(voff), "s"(sbase), "s"(ldsaddr) : "memory");
    else asm volatile("s_mov_b32 m0, %2\n\ts_nop 0\n\tglobal_load_lds_dwordx4 %0, %1" :: "v"(voff), "s"(sbase), "s"(ldsaddr) : "memory");
}
template <bool SC> DI void dma16m(const void* sbase, unsigned voff, unsigned ldsaddr, unsigned long long mask) {
    if (SC) asm volatile("s_mov_b32 m0, %2\n\ts_mov_b64 exec, %3\n\tglobal_load_lds_dwordx4 %0, %1 sc1\n\ts_mov_b64 exec, -1" :: "v"(voff), "s"(sbase), "s"(ldsaddr), "s"(mask) : "memory");
    else asm volatile("s_mov_b32 m0, %2\n\ts_mov_b64 exec, %3\n\tglobal_load_lds_dwordx4 %0, %1\n\ts_mov_b64 exec, -1" :: "v"(voff), "s"(sbase), "s"(ldsaddr), "s"(mask) : "memory");
}
constexpr int imin(int a, int b) { return a < b ? a : b; }
constexpr int imax(int a, int b) { return a > b ? a : b; }
constexpr int fly(int s, int R, int NS) { return imax(0, imin(R - 3, NS - 2 - s)); }
constexpr int fly2(int s, int R, int NS, int na, int nw) {
    const int need = imin(s + 1, NS - 1);
    const int pro = imin(R - 1, NS);
    const int issued = pro * na + imax(0, imin(s, NS - R + 1)) * (na + nw);
    const int endpos = need < pro ? (need + 1) * na : pro * na + (need - pro + 1) * (na + nw);
    return imax(0, issued - endpos);
}
DI int rot(int i, int rho, int ns) { const int t = i + rho; return t >= ns ? t - ns : t; }
DI unsigned cvtpk(float lo, float hi) { f32x2 v = {lo, hi}; return __builtin_bit_cast(unsigned, __builtin_convertvector(v, bf16x2)); }
DI float bflo(unsigned w) { return __uint_as_float(w << 16); }
DI float bfhi(unsigned w) { return __uint_as_float(w & 0xffff0000u); }
DI s16x8 ld128(lcptr p) { return *(const LAS s16x8*)p; }
DI s16x4 ld64(lcptr p) { return *(const LAS s16x4*)p; }
DI s16x4 ldtr(lcptr p) { return __builtin_bit_cast(s16x4, __builtin_amdgcn_ds_read_tr16_b64_v4i16((LAS s16x4*)p)); }
DI s16x8 cat(s16x4 a, s16x4 b) { return __builtin_shufflevector(a, b, 0, 1, 2, 3, 4, 5, 6, 7); }
DI f32x4 sel4(unsigned m, f32x4 a, f32x4 b) { const u32x4 x = __builtin_bit_cast(u32x4, a), y = __builtin_bit_cast(u32x4, b); return __builtin_bit_cast(f32x4, (x & m) | (y & ~m)); }
DI float swap32_add(float v) { auto r = __builtin_amdgcn_permlane32_swap(__float_as_uint(v), __float_as_uint(v), false, false); return __uint_as_float(r[0]) + __uint_as_float(r[1]); }
DI float swap32_max(float v) { auto r = __builtin_amdgcn_permlane32_swap(__float_as_uint(v), __float_as_uint(v), false, false); return fmaxf(__uint_as_float(r[0]), __uint_as_float(r[1])); }
DI float upper_half(float v) { auto r = __builtin_amdgcn_permlane32_swap(__float_as_uint(v), __float_as_uint(v), false, false); return __uint_as_float(r[1]); }
DI void st128(rsrc_t rs, unsigned off, u32x4 v) { __builtin_amdgcn_raw_buffer_store_b128(v, rs, off, 0, 16); }
DI void st64(rsrc_t rs, unsigned off, u32x2 v) { __builtin_amdgcn_raw_buffer_store_b64(v, rs, off, 0, 16); }
DI void st32(rsrc_t rs, unsigned off, unsigned v) { __builtin_amdgcn_raw_buffer_store_b32(v, rs, off, 0, 16); }
DI u32x4 lg128(rsrc_t rs, unsigned off) { return __builtin_amdgcn_raw_buffer_load_b128(rs, off, 0, 16); }
DI u32x2 lg64(rsrc_t rs, unsigned off) { return __builtin_amdgcn_raw_buffer_load_b64(rs, off, 0, 16); }
DI unsigned lg32(rsrc_t rs, unsigned off) { return __builtin_amdgcn_raw_buffer_load_b32(rs, off, 0, 16); }

DI void ldg_async(s16x8& dst, const void* sbase, unsigned voff) { asm volatile("global_load_dwordx4 %0, %1, %2 sc1" : "=v"(dst) : "v"(voff), "s"(sbase) : "memory"); }
#define WAIT_USE(N, x) asm volatile("s_waitcnt vmcnt(%1)" : "+v"(x) : "i"(N) : "memory")
struct Ctx { const char *x, *wqkv, *wo, *w1, *w3, *w2; char *out, *ws; rsrc_t rs; lptr lds; unsigned lds0, tag; int b, lane, wid; };

DI unsigned flag_off(int kind, int wg) { return WS_FLAG + (unsigned)(kind * 256 + wg) * 128u; }
DI void publish(const Ctx& c, int kind) {
    asm volatile("s_barrier" ::: "memory");
    if (threadIdx.x == 0) st32(c.rs, flag_off(kind, c.b), c.tag);
}
template <int N> DI void wait_flags(const Ctx& c, const unsigned (&off)[N]) {
    if (c.wid == 0) {
        for (unsigned spins = 0; spins < SPIN_CAP; ++spins) {
            unsigned v[N];
#pragma unroll
            for (int i = 0; i < N; ++i) v[i] = lg32(c.rs, off[i]);
            bool ok = true;
#pragma unroll
            for (int i = 0; i < N; ++i) ok &= v[i] == c.tag;
            if (__all(ok)) break;
            __builtin_amdgcn_s_sleep(1);
        }
    }
    asm volatile("s_waitcnt lgkmcnt(0)\n\ts_barrier" ::: "memory");
}
DI int wg_p1(int pb, int nb) { return ((pb >> 3) | ((nb >> 2) << 1)) + 8 * ((pb & 7) + 8 * (nb & 3)); }
DI int wg_p2(int hh, int qb) { return (hh >> 2) + 8 * ((hh & 3) + 4 * qb); }
DI int wg_p3(int pp, int cc) { return (((pp >> 3) << 1) | (cc >> 2)) + 8 * ((pp & 7) + 8 * (cc & 3)); }
DI int wg_p4(int qb, int fc) { return ((qb >> 3) | ((fc >> 2) << 1)) + 8 * ((qb & 7) + 8 * (fc & 3)); }

constexpr int P1_R = 5, P1_SLOT = 32768, P1_NS = 10;
constexpr int P1_STG = 264;
constexpr int P1_OUT = 65536;
constexpr int P1_ST = P1_OUT + 40960;
constexpr int P1_CS = P1_ST + 1024;

DI void repack_issue_one(const Ctx& c, int i);
template <int UP>
DI void p1_loop(Ctx& c, unsigned xoff, unsigned woff, int tt, int rho, unsigned put, unsigned get, f32x16& acc0, f32x16& acc1, u32x2 (&part)[10]) {
    f32x16 accx = {};
    const int lane = c.lane, wid = c.wid, r = lane & 31, h = lane >> 5, q = (lane >> 2) & 3, p = lane & 3, cg = (lane >> 4) & 1;
    constexpr unsigned long long WMASK = ~((1ull << 15) | (1ull << 27) | (1ull << 39) | (1ull << 51));
    const unsigned xdst = c.lds0 + wid * 2048, wdst = c.lds0 + 16384 + wid * 2048;
    const int xrow = 32 * tt + r;
    const unsigned xa = xrow * 128 + ((h ^ ((xrow >> 1) & 7)) * 16);
    const unsigned wk = 16384 + (8 * h + q) * 256 + (p & 1) * 8;
    const unsigned wa0 = wk + (((4 * (2 * UP) + 2 * cg + (p >> 1)) ^ (4 * q)) * 16);
    const unsigned wa1 = wk + (((4 * (2 * UP + 1) + 2 * cg + (p >> 1)) ^ (4 * q)) * 16);
    const unsigned waz = wk + (((4 * tt + 2 * cg + (p >> 1)) ^ (4 * q)) * 16);
    const s16x8 ones = {0x3f80, 0x3f80, 0x3f80, 0x3f80, 0x3f80, 0x3f80, 0x3f80, 0x3f80};
#define P1_PIECE(st, slot, k) do { const int p_ = rot(st, rho, P1_NS); \
        if ((k) == 0) dma16<false>(c.x + p_ * 128, xoff, xdst + (slot)); \
        if ((k) == 1) dma16<false>(c.x + p_ * 128 + 8 * 1280, xoff ^ 64, xdst + (slot) + 1024); \
        if ((k) == 2) dma16m<false>(c.wqkv + (size_t)p_ * (64 * 3840), woff, wdst + (slot), WMASK); \
        if ((k) == 3) dma16m<false>(c.wqkv + (size_t)p_ * (64 * 3840) + 4 * 3840, woff, wdst + (slot) + 1024, WMASK); } while (0)
#define P1_ISSUE(st, slot) do { P1_PIECE(st, slot, 0); P1_PIECE(st, slot, 1); P1_PIECE(st, slot, 2); P1_PIECE(st, slot, 3); } while (0)
#define P1_READ(n) do { lcptr b_ = (lcptr)c.lds + (((n) >> 2) % P1_R) * P1_SLOT; \
        X[(n) % 3] = ld128(b_ + (xa ^ (((n) & 3) * 32))); \
        W0[(n) % 3] = cat(ldtr(b_ + wa0 + ((n) & 3) * 4096), ldtr(b_ + wa0 + ((n) & 3) * 4096 + 1024)); \
        W1[(n) % 3] = cat(ldtr(b_ + wa1 + ((n) & 3) * 4096), ldtr(b_ + wa1 + ((n) & 3) * 4096 + 1024)); \
        if (UP && (n) < 4) WZ[(n) & 3] = cat(ldtr(b_ + waz + ((n) & 3) * 4096), ldtr(b_ + waz + ((n) & 3) * 4096 + 1024)); } while (0)
#define P1_MMA(n) do { acc0 = MFMA32(W0[(n) % 3], X[(n) % 3], acc0); acc1 = MFMA32(W1[(n) % 3], X[(n) % 3], acc1); \
        if ((n) < 4) { if (UP) accx = MFMA32(WZ[(n) & 3], ones, accx); else accx = MFMA32(X[(n) % 3], X[(n) % 3], accx); } } while (0)
    P1_ISSUE(0, 0); P1_ISSUE(1, P1_SLOT); P1_ISSUE(2, 2 * P1_SLOT);
    WAIT_BAR(8);
    asm volatile("" : "+s"(c.tag));
    c.tag += 1u;
    P1_ISSUE(3, 3 * P1_SLOT);
    s16x8 X[3] = {}, W0[3] = {}, W1[3] = {}, WZ[4] = {};
    P1_READ(0); P1_READ(1);
#pragma unroll
    for (int s = 0; s < P1_NS; ++s) {
        constexpr int NX = P1_R - 1, GS = P1_NS - P1_R;
        WAIT_BARV(4 * fly(s, P1_R, P1_NS) + (s >= 1 && s + 1 <= P1_R - 1 ? 1 : 0) + (s > GS && !UP ? 10 : 0) + imax(0, s - GS - 1));
        SBAR();
#pragma unroll
        for (int ks = 0; ks < 4; ++ks) {
            const int n = 4 * s + ks;
            if (n + 2 < 4 * P1_NS) P1_READ(n + 2);
            SBAR(); P1_MMA(n); SBAR();
            if (s + NX < P1_NS) P1_PIECE(s + NX, ((s + NX) % P1_R) * P1_SLOT, ks);
            if (s > GS && ks == 1) repack_issue_one(c, s - GS - 1);
            SBAR();
        }
        if (s == 0) {
            const int tr = (r & 3) + 4 * (r >> 3); float v = 0.f;
#pragma unroll
            for (int i = 0; i < 16; ++i) v = (tr == i) ? accx[i] : v;
            st64(c.rs, put + (h == ((r >> 2) & 1) ? (tt * 32 + r) * 8 : 0x7fffff00u), (u32x2){__float_as_uint(v), c.tag});
            SBAR();
        }
        if (s == GS && !UP) {
#pragma unroll
            for (int i = 0; i < 10; ++i) part[i] = lg64(c.rs, get + i * 1024);
            SBAR();
        }
    }
#undef P1_PIECE
#undef P1_ISSUE
#undef P1_READ
#undef P1_MMA
}

DI void phase1(Ctx& c) {
    const int lane = c.lane, wid = c.wid, tid = threadIdx.x, g = c.b & 7, idx = c.b >> 3;
    const int pb = 8 * (g & 1) + (idx & 7), nb = 4 * (g >> 1) + (idx >> 3);
    const int tt = wid & 3, up = wid >> 2, r = lane & 31, h = lane >> 5;
    lptr lds = c.lds;
    if (tid < P1_R * 64) { const int sl = tid >> 6, k = tid & 63;
        *(LAS u32x4*)(lds + sl * P1_SLOT + 16384 + k * 256 + ((15 ^ (4 * (k & 3))) * 16)) = (u32x4){0x3f803f80u, 0x3f803f80u, 0x3f803f80u, 0x3f803f80u}; }
    const int xr = 16 * wid + (lane >> 3), xc = (lane & 7) ^ ((xr >> 1) & 7);
    const unsigned xoff = (pb * 128 + xr) * 1280 + xc * 16;
    const int wkk = 8 * wid + (lane >> 4), wp = (lane & 15) ^ (4 * (wkk & 3)), wpc = wp < 15 ? wp : 14;
    const unsigned woff = wkk * 3840 + nb * 240 + wpc * 16;
    f32x16 acc0 = {}, acc1 = {};
    const int rho = (pb + 3 * nb) % P1_NS;
    const unsigned put = up ? WS_COLP + (unsigned)(nb * 16 + pb) * 1024 : WS_ROWP + (unsigned)(pb * 16 + nb) * 1024;
    const int gi = (wid & 1) * 64 + lane;
    const unsigned get = ((wid & 2) ? WS_COLP + (unsigned)nb * 16384 : WS_ROWP + (unsigned)pb * 16384) + gi * 8;
    u32x2 part[10];
    if (up == 0) p1_loop<0>(c, xoff, woff, tt, rho, put, get, acc0, acc1, part);
    else p1_loop<1>(c, xoff, woff, tt, rho, put, get, acc0, acc1, part);
    float psum = 0.f;
    if (up == 0) {
        for (unsigned spins = 0; spins < SPIN_CAP; ++spins) {
            bool ok = true;
#pragma unroll
            for (int i = 0; i < 10; ++i) ok &= part[i].y == c.tag;
            if (__all(ok)) break;
            __builtin_amdgcn_s_sleep(1);
#pragma unroll
            for (int i = 0; i < 10; ++i) part[i] = lg64(c.rs, get + i * 1024);
        }
#pragma unroll
        for (int i = 0; i < 10; ++i) psum += __uint_as_float(part[i].x);
    }
    LDS_BAR();
    LAS float* st = (LAS float*)(lds + P1_ST); LAS float* cs = (LAS float*)(lds + P1_CS);
    if (wid < 2) st[gi * 2 + 1] = psum; else if (wid < 4) cs[gi] = psum;
    if (up == 1 && h == 0) st[(tt * 32 + r) * 2] = acc1[12];
    LDS_BAR();
    { const f32x2 sv = *(const LAS f32x2*)(st + (tt * 32 + r) * 2);
      const float mean = sv.x * (1.0f / D), var = fmaxf(sv.y * (1.0f / D) - mean * mean, 0.f), rstd = __builtin_amdgcn_rsqf(var + LN_EPS), rm = rstd * mean;
      lptr stg = lds + P1_OUT + (32 * tt + r) * P1_STG;
#pragma unroll
      for (int u = 0; u < 2; ++u)
#pragma unroll
        for (int gq = 0; gq < 4; ++gq) {
            const int col = (2 * up + u) * 32 + 8 * gq + 4 * h;
            const f32x4 c4 = *(const LAS f32x4*)(cs + col);
            const float sc = (nb * 120 + col < D) ? QSCALE : 1.0f, a = rstd * sc, bb = rm * sc;
            float v[4];
#pragma unroll
            for (int e = 0; e < 4; ++e) v[e] = a * (u ? acc1[4 * gq + e] : acc0[4 * gq + e]) - bb * c4[e];
            *(LAS u32x2*)(stg + col * 2) = (u32x2){cvtpk(v[0], v[1]), cvtpk(v[2], v[3])};
        }
    }
    LDS_BAR();
#pragma unroll
    for (int j = 0; j < 3; ++j) {
        const int slot = up + 2 * j, gidx = 6 * nb + slot, type = gidx >> 5, head = gidx & 31, tile = 4 * pb + tt;
        if (type < 2) {
            lcptr row = (lcptr)lds + P1_OUT + (32 * tt + r) * P1_STG + slot * 40;
            const u32x2 a0 = *(const LAS u32x2*)(row + 16 * h), a1 = *(const LAS u32x2*)(row + 16 * h + 8), a2 = *(const LAS u32x2*)(row + 32);
            const unsigned dst = type ? WS_KV + (unsigned)(head * 64 + tile) * 3072 : WS_Q + (unsigned)(head * 64 + tile) * 1536;
            st128(c.rs, dst + lane * 16, (u32x4){a0.x, a0.y, a1.x, a1.y});
            st64(c.rs, dst + 1024 + lane * 8, h ? (u32x2){type ? 0x00003f80u : 0u, 0u} : a2);
        } else {
            const int q = (lane >> 2) & 3, p = lane & 3, cg = (lane >> 4) & 1;
            lcptr vb = (lcptr)lds + P1_OUT + (32 * tt + 4 * h + q) * P1_STG + slot * 40 + (cg * 4 + p < 5 ? (cg * 4 + p) * 8 : 0);
            const unsigned dst = WS_KV + (unsigned)(head * 64 + tile) * 3072 + 1536;
#pragma unroll
            for (int s = 0; s < 2; ++s) {
                const u32x2 a = __builtin_bit_cast(u32x2, ldtr(vb + (16 * s) * P1_STG)), e = __builtin_bit_cast(u32x2, ldtr(vb + (16 * s + 8) * P1_STG));
                u32x4 v = {a.x, a.y, e.x, e.y};
                if (r == 20) v = (u32x4){0x3f803f80u, 0x3f803f80u, 0x3f803f80u, 0x3f803f80u};
                if (r > 20) v = (u32x4){0u, 0u, 0u, 0u};
                if (r < 24) st128(c.rs, dst + s * 768 + h * 384 + r * 16, v);
            }
        }
    }
}

constexpr int PK_OFF = 0;
DI int pk_job(int b, int i) { const int j = b + 256 * i; return j < 910 ? j : j - 256; }
DI void repack_issue_one(const Ctx& c, int i) {
    const int row = 8 * c.wid + (c.lane >> 3), pc = c.lane & 7;
    const int j = pk_job(c.b, i);
    const char* src; unsigned off;
    if (j < 540) { const int jj = j < 270 ? j : j - 270, kb = jj / 27, cb = jj % 27; src = j < 270 ? c.w1 : c.w3; off = (64 * kb + row) * 3456 + cb * 128 + pc * 16; }
    else if (j < 810) { const int jj = j - 540, kb = jj / 10, cb = jj % 10; src = c.w2; off = (64 * kb + row) * 1280 + cb * 128 + pc * 16; }
    else { const int jj = j - 810, kb = jj / 10, cb = jj % 10, kp = 64 * kb + row, G = kp >> 3, e = kp & 7;
        const int k = G < 64 ? 20 * (G >> 1) + 8 * (G & 1) + e : 40 * (G - 64) + 16 + (e < 4 ? e : e + 16);
        src = c.wo; off = k * 1280 + cb * 128 + pc * 16; }
    dma16<false>(src, off, c.lds0 + PK_OFF + i * 8192 + c.wid * 1024);
}
DI void repack_store(const Ctx& c) {
    const int lane = c.lane, w = c.wid, g = lane >> 4, i16 = lane & 15, q = i16 >> 2, p = i16 & 3;
#pragma unroll
    for (int i = 0; i < 4; ++i) {
        const int j = pk_job(c.b, i);
        lcptr blk = (lcptr)c.lds + PK_OFF + i * 8192 + (w >> 1) * 32 + p * 8;
        if (j < 540) {
            const int mat = j >= 270, jj = mat ? j - 270 : j, kb = jj / 27, cb = jj % 27, ks = 2 * (w & 1) + (g >> 1), hh = g & 1;
            lcptr a = blk + (16 * ks + 8 * hh + q) * 128;
            const u32x2 v0 = __builtin_bit_cast(u32x2, ldtr(a)), v1 = __builtin_bit_cast(u32x2, ldtr(a + 512));
            st128(c.rs, WS_W13P + (unsigned)((4 * cb + (w >> 1)) * 40 + 4 * kb + ks) * 1024 + (mat * 16 + i16 + 32 * hh) * 16, (u32x4){v0.x, v0.y, v1.x, v1.y});
            float s = ((bflo(v0.x) + bfhi(v0.x)) + (bflo(v0.y) + bfhi(v0.y))) + ((bflo(v1.x) + bfhi(v1.x)) + (bflo(v1.y) + bfhi(v1.y)));
            s += __shfl_xor(s, 16); s += __shfl_xor(s, 32);
            if (lane < 16) st32(c.rs, WS_CSP + (unsigned)((mat * 20 + 2 * kb + (w & 1)) * F + 64 * cb + 16 * (w >> 1) + i16) * 4, __float_as_uint(s));
        } else {
            const bool isw2 = j < 810; const int jj = isw2 ? j - 540 : j - 810, kb = jj / 10, cb = jj % 10, ks = w & 1;
            lcptr a = blk + (32 * ks + 8 * g + q) * 128;
            const u32x2 v0 = __builtin_bit_cast(u32x2, ldtr(a)), v1 = __builtin_bit_cast(u32x2, ldtr(a + 512));
            const unsigned dst = isw2 ? WS_W2P + (unsigned)((4 * cb + (w >> 1)) * 54 + 2 * kb + ks) * 1024 : WS_WOP + (unsigned)((4 * cb + (w >> 1)) * 20 + 2 * kb + ks) * 1024;
            st128(c.rs, dst + lane * 16, (u32x4){v0.x, v0.y, v1.x, v1.y});
        }
    }
}

constexpr int P2_R = 4, P2_SLOT = 24576, P2_NS = 8;
constexpr int P2_Q = 110592;
constexpr int XCH = 135168;
constexpr int P2_EX = XCH;
constexpr int P4_CS = XCH + 24576;

DI void attn_safe(const Ctx& c, unsigned kv, s16x8 q0, s16x4 q1z, f32x16& o) {
    const int lane = c.lane, r = lane & 31, h = lane >> 5, vr = r < 24 ? r : r - 16;
    float m = -INFINITY; o = (f32x16){};
    for (int t = 0; t < 64; ++t) {
        const unsigned p = kv + t * 3072;
        const u32x4 k0 = lg128(c.rs, p + lane * 16); const u32x2 k1 = lg64(c.rs, p + 1024 + lane * 8);
        const u32x4 v0 = lg128(c.rs, p + 1536 + h * 384 + vr * 16), v1 = lg128(c.rs, p + 1536 + 768 + h * 384 + vr * 16);
        f32x16 s = MFMA32(__builtin_bit_cast(s16x8, k0), q0, (f32x16){}); s = MFMA32K8(__builtin_bit_cast(s16x4, k1), q1z, s);
        float tm = s[0];
#pragma unroll
        for (int i = 1; i < 16; ++i) tm = fmaxf(tm, s[i]);
        tm = swap32_max(tm);
        const float mn = fmaxf(m, tm), al = __builtin_amdgcn_exp2f(m - mn); m = mn;
#pragma unroll
        for (int i = 0; i < 16; ++i) { o[i] *= al; s[i] = __builtin_amdgcn_exp2f(s[i] - mn); }
        const u32x4 p0 = {cvtpk(s[0], s[1]), cvtpk(s[2], s[3]), cvtpk(s[4], s[5]), cvtpk(s[6], s[7])};
        const u32x4 p1 = {cvtpk(s[8], s[9]), cvtpk(s[10], s[11]), cvtpk(s[12], s[13]), cvtpk(s[14], s[15])};
        o = MFMA32(__builtin_bit_cast(s16x8, v0), __builtin_bit_cast(s16x8, p0), o); o = MFMA32(__builtin_bit_cast(s16x8, v1), __builtin_bit_cast(s16x8, p1), o);
    }
}

DI void phase2(const Ctx& c) {
    const int lane = c.lane, wid = c.wid, g = c.b & 7, idx = c.b >> 3, hh = 4 * g + (idx & 3), qb = idx >> 2;
    const int qg = wid & 3, kh = wid >> 2, r = lane & 31, h = lane >> 5;
    lptr lds = c.lds;
    const char* kvsrc = c.ws + WS_KV + (size_t)hh * 64 * 3072 + wid * 3072;
    const unsigned dst0 = c.lds0 + wid * 3072, voff = lane * 16;
#define P2_ISSUE(st, slot) do { const char* s_ = kvsrc + (size_t)(st) * 24576; dma16<true>(s_, voff, dst0 + (slot)); dma16<true>(s_ + 1024, voff, dst0 + (slot) + 1024); dma16<true>(s_ + 2048, voff, dst0 + (slot) + 2048); } while (0)
    { const char* qsrc = c.ws + WS_Q + ((size_t)hh * 64 + 8 * qb + wid) * 1536;
      dma16<true>(qsrc, voff, c.lds0 + P2_Q + wid * 1536); dma16m<true>(qsrc + 1024, voff, c.lds0 + P2_Q + wid * 1536 + 1024, 0xffffffffull); }
    P2_ISSUE(0, 0); P2_ISSUE(1, P2_SLOT);
    const int vr = r < 24 ? r : 21 + (r & 1);
    const unsigned ka = 4 * kh * 3072 + lane * 16, kb = 4 * kh * 3072 + 1024 + lane * 8, va = 4 * kh * 3072 + 1536 + h * 384 + vr * 16;
#define LDK0(slot, t) ld128((lcptr)lds + (slot) + ka + (t) * 3072)
#define LDK1(slot, t) ld64((lcptr)lds + (slot) + kb + (t) * 3072)
#define LDV0(slot, t) ld128((lcptr)lds + (slot) + va + (t) * 3072)
#define LDV1(slot, t) ld128((lcptr)lds + (slot) + va + (t) * 3072 + 768)
    WAIT_BAR(3);
    if (threadIdx.x == 0) st32(c.rs, flag_off(FL_PK, c.b), c.tag);
    P2_ISSUE(2, 2 * P2_SLOT);
    s16x8 q0[2]; s16x4 q1z[2], q1[2];
#pragma unroll
    for (int i = 0; i < 2; ++i) { q0[i] = ld128((lcptr)lds + P2_Q + (2 * qg + i) * 1536 + lane * 16); q1z[i] = ld64((lcptr)lds + P2_Q + (2 * qg + i) * 1536 + 1024 + lane * 8); }
    { const s16x8 k0 = ld128((lcptr)lds + lane * 16); const s16x4 k1 = ld64((lcptr)lds + 1024 + lane * 8);
#pragma unroll
      for (int i = 0; i < 2; ++i) { f32x16 s = MFMA32(k0, q0[i], (f32x16){}); s = MFMA32K8(k1, q1z[i], s);
          float m = s[0];
#pragma unroll
          for (int e = 1; e < 16; ++e) m = fmaxf(m, s[e]);
          m = swap32_max(m);
          q1[i] = q1z[i]; if (h) q1[i][0] = (short)(cvtpk(-m, 0.f) & 0xffffu); } }
    s16x8 kA0 = LDK0(0, 0), kB0, vA0 = LDV0(0, 0), vA1 = LDV1(0, 0), vB0, vB1 = vA1; s16x4 kA1 = LDK1(0, 0), kB1;
    f32x16 sA, sB, o[2] = {(f32x16){}, (f32x16){}};
    sA = MFMA32(kA0, q0[0], (f32x16){}); sA = MFMA32K8(kA1, q1[0], sA);
    u32x4 p0, p1 = {0u, 0u, 0u, 0u};
    int cur = 0, nxt = P2_SLOT, fre = (P2_R - 1) * P2_SLOT;
#define EX(v) __builtin_amdgcn_exp2f(v)
#define P2_STEP(C, N, K0, K1, NQ, OC, V0, OP, VP, LOADS, LAST) do { \
        OP = MFMA32(VP, __builtin_bit_cast(s16x8, p1), OP); SBAR(); \
        LOADS; SBAR(); \
        C[0] = EX(C[0]); C[1] = EX(C[1]); C[2] = EX(C[2]); C[3] = EX(C[3]); p0[0] = cvtpk(C[0], C[1]); p0[1] = cvtpk(C[2], C[3]); PIN(p0); SBAR(); \
        if (!(LAST)) N = MFMA32(K0, q0[NQ], (f32x16){}); SBAR(); \
        C[4] = EX(C[4]); C[5] = EX(C[5]); C[6] = EX(C[6]); C[7] = EX(C[7]); p0[2] = cvtpk(C[4], C[5]); p0[3] = cvtpk(C[6], C[7]); PIN(p0); SBAR(); \
        if (!(LAST)) N = MFMA32K8(K1, q1[NQ], N); SBAR(); \
        C[8] = EX(C[8]); C[9] = EX(C[9]); C[10] = EX(C[10]); C[11] = EX(C[11]); p1[0] = cvtpk(C[8], C[9]); p1[1] = cvtpk(C[10], C[11]); PIN(p1); SBAR(); \
        OC = MFMA32(V0, __builtin_bit_cast(s16x8, p0), OC); SBAR(); \
        C[12] = EX(C[12]); C[13] = EX(C[13]); C[14] = EX(C[14]); C[15] = EX(C[15]); p1[2] = cvtpk(C[12], C[13]); p1[3] = cvtpk(C[14], C[15]); PIN(p1); SBAR(); \
    } while (0)
#define P2_PIECE(st, k) do { if ((st) >= 0) dma16<true>(kvsrc + (size_t)(st) * 24576 + (k) * 1024, voff, dst0 + fre + (k) * 1024); } while (0)
#define P2_PAIR(T, NS_, NT, LAST, ST, KA, KB) do { \
        if (((T) == 0) == (kh != 0)) __builtin_amdgcn_s_setprio(1); else __builtin_amdgcn_s_setprio(0); \
        P2_STEP(sA, sB, kA0, kA1, 1, o[0], vA0, o[1], vB1, { kB0 = LDK0(cur, (T) + 1); kB1 = LDK1(cur, (T) + 1); vB0 = LDV0(cur, (T) + 1); vB1 = LDV1(cur, (T) + 1); }, false); \
        P2_STEP(sB, sA, kB0, kB1, 0, o[1], vA0, o[0], vA1, { P2_PIECE(ST, KA); }, false); \
        P2_STEP(sA, sB, kB0, kB1, 1, o[0], vB0, o[1], vA1, { if (!(LAST)) { kA0 = LDK0(NS_, NT); kA1 = LDK1(NS_, NT); vA0 = LDV0(NS_, NT); vA1 = LDV1(NS_, NT); } }, false); \
        P2_STEP(sB, sA, kA0, kA1, 0, o[1], vB0, o[0], vB1, { if ((KB) >= 0) P2_PIECE(ST, KB); }, LAST); } while (0)
#define P2_STAGE(LAST, ST) do { P2_PAIR(0, cur, 2, false, ST, 0, 1); P2_PAIR(2, nxt, 0, LAST, ST, 2, -1); \
        fre = cur; cur = nxt; nxt = (nxt == (P2_R - 1) * P2_SLOT) ? 0 : nxt + P2_SLOT; } while (0)
#pragma unroll
    for (int s = 0; s < P2_NS - 3; ++s) { WAIT_BAR(3); P2_STAGE(false, s + P2_R - 1); }
    WAIT_BAR(3);
    unsigned pk[4];
    if (wid == 0) {
#pragma unroll
        for (int i = 0; i < 4; ++i) pk[i] = lg32(c.rs, flag_off(FL_PK, 64 * i + lane));
    }
    P2_STAGE(false, -1);
    if (wid == 0) {
        for (unsigned spins = 0; spins < SPIN_CAP; ++spins) {
            if (__all(pk[0] == c.tag && pk[1] == c.tag && pk[2] == c.tag && pk[3] == c.tag)) break;
            __builtin_amdgcn_s_sleep(1);
#pragma unroll
            for (int i = 0; i < 4; ++i) pk[i] = lg32(c.rs, flag_off(FL_PK, 64 * i + lane));
        }
    }
    WAIT_BAR(0);
    const unsigned wsc = c.lds0 + P2_Q + 12288 + wid * 1024;
    unsigned wu[6];
#pragma unroll
    for (int i = 0; i < 6; ++i) { const int L = imin((idx * 8 + wid) * 6 + i, 1519);
        wu[i] = (L < 400 ? WS_WOP + (g & 1) * 409600 + L * 1024 : WS_W13P + imin((g >> 1) * 1146880 + (L - 400) * 1024, 108 * 40960 - 1024)) + lane * 16; }
#define P2_WARM(i) dma16<true>(c.ws, wu[i], wsc)
    P2_PAIR(0, cur, 2, false, -1, 0, 1); P2_WARM(0); P2_WARM(1); P2_PAIR(2, nxt, 0, false, -1, 2, -1); P2_WARM(2);
    fre = cur; cur = nxt; nxt = (nxt == (P2_R - 1) * P2_SLOT) ? 0 : nxt + P2_SLOT;
    P2_WARM(3); P2_PAIR(0, cur, 2, false, -1, 0, 1); P2_WARM(4); P2_WARM(5); P2_PAIR(2, nxt, 0, true, -1, 2, -1);
#undef P2_WARM
    o[1] = MFMA32(vB1, __builtin_bit_cast(s16x8, p1), o[1]);
#undef P2_STEP
#undef P2_PAIR
#undef P2_PIECE
#undef P2_STAGE
#undef P2_ISSUE
    __builtin_amdgcn_s_setprio(0);
    LAS f32x4* ex = (LAS f32x4*)(lds + P2_EX);
    { const unsigned m = kh ? 0xffffffffu : 0u;
#pragma unroll
      for (int i = 0; i < 3; ++i) { const f32x4 a = {o[0][4 * i], o[0][4 * i + 1], o[0][4 * i + 2], o[0][4 * i + 3]}, e = {o[1][4 * i], o[1][4 * i + 1], o[1][4 * i + 2], o[1][4 * i + 3]};
          ex[(wid * 3 + i) * 64 + lane] = sel4(m, a, e); }
      LDS_BAR();
      f32x16 of = {};
#pragma unroll
      for (int i = 0; i < 3; ++i) { const f32x4 a = {o[0][4 * i], o[0][4 * i + 1], o[0][4 * i + 2], o[0][4 * i + 3]}, e = {o[1][4 * i], o[1][4 * i + 1], o[1][4 * i + 2], o[1][4 * i + 3]};
          const f32x4 t = sel4(m, e, a) + ex[((wid ^ 4) * 3 + i) * 64 + lane];
          of[4 * i] = t[0]; of[4 * i + 1] = t[1]; of[4 * i + 2] = t[2]; of[4 * i + 3] = t[3]; }
      float l = upper_half(of[8]);
      bool bad = !(l < 1.2676506e30f && l > 8.6736174e-19f);
#pragma unroll
      for (int i = 0; i < 12; ++i) bad |= !(fabsf(of[i]) < 3.0e38f);
      if (__any(bad)) {
          const s16x8 qq0 = ld128((lcptr)lds + P2_Q + (2 * qg + kh) * 1536 + lane * 16); const s16x4 qq1 = ld64((lcptr)lds + P2_Q + (2 * qg + kh) * 1536 + 1024 + lane * 8);
          attn_safe(c, WS_KV + (unsigned)hh * 64 * 3072, qq0, qq1, of); l = upper_half(of[8]); }
      const float il = __builtin_amdgcn_rcpf(l);
      const unsigned a0 = cvtpk(of[0] * il, of[1] * il), a1 = cvtpk(of[2] * il, of[3] * il), b0 = cvtpk(of[4] * il, of[5] * il), b1 = cvtpk(of[6] * il, of[7] * il);
      auto x0 = __builtin_amdgcn_permlane32_swap(a0, b0, false, false), x1 = __builtin_amdgcn_permlane32_swap(a1, b1, false, false);
      const unsigned t16 = 2 * (8 * qb + 2 * qg + kh) + (r >> 4);
      st128(c.rs, WS_O + (t16 * 80 + 2 * hh + h) * 256 + (r & 15) * 16, (u32x4){x0[0], x1[0], x0[1], x1[1]});
      if (h == 0) st64(c.rs, WS_O + (t16 * 80 + 64 + (hh >> 1)) * 256 + (r & 15) * 16 + (hh & 1) * 8, (u32x2){cvtpk(of[8] * il, of[9] * il), cvtpk(of[10] * il, of[11] * il)});
    }
#undef LDK0
#undef LDK1
#undef LDV0
#undef LDV1
#undef EX
}

constexpr int PJ_R = 5;
template <int NKS, int KSS> struct PJ {
    static constexpr int NS = NKS / KSS, SLOT = 5 * KSS * 1024, NWI = (5 * KSS + 7) / 8, NA = NKS / 2, PRE = imin(PJ_R, NS);
};
template <int NKS, int KSS> DI void pj_issue_w(const Ctx& c, const unsigned (&voff)[(5 * KSS + 7) / 8], int st) {
    using P = PJ<NKS, KSS>;
    const char* base = c.ws + st * (KSS * 1024);
#pragma unroll
    for (int i = 0; i < P::NWI; ++i) dma16<true>(base, voff[i], c.lds0 + (st % PJ_R) * P::SLOT + imin(c.wid + 8 * i, 5 * KSS - 1) * 1024);
}
template <int NKS, int KSS> DI void pj_voff(const Ctx& c, unsigned wbase, int cc, unsigned (&voff)[(5 * KSS + 7) / 8]) {
#pragma unroll
    for (int i = 0; i < PJ<NKS, KSS>::NWI; ++i) { const int f = imin(c.wid + 8 * i, 5 * KSS - 1); voff[i] = wbase + (unsigned)((5 * cc + f / KSS) * NKS + f % KSS) * 1024 + c.lane * 16; }
}
DI int pj_pp(int b) { return 8 * ((b & 7) >> 1) + ((b >> 3) & 7); }
DI int pj_cc(int b) { return 4 * (b & 1) + (b >> 6); }
template <int NKS, int KSS> DI void pj_prefetch(const Ctx& c, unsigned wbase) {
    unsigned voff[PJ<NKS, KSS>::NWI]; pj_voff<NKS, KSS>(c, wbase, pj_cc(c.b), voff);
#pragma unroll
    for (int s = 0; s < PJ<NKS, KSS>::PRE; ++s) pj_issue_w<NKS, KSS>(c, voff, s);
}

template <int NA, int WIN, int HK, int NS, int NWI, int NRES> constexpr int pj_younger(int j) {
    int n = imin(NA - 1, j + WIN) - j;
    if (j < WIN) n += NRES;
    for (int s = 1; s + PJ_R - 1 < NS; ++s)
        if (HK * s <= j && (j < WIN || HK * s > j - WIN)) n += NWI;
    return n;
}
template <int NA, int WIN, int HK, int NS, int NWI> constexpr int pj_bar_younger(int s) {
    const int s0 = s + 2 - PJ_R;
    int n = 0;
    for (int t = s0 + 1; t < s; ++t) if (t + PJ_R - 1 < NS) n += NWI;
    for (int j = HK * s0; j < HK * s; ++j) if (j + WIN < NA) ++n;
    return n;
}
template <int NKS, int KSS, bool FIRST, int KH>
DI void pj_body(const Ctx& c, unsigned abase, unsigned wbase, int fc, u32x2 (&res)[3]) {
    using P = PJ<NKS, KSS>;
    constexpr int NS = P::NS, NG = NKS * 4, NA = P::NA, HK = KSS / 2, NCT = KH ? 2 : 3, CT0 = KH ? 3 : 0;
    const int lane = c.lane, wid = c.wid, t16l = wid & 3, r = lane & 15, gq = lane >> 4, pp = pj_pp(c.b), cc = pj_cc(c.b);
    lptr lds = c.lds;
    const unsigned t16 = 4 * pp + t16l;
    const int tok = 16 * t16 + r;
    constexpr int WIN = imin(NA, FIRST ? 4 : 9);
    s16x8 a[NA];
    const unsigned aoff = abase + (t16 * NG + 4 * KH) * 256 + lane * 16;
#define PJ_ABASE(j) (c.ws + (j) * 2048)
#pragma unroll
    for (int j = 0; j < WIN; ++j) ldg_async(a[j], PJ_ABASE(j), aoff);
    if (FIRST) {
#pragma unroll
        for (int i = 0; i < NCT; ++i) res[i] = *(const GAS u32x2*)(c.x + (size_t)tok * 1280 + cc * 160 + (CT0 + i) * 32 + gq * 8);
    }
    constexpr int NCS = FIRST && KH ? 20 : 0;
    unsigned csv[NCS ? NCS : 1];
    const int cst = wid * 64 + lane - 256;
    if (NCS) { const int mat = cst >= 112, col = imin(fc * 112 + imin(cst, 223) - 112 * mat, F - 1);
#pragma unroll
        for (int i = 0; i < NCS; ++i) csv[i] = lg32(c.rs, WS_CSP + (unsigned)((mat * 20 + i) * F + col) * 4); }
    unsigned voff[P::NWI]; pj_voff<NKS, KSS>(c, wbase, cc, voff);
    constexpr int NRES = (FIRST ? NCT : 0) + NCS;
    WAIT_BARV(WIN + NRES);
    const unsigned wa = KH * 1024 + lane * 16;
#define PJ_READ(W, st, i) do { lcptr b_ = (lcptr)lds + ((st) % PJ_R) * P::SLOT + wa + (i) * 2048; \
        _Pragma("unroll") for (int ct = 0; ct < 5; ++ct) W[ct] = ld128(b_ + ct * KSS * 1024); } while (0)
    f32x4 acc[5];
#pragma unroll
    for (int ct = 0; ct < 5; ++ct) acc[ct] = (f32x4){};
    s16x8 w[2][5];
    PJ_READ(w[0], 0, 0);
#pragma unroll
    for (int s = 0; s < NS; ++s) {
        if (NS > PJ_R && s >= 1) {
            if (s + 1 >= PJ_R && s + 1 < NS) WAIT_BAR((pj_bar_younger<NA, WIN, HK, NS, P::NWI>(s))); else LDS_BAR();
            SBAR();
            if (s + PJ_R - 1 < NS) pj_issue_w<NKS, KSS>(c, voff, s + PJ_R - 1);
        }
#pragma unroll
        for (int i = 0; i < HK; ++i) {
            const int j = s * HK + i;
            if (i + 1 < HK) PJ_READ(w[(j + 1) & 1], s, i + 1); else if (s + 1 < NS) PJ_READ(w[(j + 1) & 1], s + 1, 0);
            if (j + WIN < NA) ldg_async(a[j + WIN], PJ_ABASE(j + WIN), aoff);
            WAIT_USE((pj_younger<NA, WIN, HK, NS, P::NWI, NRES>(j)), a[j]);
            SBAR();
#pragma unroll
            for (int ct = 0; ct < 5; ++ct) acc[ct] = MFMA16(w[j & 1][ct], a[j], acc[ct]);
            SBAR();
        }
    }
#undef PJ_READ
#undef PJ_ABASE
    if (NCS) { float s = 0.f;
#pragma unroll
        for (int i = 0; i < NCS; ++i) s += __uint_as_float(csv[i]);
        if (cst < 224) *(LAS float*)(lds + P4_CS + ((cst >= 112) * 128 + cst - 112 * (cst >= 112)) * 4) = s; }
    LDS_BAR();
    LAS f32x4* red = (LAS f32x4*)(lds + XCH);
#pragma unroll
    for (int i = 0; i < 5 - NCT; ++i) red[(wid * 3 + i) * 64 + lane] = acc[(KH ? 0 : 3) + i];
    LDS_BAR();
    float s1 = 0.f, s2 = 0.f;
#pragma unroll
    for (int i = 0; i < NCT; ++i) {
        const f32x4 o = red[((wid ^ 4) * 3 + i) * 64 + lane], m = acc[CT0 + i];
        const unsigned w0 = cvtpk(bflo(res[i].x) + m[0] + o[0], bfhi(res[i].x) + m[1] + o[1]), w1 = cvtpk(bflo(res[i].y) + m[2] + o[2], bfhi(res[i].y) + m[3] + o[3]);
        if (FIRST) {
            res[i] = (u32x2){w0, w1};
            st64(c.rs, WS_H + (t16 * 80 + 10 * cc + 2 * (CT0 + i) + (gq >> 1)) * 256 + r * 16 + (gq & 1) * 8, res[i]);
            const float a_ = bflo(w0), b_ = bfhi(w0), c_ = bflo(w1), d = bfhi(w1); s1 += (a_ + b_) + (c_ + d); s2 += (a_ * a_ + b_ * b_) + (c_ * c_ + d * d);
        } else {
            *(GAS u32x2*)(c.out + (size_t)tok * 1280 + cc * 160 + (CT0 + i) * 32 + gq * 8) = (u32x2){w0, w1};
        }
    }
    if (FIRST) {
        s1 += __shfl_xor(s1, 16); s2 += __shfl_xor(s2, 16); s1 = swap32_add(s1); s2 = swap32_add(s2);
        LAS f32x2* ex = (LAS f32x2*)(lds + XCH + 2048);
        if (KH == 1 && gq == 0) ex[t16l * 16 + r] = (f32x2){s1, s2};
        LDS_BAR();
        if (KH == 0 && gq == 0) { const f32x2 e = ex[t16l * 16 + r];
            st64(c.rs, WS_HST + ((t16 * 8 + cc) * 16 + r) * 8, (u32x2){__float_as_uint(s1 + e.x), __float_as_uint(s2 + e.y)}); }
    }
}
template <int NKS, int KSS, bool FIRST>
DI void phase_proj(const Ctx& c, unsigned abase, unsigned wbase, int fc, u32x2 (&res)[3]) {
    if (c.wid < 4) pj_body<NKS, KSS, FIRST, 0>(c, abase, wbase, fc, res);
    else pj_body<NKS, KSS, FIRST, 1>(c, abase, wbase, fc, res);
}

constexpr int P4_R = 7, P4_SLOT = 22528, P4_NS = 20;

DI void p4_wvoff(const Ctx& c, int fc, unsigned& woff0, unsigned& woff1) {
    const unsigned o0 = c.wid * 1792 + c.lane * 16, o1 = c.wid * 1792 + 1024 + (c.lane < 48 ? c.lane : 47) * 16;
    woff0 = WS_W13P + (unsigned)imin(7 * fc + (o0 >> 11), 107) * 40960 + (o0 & 2047); woff1 = WS_W13P + (unsigned)imin(7 * fc + (o1 >> 11), 107) * 40960 + (o1 & 2047);
}
DI void p4_issue_w(const Ctx& c, unsigned woff0, unsigned woff1, int st) {
    const char* w_ = c.ws + st * 2048; const unsigned wdst = c.lds0 + 8192 + c.wid * 1792 + (st % P4_R) * P4_SLOT;
    dma16<true>(w_, woff0, wdst); dma16m<true>(w_, woff1, wdst + 1024, 0xffffffffffffull);
}
DI void p4_prefetch(const Ctx& c, int fc) {
    unsigned woff0, woff1; p4_wvoff(c, fc, woff0, woff1);
#pragma unroll
    for (int s = 0; s < P4_R - 1; ++s) p4_issue_w(c, woff0, woff1, s);
}
constexpr int p4_extra(int s) { int n = 0; for (int t = s - 5 < 0 ? 0 : s - 5; t < s; ++t) if (t <= 14 && t % 3 == 2) ++n; return n; }
template <int UG>
DI void p4_body(const Ctx& c, int qb, int fc) {
    constexpr int NU = UG ? 3 : 4, U0 = UG ? 4 : 0;
    const int lane = c.lane, wid = c.wid, tt = wid & 3, r = lane & 31, h = lane >> 5;
    lptr lds = c.lds;
    const unsigned adst = c.lds0 + wid * 1024;
    const unsigned aoff = WS_H + (unsigned)(8 * qb + wid) * 80 * 256 + lane * 16;
    unsigned woff0, woff1; p4_wvoff(c, fc, woff0, woff1);
#pragma unroll
    for (int s = 0; s < P4_R - 1; ++s) dma16<true>(c.ws + s * 1024, aoff, adst + s * P4_SLOT);
    const unsigned xa = ((2 * tt + (r >> 4)) * 4 + h) * 256 + (r & 15) * 16, wa = 8192 + U0 * 2048 + lane * 16;
    const unsigned t16 = 8 * qb + 2 * tt + (r >> 4);
    u32x2 hs[4];
#pragma unroll
    for (int i = 0; i < 4; ++i) hs[i] = lg64(c.rs, WS_HST + ((t16 * 8 + 4 * h + i) * 16 + (r & 15)) * 8);
#define P4_PIECE(st, k) do { const unsigned sl_ = ((st) % P4_R) * P4_SLOT; \
        if ((k) == 0) dma16<true>(c.ws + (st) * 1024, aoff, adst + sl_); \
        if ((k) == 1) dma16<true>(c.ws + (st) * 2048, woff0, c.lds0 + 8192 + wid * 1792 + sl_); \
        if ((k) == 2) dma16m<true>(c.ws + (st) * 2048, woff1, c.lds0 + 8192 + wid * 1792 + sl_ + 1024, 0xffffffffffffull); } while (0)
#define P4_READ(n) do { lcptr b_ = (lcptr)lds + (((n) >> 1) % P4_R) * P4_SLOT; X[(n) % 3] = ld128(b_ + xa + ((n) & 1) * 512); \
        _Pragma("unroll") for (int u = 0; u < NU; ++u) W[(n) % 3][u] = ld128(b_ + wa + u * 2048 + ((n) & 1) * 1024); } while (0)
    f32x16 acc[NU];
#pragma unroll
    for (int u = 0; u < NU; ++u) acc[u] = (f32x16){};
    WAIT_BAR((P4_R - 2) + 4);
    s16x8 X[3], W[3][NU];
    P4_READ(0); P4_READ(1);
#pragma unroll
    for (int s = 0; s < P4_NS; ++s) {
        WAIT_BARV(fly2(s, P4_R, P4_NS, 1, 2) + (s + 1 < P4_R - 1 ? 4 : 0) + p4_extra(s));
        SBAR();
#define P4_NEXT(k) do { if (s + P4_R - 1 < P4_NS) P4_PIECE(s + P4_R - 1, k); SBAR(); } while (0)
#pragma unroll
        for (int ks = 0; ks < 2; ++ks) {
            const int n = 2 * s + ks;
            if (n + 2 < 2 * P4_NS) P4_READ(n + 2);
            SBAR();
            acc[0] = MFMA32(W[n % 3][0], X[n % 3], acc[0]); SBAR(); P4_NEXT(ks);
            acc[1] = MFMA32(W[n % 3][1], X[n % 3], acc[1]); acc[2] = MFMA32(W[n % 3][2], X[n % 3], acc[2]); SBAR();
            if (ks) P4_NEXT(2);
            if (NU > 3) acc[NU - 1] = MFMA32(W[n % 3][NU - 1], X[n % 3], acc[NU - 1]);
            SBAR();
        }
#undef P4_NEXT
        if (s <= 14 && s % 3 == 2) {
            const int L = imin(((c.b >> 3) * 8 + wid) * 5 + s / 3, 1079);
            dma16<true>(c.ws, WS_W2P + (c.b & 1) * 1105920 + L * 1024 + lane * 16, c.lds0 + P4_R * P4_SLOT + 1024); SBAR(); }
    }
#undef P4_READ
    LDS_BAR();
    float s1 = (__uint_as_float(hs[0].x) + __uint_as_float(hs[1].x)) + (__uint_as_float(hs[2].x) + __uint_as_float(hs[3].x));
    float s2 = (__uint_as_float(hs[0].y) + __uint_as_float(hs[1].y)) + (__uint_as_float(hs[2].y) + __uint_as_float(hs[3].y));
    s1 = swap32_add(s1); s2 = swap32_add(s2);
    const float mean = s1 * (1.0f / D), var = fmaxf(s2 * (1.0f / D) - mean * mean, 0.f), rstd = __builtin_amdgcn_rsqf(var + LN_EPS);
    const f32x2 nm = {-mean, -mean}, k1 = {-1.4426950408889634f * rstd, -1.4426950408889634f * rstd}, r2 = {rstd * rstd, rstd * rstd}, one = {1.0f, 1.0f};
    const LAS float* csl = (const LAS float*)(lds + P4_CS);
#pragma unroll
    for (int u = 0; u < NU; ++u) {
        if (7 * fc + U0 + u < 108) {
            u32x2 w[2];
#pragma unroll
            for (int gq = 0; gq < 2; ++gq) {
                const f32x4 cg_ = *(const LAS f32x4*)(csl + (U0 + u) * 16 + 8 * gq + 4 * h), cu = *(const LAS f32x4*)(csl + 128 + (U0 + u) * 16 + 8 * gq + 4 * h);
                f32x2 o[2];
#pragma unroll
                for (int e = 0; e < 2; ++e) {
                    const f32x2 a = __builtin_elementwise_fma(nm, (f32x2){cg_[2 * e], cg_[2 * e + 1]}, (f32x2){acc[u][4 * gq + 2 * e], acc[u][4 * gq + 2 * e + 1]});
                    const f32x2 b2 = __builtin_elementwise_fma(nm, (f32x2){cu[2 * e], cu[2 * e + 1]}, (f32x2){acc[u][8 + 4 * gq + 2 * e], acc[u][8 + 4 * gq + 2 * e + 1]});
                    const f32x2 t = a * k1, d = (f32x2){__builtin_amdgcn_exp2f(t.x), __builtin_amdgcn_exp2f(t.y)} + one;
                    const f32x2 rc = {__builtin_amdgcn_rcpf(d.x), __builtin_amdgcn_rcpf(d.y)};
                    o[e] = (a * b2) * (rc * r2);
                }
                w[gq] = (u32x2){cvtpk(o[0].x, o[0].y), cvtpk(o[1].x, o[1].y)};
            }
            auto x0 = __builtin_amdgcn_permlane32_swap(w[0].x, w[1].x, false, false), x1 = __builtin_amdgcn_permlane32_swap(w[0].y, w[1].y, false, false);
            st128(c.rs, WS_HM + (t16 * 216 + 14 * fc + 2 * (U0 + u) + h) * 256 + (r & 15) * 16, (u32x4){x0[0], x1[0], x0[1], x1[1]});
        }
    }
}
DI void phase4(const Ctx& c, int qb, int fc) {
    if (c.wid < 4) p4_body<0>(c, qb, fc); else p4_body<1>(c, qb, fc);
}
__global__ __launch_bounds__(512, 2) void block_fwd(const char* __restrict__ x, const char* __restrict__ wqkv, const char* __restrict__ wo, const char* __restrict__ w1,
                                                    const char* __restrict__ w3, const char* __restrict__ w2, char* __restrict__ out, char* __restrict__ ws) {
    extern __shared__ __attribute__((aligned(16))) char smem[];
    {
        unsigned long long pc; asm volatile("s_getpc_b64 %0" : "=s"(pc));
        const unsigned wv = __builtin_amdgcn_readfirstlane(threadIdx.x >> 6), piece = ((blockIdx.x >> 3) * 8 + wv) * 1024;
        dma16<false>((const char*)(pc & ~1023ull), (piece < CODE_BYTES ? piece : CODE_BYTES - 1024) + (threadIdx.x & 63) * 16, (unsigned)(uintptr_t)smem + 4 * P1_SLOT + wv * 2048);
    }
    Ctx c; c.x = x; c.wqkv = wqkv; c.wo = wo; c.w1 = w1; c.w3 = w3; c.w2 = w2; c.out = out; c.ws = ws;
    c.rs = __builtin_amdgcn_make_buffer_rsrc(ws, 0, WS_END, 0x00020000);
    c.lds = (lptr)smem; c.lds0 = (unsigned)(uintptr_t)smem; c.b = blockIdx.x; c.lane = threadIdx.x & 63; c.wid = __builtin_amdgcn_readfirstlane(threadIdx.x >> 6);
    const int lane = c.lane, g = c.b & 7, idx = c.b >> 3;
    c.tag = *(const __attribute__((address_space(4))) unsigned*)(ws + WS_E);
    u32x2 res[3] = {(u32x2){0u, 0u}, (u32x2){0u, 0u}, (u32x2){0u, 0u}};
    const int qb4 = 8 * (g & 1) + (idx & 7), fc = 4 * (g >> 1) + (idx >> 3), pp = pj_pp(c.b);

    phase1(c);
    WAIT_VM(0);
    publish(c, FL_KV);
    repack_store(c);

    { const int hh = 4 * g + (idx & 3), qb = idx >> 2;
      const int wg = lane < 16 ? wg_p1(lane, (32 + hh) / 6) : lane < 32 ? wg_p1(lane - 16, (64 + hh) / 6) : wg_p1(2 * qb + (lane & 1), hh / 6);
      const unsigned off[1] = {flag_off(FL_KV, wg)}; wait_flags(c, off); }
    phase2(c);
    WAIT_VM(0);
    publish(c, FL_O);
    pj_prefetch<20, 4>(c, WS_WOP);

    { const unsigned off[1] = {flag_off(FL_O, wg_p2(lane & 31, pp >> 2))}; wait_flags(c, off); }
    phase_proj<20, 4, true>(c, WS_O, WS_WOP, fc, res);
    WAIT_VM(0);
    publish(c, FL_H);
    p4_prefetch(c, fc);

    { const unsigned off[1] = {flag_off(FL_H, wg_p3(2 * qb4 + (lane & 1), (lane >> 1) & 7))}; wait_flags(c, off); }
    phase4(c, qb4, fc);
    WAIT_VM(0);
    publish(c, FL_M);
    pj_prefetch<54, 6>(c, WS_W2P);

    { const unsigned fm = flag_off(FL_M, wg_p4(pp >> 1, lane & 15));
      if (c.b == 0) {
          const unsigned off[5] = {fm, flag_off(FL_O, lane), flag_off(FL_O, 64 + lane), flag_off(FL_O, 128 + lane), flag_off(FL_O, 192 + lane)}; wait_flags(c, off);
      } else { const unsigned off[1] = {fm}; wait_flags(c, off); } }
    phase_proj<54, 6, false>(c, WS_HM, WS_W2P, fc, res);
    if (c.b == 0 && threadIdx.x == 0) st32(c.rs, WS_E, c.tag);
}

}

extern "C" void kernel_launch(void* const* d_in, const int* in_sizes, int n_in, void* d_out, int out_size, void* d_ws, size_t ws_size, hipStream_t stream) {
    using namespace blk;
    const char* x = (const char*)d_in[0]; const char* wqkv = (const char*)d_in[1]; const char* wo = (const char*)d_in[2];
    const char* w1 = (const char*)d_in[4]; const char* w3 = (const char*)d_in[5]; const char* w2 = (const char*)d_in[6];
    char* ws = (char*)d_ws; char* out = (char*)d_out;
    hipFuncSetAttribute((const void*)block_fwd, hipFuncAttributeMaxDynamicSharedMemorySize, LDS_BYTES);
    block_fwd<<<256, 512, LDS_BYTES, stream>>>(x, wqkv, wo, w1, w3, w2, out, ws);
}
```

```cpp
#include <hip/hip_runtime.h>
#include <hip/hip_bf16.h>
#include <stdint.h>

namespace blk {

constexpr int S = 2048, D = 640, H = 32, F = 1728;
constexpr float LN_EPS = 1e-5f;
constexpr float QSCALE = 0.22360679774997896f * 1.4426950408889634f;

#define LAS __attribute__((address_space(3)))
#define GAS __attribute__((address_space(1)))
#define DI __device__ __forceinline__
typedef short s16x8 __attribute__((ext_vector_type(8)));
typedef short s16x4 __attribute__((ext_vector_type(4)));
typedef float f32x16 __attribute__((ext_vector_type(16)));
typedef float f32x4 __attribute__((ext_vector_type(4)));
typedef float f32x2 __attribute__((ext_vector_type(2)));
typedef unsigned u32x4 __attribute__((ext_vector_type(4)));
typedef unsigned u32x2 __attribute__((ext_vector_type(2)));
typedef __bf16 bf16x2 __attribute__((ext_vector_type(2)));
typedef LAS char* lptr;
typedef LAS const char* lcptr;
typedef __amdgpu_buffer_rsrc_t rsrc_t;

constexpr unsigned WS_E = 0;
constexpr unsigned WS_FLAG = 4096;
constexpr unsigned WS_ROWP = 256u << 10;
constexpr unsigned WS_COLP = 512u << 10;
constexpr unsigned WS_Q = 1u << 20;
constexpr unsigned WS_KV = 4u << 20;
constexpr unsigned WS_O = 12u << 20;
constexpr unsigned WS_H = 16u << 20;
constexpr unsigned WS_HST = 20u << 20;
constexpr unsigned WS_CSP = 21u << 20;
constexpr unsigned WS_HM = 24u << 20;
constexpr unsigned WS_W13P = 32u << 20;
constexpr unsigned WS_W2P = 40u << 20;
constexpr unsigned WS_WOP = 44u << 20;
constexpr unsigned WS_END = 48u << 20;
enum { FL_KV = 0, FL_PK = 1, FL_O = 2, FL_H = 3, FL_M = 4 };
constexpr int LDS_BYTES = 163840;
constexpr unsigned SPIN_CAP = 1u << 16;
constexpr unsigned CODE_BYTES = 64 * 1024;

#define SBAR() __builtin_amdgcn_sched_barrier(0)
#define PIN(x) asm volatile("" : "+v"(x))
#define MFMA32(a, b, c) __builtin_amdgcn_mfma_f32_32x32x16_bf16(a, b, c, 0, 0, 0)
#define MFMA32K8(a, b, c) __builtin_amdgcn_mfma_f32_32x32x8bf16_1k(a, b, c, 0, 0, 0)
#define MFMA16(a, b, c) __builtin_amdgcn_mfma_f32_16x16x32_bf16(a, b, c, 0, 0, 0)
#define WAIT_BAR(N) asm volatile("s_waitcnt vmcnt(%0) lgkmcnt(0)\n\ts_barrier" :: "i"(N) : "memory")
#define WAIT_BARV(N) asm volatile("s_waitcnt vmcnt(%0)\n\ts_barrier" :: "i"(N) : "memory")
#define WAIT_VM(N) asm volatile("s_waitcnt vmcnt(%0)" :: "i"(N) : "memory")
#define LDS_BAR() asm volatile("s_waitcnt lgkmcnt(0)\n\ts_barrier" ::: "memory")

template <bool SC> DI void dma16(const void* sbase, unsigned voff, unsigned ldsaddr) {
    if (SC) asm volatile("s_mov_b32 m0, %2\n\ts_nop 0\n\tglobal_load_lds_dwordx4 %0, %1 sc1" :: "v"(voff), "s"(sbase), "s"(ldsaddr) : "memory");
    else asm volatile("s_mov_b32 m0, %2\n\ts_nop 0\n\tglobal_load_lds_dwordx4 %0, %1" :: "v"(voff), "s"(sbase), "s"(ldsaddr) : "memory");
}
template <bool SC> DI void dma16m(const void* sbase, unsigned voff, unsigned ldsaddr, unsigned long long mask) {
    if (SC) asm volatile("s_mov_b32 m0, %2\n\ts_mov_b64 exec, %3\n\tglobal_load_lds_dwordx4 %0, %1 sc1\n\ts_mov_b64 exec, -1" :: "v"(voff), "s"(sbase), "s"(ldsaddr), "s"(mask) : "memory");
    else asm volatile("s_mov_b32 m0, %2\n\ts_mov_b64 exec, %3\n\tglobal_load_lds_dwordx4 %0, %1\n\ts_mov_b64 exec, -1" :: "v"(voff), "s"(sbase), "s"(ldsaddr), "s"(mask) : "memory");
}
constexpr int imin(int a, int b) { return a < b ? a : b; }
constexpr int imax(int a, int b) { return a > b ? a : b; }
constexpr int fly(int s, int R, int NS) { return imax(0, imin(R - 3, NS - 2 - s)); }
constexpr int fly2(int s, int R, int NS, int na, int nw) {
    const int need = imin(s + 1, NS - 1);
    const int pro = imin(R - 1, NS);
    const int issued = pro * na + imax(0, imin(s, NS - R + 1)) * (na + nw);
    const int endpos = need < pro ? (need + 1) * na : pro * na + (need - pro + 1) * (na + nw);
    return imax(0, issued - endpos);
}
DI int rot(int i, int rho, int ns) { const int t = i + rho; return t >= ns ? t - ns : t; }
DI unsigned cvtpk(float lo, float hi) { f32x2 v = {lo, hi}; return __builtin_bit_cast(unsigned, __builtin_convertvector(v, bf16x2)); }
DI float bflo(unsigned w) { return __uint_as_float(w << 16); }
DI float bfhi(unsigned w) { return __uint_as_float(w & 0xffff0000u); }
DI s16x8 ld128(lcptr p) { return *(const LAS s16x8*)p; }
DI s16x4 ld64(lcptr p) { return *(const LAS s16x4*)p; }
DI s16x4 ldtr(lcptr p) { return __builtin_bit_cast(s16x4, __builtin_amdgcn_ds_read_tr16_b64_v4i16((LAS s16x4*)p)); }
DI s16x8 cat(s16x4 a, s16x4 b) { return __builtin_shufflevector(a, b, 0, 1, 2, 3, 4, 5, 6, 7); }
DI f32x4 sel4(unsigned m, f32x4 a, f32x4 b) { const u32x4 x = __builtin_bit_cast(u32x4, a), y = __builtin_bit_cast(u32x4, b); return __builtin_bit_cast(f32x4, (x & m) | (y & ~m)); }
DI float swap32_add(float v) { auto r = __builtin_amdgcn_permlane32_swap(__float_as_uint(v), __float_as_uint(v), false, false); return __uint_as_float(r[0]) + __uint_as_float(r[1]); }
DI float swap32_max(float v) { auto r = __builtin_amdgcn_permlane32_swap(__float_as_uint(v), __float_as_uint(v), false, false); return fmaxf(__uint_as_float(r[0]), __uint_as_float(r[1])); }
DI float upper_half(float v) { auto r = __builtin_amdgcn_permlane32_swap(__float_as_uint(v), __float_as_uint(v), false, false); return __uint_as_float(r[1]); }
DI void st128(rsrc_t rs, unsigned off, u32x4 v) { __builtin_amdgcn_raw_buffer_store_b128(v, rs, off, 0, 16); }
DI void st64(rsrc_t rs, unsigned off, u32x2 v) { __builtin_amdgcn_raw_buffer_store_b64(v, rs, off, 0, 16); }
DI void st32(rsrc_t rs, unsigned off, unsigned v) { __builtin_amdgcn_raw_buffer_store_b32(v, rs, off, 0, 16); }
DI u32x4 lg128(rsrc_t rs, unsigned off) { return __builtin_amdgcn_raw_buffer_load_b128(rs, off, 0, 16); }
DI u32x2 lg64(rsrc_t rs, unsigned off) { return __builtin_amdgcn_raw_buffer_load_b64(rs, off, 0, 16); }
DI unsigned lg32(rsrc_t rs, unsigned off) { return __builtin_amdgcn_raw_buffer_load_b32(rs, off, 0, 16); }

DI void ldg_async(s16x8& dst, const void* sbase, unsigned voff) { asm volatile("global_load_dwordx4 %0, %1, %2 sc1" : "=v"(dst) : "v"(voff), "s"(sbase) : "memory"); }
#define WAIT_USE(N, x) asm volatile("s_waitcnt vmcnt(%1)" : "+v"(x) : "i"(N) : "memory")
struct Ctx { const char *x, *wqkv, *wo, *w1, *w3, *w2; char *out, *ws; rsrc_t rs; lptr lds; unsigned lds0, tag; int b, lane, wid; };

DI unsigned flag_off(int kind, int wg) { return WS_FLAG + (unsigned)(kind * 256 + wg) * 128u; }
DI void publish(const Ctx& c, int kind) {
    asm volatile("s_barrier" ::: "memory");
    if (threadIdx.x == 0) st32(c.rs, flag_off(kind, c.b), c.tag);
}
template <int N> DI void wait_flags(const Ctx& c, const unsigned (&off)[N]) {
    if (c.wid == 0) {
        for (unsigned spins = 0; spins < SPIN_CAP; ++spins) {
            unsigned v[N];
#pragma unroll
            for (int i = 0; i < N; ++i) v[i] = lg32(c.rs, off[i]);
            bool ok = true;
#pragma unroll
            for (int i = 0; i < N; ++i) ok &= v[i] == c.tag;
            if (__all(ok)) break;
            __builtin_amdgcn_s_sleep(1);
        }
    }
    asm volatile("s_waitcnt lgkmcnt(0)\n\ts_barrier" ::: "memory");
}
DI int wg_p1(int pb, int nb) { return ((pb >> 3) | ((nb >> 2) << 1)) + 8 * ((pb & 7) + 8 * (nb & 3)); }
DI int wg_p2(int hh, int qb) { return (hh >> 2) + 8 * ((hh & 3) + 4 * qb); }
DI int wg_p3(int pp, int cc) { return (((pp >> 3) << 1) | (cc >> 2)) + 8 * ((pp & 7) + 8 * (cc & 3)); }
DI int wg_p4(int qb, int fc) { return ((qb >> 3) | ((fc >> 2) << 1)) + 8 * ((qb & 7) + 8 * (fc & 3)); }

constexpr int P1_R = 5, P1_SLOT = 32768, P1_NS = 10;
constexpr int P1_STG = 264;
constexpr int P1_OUT = 65536;
constexpr int P1_ST = P1_OUT + 40960;
constexpr int P1_CS = P1_ST + 1024;

DI void repack_issue_one(const Ctx& c, int i);
template <int UP>
DI void p1_loop(Ctx& c, unsigned xoff, unsigned woff, int tt, int rho, unsigned put, unsigned get, f32x16& acc0, f32x16& acc1, u32x2 (&part)[10]) {
    f32x16 accx = {};
    const int lane = c.lane, wid = c.wid, r = lane & 31, h = lane >> 5, q = (lane >> 2) & 3, p = lane & 3, cg = (lane >> 4) & 1;
    constexpr unsigned long long WMASK = ~((1ull << 15) | (1ull << 27) | (1ull << 39) | (1ull << 51));
    const unsigned xdst = c.lds0 + wid * 2048, wdst = c.lds0 + 16384 + wid * 2048;
    const int xrow = 32 * tt + r;
    const unsigned xa = xrow * 128 + ((h ^ ((xrow >> 1) & 7)) * 16);
    const unsigned wk = 16384 + (8 * h + q) * 256 + (p & 1) * 8;
    const unsigned wa0 = wk + (((4 * (2 * UP) + 2 * cg + (p >> 1)) ^ (4 * q)) * 16);
    const unsigned wa1 = wk + (((4 * (2 * UP + 1) + 2 * cg + (p >> 1)) ^ (4 * q)) * 16);
    const unsigned waz = wk + (((4 * tt + 2 * cg + (p >> 1)) ^ (4 * q)) * 16);
    const s16x8 ones = {0x3f80, 0x3f80, 0x3f80, 0x3f80, 0x3f80, 0x3f80, 0x3f80, 0x3f80};
#define P1_PIECE(st, slot, k) do { const int p_ = rot(st, rho, P1_NS); \
        if ((k) == 0) dma16<false>(c.x + p_ * 128, xoff, xdst + (slot)); \
        if ((k) == 1) dma16<false>(c.x + p_ * 128 + 8 * 1280, xoff ^ 64, xdst + (slot) + 1024); \
        if ((k) == 2) dma16m<false>(c.wqkv + (size_t)p_ * (64 * 3840), woff, wdst + (slot), WMASK); \
        if ((k) == 3) dma16m<false>(c.wqkv + (size_t)p_ * (64 * 3840) + 4 * 3840, woff, wdst + (slot) + 1024, WMASK); } while (0)
#define P1_ISSUE(st, slot) do { P1_PIECE(st, slot, 0); P1_PIECE(st, slot, 1); P1_PIECE(st, slot, 2); P1_PIECE(st, slot, 3); } while (0)
#define P1_READ(n) do { lcptr b_ = (lcptr)c.lds + (((n) >> 2) % P1_R) * P1_SLOT; \
        X[(n) % 3] = ld128(b_ + (xa ^ (((n) & 3) * 32))); \
        W0[(n) % 3] = cat(ldtr(b_ + wa0 + ((n) & 3) * 4096), ldtr(b_ + wa0 + ((n) & 3) * 4096 + 1024)); \
        W1[(n) % 3] = cat(ldtr(b_ + wa1 + ((n) & 3) * 4096), ldtr(b_ + wa1 + ((n) & 3) * 4096 + 1024)); \
        if (UP && (n) < 4) WZ[(n) & 3] = cat(ldtr(b_ + waz + ((n) & 3) * 4096), ldtr(b_ + waz + ((n) & 3) * 4096 + 1024)); } while (0)
#define P1_MMA(n) do { acc0 = MFMA32(W0[(n) % 3], X[(n) % 3], acc0); acc1 = MFMA32(W1[(n) % 3], X[(n) % 3], acc1); \
        if ((n) < 4) { if (UP) accx = MFMA32(WZ[(n) & 3], ones, accx); else accx = MFMA32(X[(n) % 3], X[(n) % 3], accx); } } while (0)
    P1_ISSUE(0, 0); P1_ISSUE(1, P1_SLOT); P1_ISSUE(2, 2 * P1_SLOT);
    WAIT_BAR(8);
    asm volatile("" : "+s"(c.tag));
    c.tag += 1u;
    P1_ISSUE(3, 3 * P1_SLOT);
    s16x8 X[3] = {}, W0[3] = {}, W1[3] = {}, WZ[4] = {};
    P1_READ(0); P1_READ(1);
#pragma unroll
    for (int s = 0; s < P1_NS; ++s) {
        constexpr int NX = P1_R - 1, GS = P1_NS - P1_R;
        WAIT_BARV(4 * fly(s, P1_R, P1_NS) + (s >= 1 && s + 1 <= P1_R - 1 ? 1 : 0) + (s > GS && !UP ? 10 : 0) + imax(0, s - GS - 1));
        SBAR();
#pragma unroll
        for (int ks = 0; ks < 4; ++ks) {
            const int n = 4 * s + ks;
            if (n + 2 < 4 * P1_NS) P1_READ(n + 2);
            SBAR(); P1_MMA(n); SBAR();
            if (s + NX < P1_NS) P1_PIECE(s + NX, ((s + NX) % P1_R) * P1_SLOT, ks);
            if (s > GS && ks == 1) repack_issue_one(c, s - GS - 1);
            SBAR();
        }
        if (s == 0) {
            const int tr = (r & 3) + 4 * (r >> 3); float v = 0.f;
#pragma unroll
            for (int i = 0; i < 16; ++i) v = (tr == i) ? accx[i] : v;
            st64(c.rs, put + (h == ((r >> 2) & 1) ? (tt * 32 + r) * 8 : 0x7fffff00u), (u32x2){__float_as_uint(v), c.tag});
            SBAR();
        }
        if (s == GS && !UP) {
#pragma unroll
            for (int i = 0; i < 10; ++i) part[i] = lg64(c.rs, get + i * 1024);
            SBAR();
        }
    }
#undef P1_PIECE
#undef P1_ISSUE
#undef P1_READ
#undef P1_MMA
}

DI void phase1(Ctx& c) {
    const int lane = c.lane, wid = c.wid, tid = threadIdx.x, g = c.b & 7, idx = c.b >> 3;
    const int pb = 8 * (g & 1) + (idx & 7), nb = 4 * (g >> 1) + (idx >> 3);
    const int tt = wid & 3, up = wid >> 2, r = lane & 31, h = lane >> 5;
    lptr lds = c.lds;
    if (tid < P1_R * 64) { const int sl = tid >> 6, k = tid & 63;
        *(LAS u32x4*)(lds + sl * P1_SLOT + 16384 + k * 256 + ((15 ^ (4 * (k & 3))) * 16)) = (u32x4){0x3f803f80u, 0x3f803f80u, 0x3f803f80u, 0x3f803f80u}; }
    const int xr = 16 * wid + (lane >> 3), xc = (lane & 7) ^ ((xr >> 1) & 7);
    const unsigned xoff = (pb * 128 + xr) * 1280 + xc * 16;
    const int wkk = 8 * wid + (lane >> 4), wp = (lane & 15) ^ (4 * (wkk & 3)), wpc = wp < 15 ? wp : 14;
    const unsigned woff = wkk * 3840 + nb * 240 + wpc * 16;
    f32x16 acc0 = {}, acc1 = {};
    const int rho = (pb + 3 * nb) % P1_NS;
    const unsigned put = up ? WS_COLP + (unsigned)(nb * 16 + pb) * 1024 : WS_ROWP + (unsigned)(pb * 16 + nb) * 1024;
    const int gi = (wid & 1) * 64 + lane;
    const unsigned get = ((wid & 2) ? WS_COLP + (unsigned)nb * 16384 : WS_ROWP + (unsigned)pb * 16384) + gi * 8;
    u32x2 part[10];
    if (up == 0) p1_loop<0>(c, xoff, woff, tt, rho, put, get, acc0, acc1, part);
    else p1_loop<1>(c, xoff, woff, tt, rho, put, get, acc0, acc1, part);
    float psum = 0.f;
    if (up == 0) {
        for (unsigned spins = 0; spins < SPIN_CAP; ++spins) {
            bool ok = true;
#pragma unroll
            for (int i = 0; i < 10; ++i) ok &= part[i].y == c.tag;
            if (__all(ok)) break;
            __builtin_amdgcn_s_sleep(1);
#pragma unroll
            for (int i = 0; i < 10; ++i) part[i] = lg64(c.rs, get + i * 1024);
        }
#pragma unroll
        for (int i = 0; i < 10; ++i) psum += __uint_as_float(part[i].x);
    }
    LDS_BAR();
    LAS float* st = (LAS float*)(lds + P1_ST); LAS float* cs = (LAS float*)(lds + P1_CS);
    if (wid < 2) st[gi * 2 + 1] = psum; else if (wid < 4) cs[gi] = psum;
    if (up == 1 && h == 0) st[(tt * 32 + r) * 2] = acc1[12];
    LDS_BAR();
    { const f32x2 sv = *(const LAS f32x2*)(st + (tt * 32 + r) * 2);
      const float mean = sv.x * (1.0f / D), var = fmaxf(sv.y * (1.0f / D) - mean * mean, 0.f), rstd = __builtin_amdgcn_rsqf(var + LN_EPS), rm = rstd * mean;
      lptr stg = lds + P1_OUT + (32 * tt + r) * P1_STG;
#pragma unroll
      for (int u = 0; u < 2; ++u)
#pragma unroll
        for (int gq = 0; gq < 4; ++gq) {
            const int col = (2 * up + u) * 32 + 8 * gq + 4 * h;
            const f32x4 c4 = *(const LAS f32x4*)(cs + col);
            const float sc = (nb * 120 + col < D) ? QSCALE : 1.0f, a = rstd * sc, bb = rm * sc;
            float v[4];
#pragma unroll
            for (int e = 0; e < 4; ++e) v[e] = a * (u ? acc1[4 * gq + e] : acc0[4 * gq + e]) - bb * c4[e];
            *(LAS u32x2*)(stg + col * 2) = (u32x2){cvtpk(v[0], v[1]), cvtpk(v[2], v[3])};
        }
    }
    LDS_BAR();
#pragma unroll
    for (int j = 0; j < 3; ++j) {
        const int slot = up + 2 * j, gidx = 6 * nb + slot, type = gidx >> 5, head = gidx & 31, tile = 4 * pb + tt;
        if (type < 2) {
            lcptr row = (lcptr)lds + P1_OUT + (32 * tt + r) * P1_STG + slot * 40;
            const u32x2 a0 = *(const LAS u32x2*)(row + 16 * h), a1 = *(const LAS u32x2*)(row + 16 * h + 8), a2 = *(const LAS u32x2*)(row + 32);
            const unsigned dst = type ? WS_KV + (unsigned)(head * 64 + tile) * 3072 : WS_Q + (unsigned)(head * 64 + tile) * 1536;
            st128(c.rs, dst + lane * 16, (u32x4){a0.x, a0.y, a1.x, a1.y});
            st64(c.rs, dst + 1024 + lane * 8, h ? (u32x2){type ? 0x00003f80u : 0u, 0u} : a2);
        } else {
            const int q = (lane >> 2) & 3, p = lane & 3, cg = (lane >> 4) & 1;
            lcptr vb = (lcptr)lds + P1_OUT + (32 * tt + 4 * h + q) * P1_STG + slot * 40 + (cg * 4 + p < 5 ? (cg * 4 + p) * 8 : 0);
            const unsigned dst = WS_KV + (unsigned)(head * 64 + tile) * 3072 + 1536;
#pragma unroll
            for (int s = 0; s < 2; ++s) {
                const u32x2 a = __builtin_bit_cast(u32x2, ldtr(vb + (16 * s) * P1_STG)), e = __builtin_bit_cast(u32x2, ldtr(vb + (16 * s + 8) * P1_STG));
                u32x4 v = {a.x, a.y, e.x, e.y};
                if (r == 20) v = (u32x4){0x3f803f80u, 0x3f803f80u, 0x3f803f80u, 0x3f803f80u};
                if (r > 20) v = (u32x4){0u, 0u, 0u, 0u};
                if (r < 24) st128(c.rs, dst + s * 768 + h * 384 + r * 16, v);
            }
        }
    }
}

constexpr int PK_OFF = 0;
DI int pk_job(int b, int i) { const int j = b + 256 * i; return j < 910 ? j : j - 256; }
DI void repack_issue_one(const Ctx& c, int i) {
    const int row = 8 * c.wid + (c.lane >> 3), pc = c.lane & 7;
    const int j = pk_job(c.b, i);
    const char* src; unsigned off;
    if (j < 540) { const int jj = j < 270 ? j : j - 270, kb = jj / 27, cb = jj % 27; src = j < 270 ? c.w1 : c.w3; off = (64 * kb + row) * 3456 + cb * 128 + pc * 16; }
    else if (j < 810) { const int jj = j - 540, kb = jj / 10, cb = jj % 10; src = c.w2; off = (64 * kb + row) * 1280 + cb * 128 + pc * 16; }
    else { const int jj = j - 810, kb = jj / 10, cb = jj % 10, kp = 64 * kb + row, G = kp >> 3, e = kp & 7;
        const int k = G < 64 ? 20 * (G >> 1) + 8 * (G & 1) + e : 40 * (G - 64) + 16 + (e < 4 ? e : e + 16);
        src = c.wo; off = k * 1280 + cb * 128 + pc * 16; }
    dma16<false>(src, off, c.lds0 + PK_OFF + i * 8192 + c.wid * 1024);
}
DI void repack_store(const Ctx& c) {
    const int lane = c.lane, w = c.wid, g = lane >> 4, i16 = lane & 15, q = i16 >> 2, p = i16 & 3;
#pragma unroll
    for (int i = 0; i < 4; ++i) {
        const int j = pk_job(c.b, i);
        lcptr blk = (lcptr)c.lds + PK_OFF + i * 8192 + (w >> 1) * 32 + p * 8;
        if (j < 540) {
            const int mat = j >= 270, jj = mat ? j - 270 : j, kb = jj / 27, cb = jj % 27, ks = 2 * (w & 1) + (g >> 1), hh = g & 1;
            lcptr a = blk + (16 * ks + 8 * hh + q) * 128;
            const u32x2 v0 = __builtin_bit_cast(u32x2, ldtr(a)), v1 = __builtin_bit_cast(u32x2, ldtr(a + 512));
            st128(c.rs, WS_W13P + (unsigned)((4 * cb + (w >> 1)) * 40 + 4 * kb + ks) * 1024 + (mat * 16 + i16 + 32 * hh) * 16, (u32x4){v0.x, v0.y, v1.x, v1.y});
            float s = ((bflo(v0.x) + bfhi(v0.x)) + (bflo(v0.y) + bfhi(v0.y))) + ((bflo(v1.x) + bfhi(v1.x)) + (bflo(v1.y) + bfhi(v1.y)));
            s += __shfl_xor(s, 16); s += __shfl_xor(s, 32);
            if (lane < 16) st32(c.rs, WS_CSP + (unsigned)((mat * 20 + 2 * kb + (w & 1)) * F + 64 * cb + 16 * (w >> 1) + i16) * 4, __float_as_uint(s));
        } else {
            const bool isw2 = j < 810; const int jj = isw2 ? j - 540 : j - 810, kb = jj / 10, cb = jj % 10, ks = w & 1;
            lcptr a = blk + (32 * ks + 8 * g + q) * 128;
            const u32x2 v0 = __builtin_bit_cast(u32x2, ldtr(a)), v1 = __builtin_bit_cast(u32x2, ldtr(a + 512));
            const unsigned dst = isw2 ? WS_W2P + (unsigned)((4 * cb + (w >> 1)) * 54 + 2 * kb + ks) * 1024 : WS_WOP + (unsigned)((4 * cb + (w >> 1)) * 20 + 2 * kb + ks) * 1024;
            st128(c.rs, dst + lane * 16, (u32x4){v0.x, v0.y, v1.x, v1.y});
        }
    }
}

constexpr int P2_R = 4, P2_SLOT = 24576, P2_NS = 8;
constexpr int P2_Q = 110592;
constexpr int XCH = 135168;
constexpr int P2_EX = XCH;
constexpr int P4_CS = XCH + 24576;

DI void attn_safe(const Ctx& c, unsigned kv, s16x8 q0, s16x4 q1z, f32x16& o) {
    const int lane = c.lane, r = lane & 31, h = lane >> 5, vr = r < 24 ? r : r - 16;
    float m = -INFINITY; o = (f32x16){};
    for (int t = 0; t < 64; ++t) {
        const unsigned p = kv + t * 3072;
        const u32x4 k0 = lg128(c.rs, p + lane * 16); const u32x2 k1 = lg64(c.rs, p + 1024 + lane * 8);
        const u32x4 v0 = lg128(c.rs, p + 1536 + h * 384 + vr * 16), v1 = lg128(c.rs, p + 1536 + 768 + h * 384 + vr * 16);
        f32x16 s = MFMA32(__builtin_bit_cast(s16x8, k0), q0, (f32x16){}); s = MFMA32K8(__builtin_bit_cast(s16x4, k1), q1z, s);
        float tm = s[0];
#pragma unroll
        for (int i = 1; i < 16; ++i) tm = fmaxf(tm, s[i]);
        tm = swap32_max(tm);
        const float mn = fmaxf(m, tm), al = __builtin_amdgcn_exp2f(m - mn); m = mn;
#pragma unroll
        for (int i = 0; i < 16; ++i) { o[i] *= al; s[i] = __builtin_amdgcn_exp2f(s[i] - mn); }
        const u32x4 p0 = {cvtpk(s[0], s[1]), cvtpk(s[2], s[3]), cvtpk(s[4], s[5]), cvtpk(s[6], s[7])};
        const u32x4 p1 = {cvtpk(s[8], s[9]), cvtpk(s[10], s[11]), cvtpk(s[12], s[13]), cvtpk(s[14], s[15])};
        o = MFMA32(__builtin_bit_cast(s16x8, v0), __builtin_bit_cast(s16x8, p0), o); o = MFMA32(__builtin_bit_cast(s16x8, v1), __builtin_bit_cast(s16x8, p1), o);
    }
}

DI void phase2(const Ctx& c) {
    const int lane = c.lane, wid = c.wid, g = c.b & 7, idx = c.b >> 3, hh = 4 * g + (idx & 3), qb = idx >> 2;
    const int qg = wid & 3, kh = wid >> 2, r = lane & 31, h = lane >> 5;
    lptr lds = c.lds;
    const char* kvsrc = c.ws + WS_KV + (size_t)hh * 64 * 3072 + wid * 3072;
    const unsigned dst0 = c.lds0 + wid * 3072, voff = lane * 16;
#define P2_ISSUE(st, slot) do { const char* s_ = kvsrc + (size_t)(st) * 24576; dma16<true>(s_, voff, dst0 + (slot)); dma16<true>(s_ + 1024, voff, dst0 + (slot) + 1024); dma16<true>(s_ + 2048, voff, dst0 + (slot) + 2048); } while (0)
    { const char* qsrc = c.ws + WS_Q + ((size_t)hh * 64 + 8 * qb + wid) * 1536;
      dma16<true>(qsrc, voff, c.lds0 + P2_Q + wid * 1536); dma16m<true>(qsrc + 1024, voff, c.lds0 + P2_Q + wid * 1536 + 1024, 0xffffffffull); }
    P2_ISSUE(0, 0); P2_ISSUE(1, P2_SLOT);
    const int vr = r < 24 ? r : 21 + (r & 1);
    const unsigned ka = 4 * kh * 3072 + lane * 16, kb = 4 * kh * 3072 + 1024 + lane * 8, va = 4 * kh * 3072 + 1536 + h * 384 + vr * 16;
#define LDK0(slot, t) ld128((lcptr)lds + (slot) + ka + (t) * 3072)
#define LDK1(slot, t) ld64((lcptr)lds + (slot) + kb + (t) * 3072)
#define LDV0(slot, t) ld128((lcptr)lds + (slot) + va + (t) * 3072)
#define LDV1(slot, t) ld128((lcptr)lds + (slot) + va + (t) * 3072 + 768)
    WAIT_BAR(3);
    if (threadIdx.x == 0) st32(c.rs, flag_off(FL_PK, c.b), c.tag);
    P2_ISSUE(2, 2 * P2_SLOT);
    s16x8 q0[2]; s16x4 q1z[2], q1[2];
#pragma unroll
    for (int i = 0; i < 2; ++i) { q0[i] = ld128((lcptr)lds + P2_Q + (2 * qg + i) * 1536 + lane * 16); q1z[i] = ld64((lcptr)lds + P2_Q + (2 * qg + i) * 1536 + 1024 + lane * 8); }
    { const s16x8 k0 = ld128((lcptr)lds + lane * 16); const s16x4 k1 = ld64((lcptr)lds + 1024 + lane * 8);
#pragma unroll
      for (int i = 0; i < 2; ++i) { f32x16 s = MFMA32(k0, q0[i], (f32x16){}); s = MFMA32K8(k1, q1z[i], s);
          float m = s[0];
#pragma unroll
          for (int e = 1; e < 16; ++e) m = fmaxf(m, s[e]);
          m = swap32_max(m);
          q1[i] = q1z[i]; if (h) q1[i][0] = (short)(cvtpk(-m, 0.f) & 0xffffu); } }
    s16x8 kA0 = LDK0(0, 0), kB0, vA0 = LDV0(0, 0), vA1 = LDV1(0, 0), vB0, vB1 = vA1; s16x4 kA1 = LDK1(0, 0), kB1;
    f32x16 sA, sB, o[2] = {(f32x16){}, (f32x16){}};
    sA = MFMA32(kA0, q0[0], (f32x16){}); sA = MFMA32K8(kA1, q1[0], sA);
    u32x4 p0, p1 = {0u, 0u, 0u, 0u};
    int cur = 0, nxt = P2_SLOT, fre = (P2_R - 1) * P2_SLOT;
#define EX(v) __builtin_amdgcn_exp2f(v)
#define P2_STEP(C, N, K0, K1, NQ, OC, V0, OP, VP, LOADS, LAST) do { \
        OP = MFMA32(VP, __builtin_bit_cast(s16x8, p1), OP); SBAR(); \
        LOADS; SBAR(); \
        C[0] = EX(C[0]); C[1] = EX(C[1]); C[2] = EX(C[2]); C[3] = EX(C[3]); p0[0] = cvtpk(C[0], C[1]); p0[1] = cvtpk(C[2], C[3]); PIN(p0); SBAR(); \
        if (!(LAST)) N = MFMA32(K0, q0[NQ], (f32x16){}); SBAR(); \
        C[4] = EX(C[4]); C[5] = EX(C[5]); C[6] = EX(C[6]); C[7] = EX(C[7]); p0[2] = cvtpk(C[4], C[5]); p0[3] = cvtpk(C[6], C[7]); PIN(p0); SBAR(); \
        if (!(LAST)) N = MFMA32K8(K1, q1[NQ], N); SBAR(); \
        C[8] = EX(C[8]); C[9] = EX(C[9]); C[10] = EX(C[10]); C[11] = EX(C[11]); p1[0] = cvtpk(C[8], C[9]); p1[1] = cvtpk(C[10], C[11]); PIN(p1); SBAR(); \
        OC = MFMA32(V0, __builtin_bit_cast(s16x8, p0), OC); SBAR(); \
        C[12] = EX(C[12]); C[13] = EX(C[13]); C[14] = EX(C[14]); C[15] = EX(C[15]); p1[2] = cvtpk(C[12], C[13]); p1[3] = cvtpk(C[14], C[15]); PIN(p1); SBAR(); \
    } while (0)
#define P2_PIECE(st, k) do { if ((st) >= 0) dma16<true>(kvsrc + (size_t)(st) * 24576 + (k) * 1024, voff, dst0 + fre + (k) * 1024); } while (0)
#define P2_PAIR(T, NS_, NT, LAST, ST, KA, KB) do { \
        if (((T) == 0) == (kh != 0)) __builtin_amdgcn_s_setprio(1); else __builtin_amdgcn_s_setprio(0); \
        P2_STEP(sA, sB, kA0, kA1, 1, o[0], vA0, o[1], vB1, { kB0 = LDK0(cur, (T) + 1); kB1 = LDK1(cur, (T) + 1); vB0 = LDV0(cur, (T) + 1); vB1 = LDV1(cur, (T) + 1); }, false); \
        P2_STEP(sB, sA, kB0, kB1, 0, o[1], vA0, o[0], vA1, { P2_PIECE(ST, KA); }, false); \
        P2_STEP(sA, sB, kB0, kB1, 1, o[0], vB0, o[1], vA1, { if (!(LAST)) { kA0 = LDK0(NS_, NT); kA1 = LDK1(NS_, NT); vA0 = LDV0(NS_, NT); vA1 = LDV1(NS_, NT); } }, false); \
        P2_STEP(sB, sA, kA0, kA1, 0, o[1], vB0, o[0], vB1, { if ((KB) >= 0) P2_PIECE(ST, KB); }, LAST); } while (0)
#define P2_STAGE(LAST, ST) do { P2_PAIR(0, cur, 2, false, ST, 0, 1); P2_PAIR(2, nxt, 0, LAST, ST, 2, -1); \
        fre = cur; cur = nxt; nxt = (nxt == (P2_R - 1) * P2_SLOT) ? 0 : nxt + P2_SLOT; } while (0)
#pragma unroll
    for (int s = 0; s < P2_NS - 3; ++s) { WAIT_BAR(3); P2_STAGE(false, s + P2_R - 1); }
    WAIT_BAR(3);
    unsigned pk[4];
    if (wid == 0) {
#pragma unroll
        for (int i = 0; i < 4; ++i) pk[i] = lg32(c.rs, flag_off(FL_PK, 64 * i + lane));
    }
    P2_STAGE(false, -1);
    if (wid == 0) {
        for (unsigned spins = 0; spins < SPIN_CAP; ++spins) {
            if (__all(pk[0] == c.tag && pk[1] == c.tag && pk[2] == c.tag && pk[3] == c.tag)) break;
            __builtin_amdgcn_s_sleep(1);
#pragma unroll
            for (int i = 0; i < 4; ++i) pk[i] = lg32(c.rs, flag_off(FL_PK, 64 * i + lane));
        }
    }
    WAIT_BAR(0);
    const unsigned wsc = c.lds0 + P2_Q + 12288 + wid * 1024;
    unsigned wu[6];
#pragma unroll
    for (int i = 0; i < 6; ++i) { const int L = imin((idx * 8 + wid) * 6 + i, 1519);
        wu[i] = (L < 400 ? WS_WOP + (g & 1) * 409600 + L * 1024 : WS_W13P + imin((g >> 1) * 1146880 + (L - 400) * 1024, 108 * 40960 - 1024)) + lane * 16; }
#define P2_WARM(i) dma16<true>(c.ws, wu[i], wsc)
    P2_PAIR(0, cur, 2, false, -1, 0, 1); P2_WARM(0); P2_WARM(1); P2_PAIR(2, nxt, 0, false, -1, 2, -1); P2_WARM(2);
    fre = cur; cur = nxt; nxt = (nxt == (P2_R - 1) * P2_SLOT) ? 0 : nxt + P2_SLOT;
    P2_WARM(3); P2_PAIR(0, cur, 2, false, -1, 0, 1); P2_WARM(4); P2_WARM(5); P2_PAIR(2, nxt, 0, true, -1, 2, -1);
#undef P2_WARM
    o[1] = MFMA32(vB1, __builtin_bit_cast(s16x8, p1), o[1]);
#undef P2_STEP
#undef P2_PAIR
#undef P2_PIECE
#undef P2_STAGE
#undef P2_ISSUE
    __builtin_amdgcn_s_setprio(0);
    LAS f32x4* ex = (LAS f32x4*)(lds + P2_EX);
    { const unsigned m = kh ? 0xffffffffu : 0u;
#pragma unroll
      for (int i = 0; i < 3; ++i) { const f32x4 a = {o[0][4 * i], o[0][4 * i + 1], o[0][4 * i + 2], o[0][4 * i + 3]}, e = {o[1][4 * i], o[1][4 * i + 1], o[1][4 * i + 2], o[1][4 * i + 3]};
          ex[(wid * 3 + i) * 64 + lane] = sel4(m, a, e); }
      LDS_BAR();
      f32x16 of = {};
#pragma unroll
      for (int i = 0; i < 3; ++i) { const f32x4 a = {o[0][4 * i], o[0][4 * i + 1], o[0][4 * i + 2], o[0][4 * i + 3]}, e = {o[1][4 * i], o[1][4 * i + 1], o[1][4 * i + 2], o[1][4 * i + 3]};
          const f32x4 t = sel4(m, e, a) + ex[((wid ^ 4) * 3 + i) * 64 + lane];
          of[4 * i] = t[0]; of[4 * i + 1] = t[1]; of[4 * i + 2] = t[2]; of[4 * i + 3] = t[3]; }
      float l = upper_half(of[8]);
      bool bad = !(l < 1.2676506e30f && l > 8.6736174e-19f);
#pragma unroll
      for (int i = 0; i < 12; ++i) bad |= !(fabsf(of[i]) < 3.0e38f);
      if (__any(bad)) {
          const s16x8 qq0 = ld128((lcptr)lds + P2_Q + (2 * qg + kh) * 1536 + lane * 16); const s16x4 qq1 = ld64((lcptr)lds + P2_Q + (2 * qg + kh) * 1536 + 1024 + lane * 8);
          attn_safe(c, WS_KV + (unsigned)hh * 64 * 3072, qq0, qq1, of); l = upper_half(of[8]); }
      const float il = __builtin_amdgcn_rcpf(l);
      const unsigned a0 = cvtpk(of[0] * il, of[1] * il), a1 = cvtpk(of[2] * il, of[3] * il), b0 = cvtpk(of[4] * il, of[5] * il), b1 = cvtpk(of[6] * il, of[7] * il);
      auto x0 = __builtin_amdgcn_permlane32_swap(a0, b0, false, false), x1 = __builtin_amdgcn_permlane32_swap(a1, b1, false, false);
      const unsigned t16 = 2 * (8 * qb + 2 * qg + kh) + (r >> 4);
      st128(c.rs, WS_O + (t16 * 80 + 2 * hh + h) * 256 + (r & 15) * 16, (u32x4){x0[0], x1[0], x0[1], x1[1]});
      if (h == 0) st64(c.rs, WS_O + (t16 * 80 + 64 + (hh >> 1)) * 256 + (r & 15) * 16 + (hh & 1) * 8, (u32x2){cvtpk(of[8] * il, of[9] * il), cvtpk(of[10] * il, of[11] * il)});
    }
#undef LDK0
#undef LDK1
#undef LDV0
#undef LDV1
#undef EX
}

constexpr int PJ_R = 5;
template <int NKS, int KSS> struct PJ {
    static constexpr int NS = NKS / KSS, SLOT = 5 * KSS * 1024, NWI = (5 * KSS + 7) / 8, NA = NKS / 2, PRE = imin(PJ_R, NS);
};
template <int NKS, int KSS> DI void pj_issue_w(const Ctx& c, const unsigned (&voff)[(5 * KSS + 7) / 8], int st) {
    using P = PJ<NKS, KSS>;
    const char* base = c.ws + st * (KSS * 1024);
#pragma unroll
    for (int i = 0; i < P::NWI; ++i) dma16<true>(base, voff[i], c.lds0 + (st % PJ_R) * P::SLOT + imin(c.wid + 8 * i, 5 * KSS - 1) * 1024);
}
template <int NKS, int KSS> DI void pj_voff(const Ctx& c, unsigned wbase, int cc, unsigned (&voff)[(5 * KSS + 7) / 8]) {
#pragma unroll
    for (int i = 0; i < PJ<NKS, KSS>::NWI; ++i) { const int f = imin(c.wid + 8 * i, 5 * KSS - 1); voff[i] = wbase + (unsigned)((5 * cc + f / KSS) * NKS + f % KSS) * 1024 + c.lane * 16; }
}
DI int pj_pp(int b) { return 8 * ((b & 7) >> 1) + ((b >> 3) & 7); }
DI int pj_cc(int b) { return 4 * (b & 1) + (b >> 6); }
template <int NKS, int KSS> DI void pj_prefetch(const Ctx& c, unsigned wbase) {
    unsigned voff[PJ<NKS, KSS>::NWI]; pj_voff<NKS, KSS>(c, wbase, pj_cc(c.b), voff);
#pragma unroll
    for (int s = 0; s < PJ<NKS, KSS>::PRE; ++s) pj_issue_w<NKS, KSS>(c, voff, s);
}

template <int NA, int WIN, int HK, int NS, int NWI, int NRES> constexpr int pj_younger(int j) {
    int n = imin(NA - 1, j + WIN) - j;
    if (j < WIN) n += NRES;
    for (int s = 1; s + PJ_R - 1 < NS; ++s)
        if (HK * s <= j && (j < WIN || HK * s > j - WIN)) n += NWI;
    return n;
}
template <int NA, int WIN, int HK, int NS, int NWI> constexpr int pj_bar_younger(int s) {
    const int s0 = s + 2 - PJ_R;
    int n = 0;
    for (int t = s0 + 1; t < s; ++t) if (t + PJ_R - 1 < NS) n += NWI;
    for (int j = HK * s0; j < HK * s; ++j) if (j + WIN < NA) ++n;
    return n;
}
template <int NKS, int KSS, bool FIRST, int KH>
DI void pj_body(const Ctx& c, unsigned abase, unsigned wbase, int fc, u32x2 (&res)[3]) {
    using P = PJ<NKS, KSS>;
    constexpr int NS = P::NS, NG = NKS * 4, NA = P::NA, HK = KSS / 2, NCT = KH ? 2 : 3, CT0 = KH ? 3 : 0;
    const int lane = c.lane, wid = c.wid, t16l = wid & 3, r = lane & 15, gq = lane >> 4, pp = pj_pp(c.b), cc = pj_cc(c.b);
    lptr lds = c.lds;
    const unsigned t16 = 4 * pp + t16l;
    const int tok = 16 * t16 + r;
    constexpr int WIN = imin(NA, FIRST ? 4 : 9);
    s16x8 a[NA];
    const unsigned aoff = abase + (t16 * NG + 4 * KH) * 256 + lane * 16;
#define PJ_ABASE(j) (c.ws + (j) * 2048)
#pragma unroll
    for (int j = 0; j < WIN; ++j) ldg_async(a[j], PJ_ABASE(j), aoff);
    if (FIRST) {
#pragma unroll
        for (int i = 0; i < NCT; ++i) res[i] = *(const GAS u32x2*)(c.x + (size_t)tok * 1280 + cc * 160 + (CT0 + i) * 32 + gq * 8);
    }
    constexpr int NCS = FIRST && KH ? 20 : 0;
    unsigned csv[NCS ? NCS : 1];
    const int cst = wid * 64 + lane - 256;
    if (NCS) { const int mat = cst >= 112, col = imin(fc * 112 + imin(cst, 223) - 112 * mat, F - 1);
#pragma unroll
        for (int i = 0; i < NCS; ++i) csv[i] = lg32(c.rs, WS_CSP + (unsigned)((mat * 20 + i) * F + col) * 4); }
    unsigned voff[P::NWI]; pj_voff<NKS, KSS>(c, wbase, cc, voff);
    constexpr int NRES = (FIRST ? NCT : 0) + NCS;
    WAIT_BARV(WIN + NRES);
    const unsigned wa = KH * 1024 + lane * 16;
#define PJ_READ(W, st, i) do { lcptr b_ = (lcptr)lds + ((st) % PJ_R) * P::SLOT + wa + (i) * 2048; \
        _Pragma("unroll") for (int ct = 0; ct < 5; ++ct) W[ct] = ld128(b_ + ct * KSS * 1024); } while (0)
    f32x4 acc[5];
#pragma unroll
    for (int ct = 0; ct < 5; ++ct) acc[ct] = (f32x4){};
    s16x8 w[2][5];
    PJ_READ(w[0], 0, 0);
#pragma unroll
    for (int s = 0; s < NS; ++s) {
        if (NS > PJ_R && s >= 1) {
            if (s + 1 >= PJ_R && s + 1 < NS) WAIT_BAR((pj_bar_younger<NA, WIN, HK, NS, P::NWI>(s))); else LDS_BAR();
            SBAR();
            if (s + PJ_R - 1 < NS) pj_issue_w<NKS, KSS>(c, voff, s + PJ_R - 1);
        }
#pragma unroll
        for (int i = 0; i < HK; ++i) {
            const int j = s * HK + i;
            if (i + 1 < HK) PJ_READ(w[(j + 1) & 1], s, i + 1); else if (s + 1 < NS) PJ_READ(w[(j + 1) & 1], s + 1, 0);
            if (j + WIN < NA) ldg_async(a[j + WIN], PJ_ABASE(j + WIN), aoff);
            WAIT_USE((pj_younger<NA, WIN, HK, NS, P::NWI, NRES>(j)), a[j]);
            SBAR();
#pragma unroll
            for (int ct = 0; ct < 5; ++ct) acc[ct] = MFMA16(w[j & 1][ct], a[j], acc[ct]);
            SBAR();
        }
    }
#undef PJ_READ
#undef PJ_ABASE
    if (NCS) { float s = 0.f;
#pragma unroll
        for (int i = 0; i < NCS; ++i) s += __uint_as_float(csv[i]);
        if (cst < 224) *(LAS float*)(lds + P4_CS + ((cst >= 112) * 128 + cst - 112 * (cst >= 112)) * 4) = s; }
    LDS_BAR();
    LAS f32x4* red = (LAS f32x4*)(lds + XCH);
#pragma unroll
    for (int i = 0; i < 5 - NCT; ++i) red[(wid * 3 + i) * 64 + lane] = acc[(KH ? 0 : 3) + i];
    LDS_BAR();
    float s1 = 0.f, s2 = 0.f;
#pragma unroll
    for (int i = 0; i < NCT; ++i) {
        const f32x4 o = red[((wid ^ 4) * 3 + i) * 64 + lane], m = acc[CT0 + i];
        const unsigned w0 = cvtpk(bflo(res[i].x) + m[0] + o[0], bfhi(res[i].x) + m[1] + o[1]), w1 = cvtpk(bflo(res[i].y) + m[2] + o[2], bfhi(res[i].y) + m[3] + o[3]);
        if (FIRST) {
            res[i] = (u32x2){w0, w1};
            st64(c.rs, WS_H + (t16 * 80 + 10 * cc + 2 * (CT0 + i) + (gq >> 1)) * 256 + r * 16 + (gq & 1) * 8, res[i]);
            const float a_ = bflo(w0), b_ = bfhi(w0), c_ = bflo(w1), d = bfhi(w1); s1 += (a_ + b_) + (c_ + d); s2 += (a_ * a_ + b_ * b_) + (c_ * c_ + d * d);
        } else {
            *(GAS u32x2*)(c.out + (size_t)tok * 1280 + cc * 160 + (CT0 + i) * 32 + gq * 8) = (u32x2){w0, w1};
        }
    }
    if (FIRST) {
        s1 += __shfl_xor(s1, 16); s2 += __shfl_xor(s2, 16); s1 = swap32_add(s1); s2 = swap32_add(s2);
        LAS f32x2* ex = (LAS f32x2*)(lds + XCH + 2048);
        if (KH == 1 && gq == 0) ex[t16l * 16 + r] = (f32x2){s1, s2};
        LDS_BAR();
        if (KH == 0 && gq == 0) { const f32x2 e = ex[t16l * 16 + r];
            st64(c.rs, WS_HST + ((t16 * 8 + cc) * 16 + r) * 8, (u32x2){__float_as_uint(s1 + e.x), __float_as_uint(s2 + e.y)}); }
    }
}
template <int NKS, int KSS, bool FIRST>
DI void phase_proj(const Ctx& c, unsigned abase, unsigned wbase, int fc, u32x2 (&res)[3]) {
    if (c.wid < 4) pj_body<NKS, KSS, FIRST, 0>(c, abase, wbase, fc, res);
    else pj_body<NKS, KSS, FIRST, 1>(c, abase, wbase, fc, res);
}

constexpr int P4_R = 7, P4_SLOT = 22528, P4_NS = 20;

DI void p4_wvoff(const Ctx& c, int fc, unsigned& woff0, unsigned& woff1) {
    const unsigned o0 = c.wid * 1792 + c.lane * 16, o1 = c.wid * 1792 + 1024 + (c.lane < 48 ? c.lane : 47) * 16;
    woff0 = WS_W13P + (unsigned)imin(7 * fc + (o0 >> 11), 107) * 40960 + (o0 & 2047); woff1 = WS_W13P + (unsigned)imin(7 * fc + (o1 >> 11), 107) * 40960 + (o1 & 2047);
}
DI void p4_issue_w(const Ctx& c, unsigned woff0, unsigned woff1, int st) {
    const char* w_ = c.ws + st * 2048; const unsigned wdst = c.lds0 + 8192 + c.wid * 1792 + (st % P4_R) * P4_SLOT;
    dma16<true>(w_, woff0, wdst); dma16m<true>(w_, woff1, wdst + 1024, 0xffffffffffffull);
}
DI void p4_prefetch(const Ctx& c, int fc) {
    unsigned woff0, woff1; p4_wvoff(c, fc, woff0, woff1);
#pragma unroll
    for (int s = 0; s < P4_R - 1; ++s) p4_issue_w(c, woff0, woff1, s);
}
constexpr int p4_extra(int s) { int n = 0; for (int t = s - 5 < 0 ? 0 : s - 5; t < s; ++t) if (t <= 14 && t % 3 == 2) ++n; return n; }
template <int UG>
DI void p4_body(const Ctx& c, int qb, int fc) {
    constexpr int NU = UG ? 3 : 4, U0 = UG ? 4 : 0;
    const int lane = c.lane, wid = c.wid, tt = wid & 3, r = lane & 31, h = lane >> 5;
    lptr lds = c.lds;
    const unsigned adst = c.lds0 + wid * 1024;
    const unsigned aoff = WS_H + (unsigned)(8 * qb + wid) * 80 * 256 + lane * 16;
    unsigned woff0, woff1; p4_wvoff(c, fc, woff0, woff1);
#pragma unroll
    for (int s = 0; s < P4_R - 1; ++s) dma16<true>(c.ws + s * 1024, aoff, adst + s * P4_SLOT);
    const unsigned xa = ((2 * tt + (r >> 4)) * 4 + h) * 256 + (r & 15) * 16, wa = 8192 + U0 * 2048 + lane * 16;
    const unsigned t16 = 8 * qb + 2 * tt + (r >> 4);
    u32x2 hs[4];
#pragma unroll
    for (int i = 0; i < 4; ++i) hs[i] = lg64(c.rs, WS_HST + ((t16 * 8 + 4 * h + i) * 16 + (r & 15)) * 8);
#define P4_PIECE(st, k) do { const unsigned sl_ = ((st) % P4_R) * P4_SLOT; \
        if ((k) == 0) dma16<true>(c.ws + (st) * 1024, aoff, adst + sl_); \
        if ((k) == 1) dma16<true>(c.ws + (st) * 2048, woff0, c.lds0 + 8192 + wid * 1792 + sl_); \
        if ((k) == 2) dma16m<true>(c.ws + (st) * 2048, woff1, c.lds0 + 8192 + wid * 1792 + sl_ + 1024, 0xffffffffffffull); } while (0)
#define P4_READ(n) do { lcptr b_ = (lcptr)lds + (((n) >> 1) % P4_R) * P4_SLOT; X[(n) % 3] = ld128(b_ + xa + ((n) & 1) * 512); \
        _Pragma("unroll") for (int u = 0; u < NU; ++u) W[(n) % 3][u] = ld128(b_ + wa + u * 2048 + ((n) & 1) * 1024); } while (0)
    f32x16 acc[NU];
#pragma unroll
    for (int u = 0; u < NU; ++u) acc[u] = (f32x16){};
    WAIT_BAR((P4_R - 2) + 4);
    s16x8 X[3], W[3][NU];
    P4_READ(0); P4_READ(1);
#pragma unroll
    for (int s = 0; s < P4_NS; ++s) {
        WAIT_BARV(fly2(s, P4_R, P4_NS, 1, 2) + (s + 1 < P4_R - 1 ? 4 : 0) + p4_extra(s));
        SBAR();
#define P4_NEXT(k) do { if (s + P4_R - 1 < P4_NS) P4_PIECE(s + P4_R - 1, k); SBAR(); } while (0)
#pragma unroll
        for (int ks = 0; ks < 2; ++ks) {
            const int n = 2 * s + ks;
            if (n + 2 < 2 * P4_NS) P4_READ(n + 2);
            SBAR();
            acc[0] = MFMA32(W[n % 3][0], X[n % 3], acc[0]); SBAR(); P4_NEXT(ks);
            acc[1] = MFMA32(W[n % 3][1], X[n % 3], acc[1]); acc[2] = MFMA32(W[n % 3][2], X[n % 3], acc[2]); SBAR();
            if (ks) P4_NEXT(2);
            if (NU > 3) acc[NU - 1] = MFMA32(W[n % 3][NU - 1], X[n % 3], acc[NU - 1]);
            SBAR();
        }
#undef P4_NEXT
        if (s <= 14 && s % 3 == 2) {
            const int L = imin(((c.b >> 3) * 8 + wid) * 5 + s / 3, 1079);
            dma16<true>(c.ws, WS_W2P + (c.b & 1) * 1105920 + L * 1024 + lane * 16, c.lds0 + P4_R * P4_SLOT + 1024); SBAR(); }
    }
#undef P4_READ
    LDS_BAR();
    float s1 = (__uint_as_float(hs[0].x) + __uint_as_float(hs[1].x)) + (__uint_as_float(hs[2].x) + __uint_as_float(hs[3].x));
    float s2 = (__uint_as_float(hs[0].y) + __uint_as_float(hs[1].y)) + (__uint_as_float(hs[2].y) + __uint_as_float(hs[3].y));
    s1 = swap32_add(s1); s2 = swap32_add(s2);
    const float mean = s1 * (1.0f / D), var = fmaxf(s2 * (1.0f / D) - mean * mean, 0.f), rstd = __builtin_amdgcn_rsqf(var + LN_EPS);
    const f32x2 nm = {-mean, -mean}, k1 = {-1.4426950408889634f * rstd, -1.4426950408889634f * rstd}, r2 = {rstd * rstd, rstd * rstd}, one = {1.0f, 1.0f};
    const LAS float* csl = (const LAS float*)(lds + P4_CS);
#pragma unroll
    for (int u = 0; u < NU; ++u) {
        if (7 * fc + U0 + u < 108) {
            u32x2 w[2];
#pragma unroll
            for (int gq = 0; gq < 2; ++gq) {
                const f32x4 cg_ = *(const LAS f32x4*)(csl + (U0 + u) * 16 + 8 * gq + 4 * h), cu = *(const LAS f32x4*)(csl + 128 + (U0 + u) * 16 + 8 * gq + 4 * h);
                f32x2 o[2];
#pragma unroll
                for (int e = 0; e < 2; ++e) {
                    const f32x2 a = __builtin_elementwise_fma(nm, (f32x2){cg_[2 * e], cg_[2 * e + 1]}, (f32x2){acc[u][4 * gq + 2 * e], acc[u][4 * gq + 2 * e + 1]});
                    const f32x2 b2 = __builtin_elementwise_fma(nm, (f32x2){cu[2 * e], cu[2 * e + 1]}, (f32x2){acc[u][8 + 4 * gq + 2 * e], acc[u][8 + 4 * gq + 2 * e + 1]});
                    const f32x2 t = a * k1, d = (f32x2){__builtin_amdgcn_exp2f(t.x), __builtin_amdgcn_exp2f(t.y)} + one;
                    const f32x2 rc = {__builtin_amdgcn_rcpf(d.x), __builtin_amdgcn_rcpf(d.y)};
                    o[e] = (a * b2) * (rc * r2);
                }
                w[gq] = (u32x2){cvtpk(o[0].x, o[0].y), cvtpk(o[1].x, o[1].y)};
            }
            auto x0 = __builtin_amdgcn_permlane32_swap(w[0].x, w[1].x, false, false), x1 = __builtin_amdgcn_permlane32_swap(w[0].y, w[1].y, false, false);
            st128(c.rs, WS_HM + (t16 * 216 + 14 * fc + 2 * (U0 + u) + h) * 256 + (r & 15) * 16, (u32x4){x0[0], x1[0], x0[1], x1[1]});
        }
    }
}
DI void phase4(const Ctx& c, int qb, int fc) {
    if (c.wid < 4) p4_body<0>(c, qb, fc); else p4_body<1>(c, qb, fc);
}
__global__ __launch_bounds__(512, 2) void block_fwd(const char* __restrict__ x, const char* __restrict__ wqkv, const char* __restrict__ wo, const char* __restrict__ w1,
                                                    const char* __restrict__ w3, const char* __restrict__ w2, char* __restrict__ out, char* __restrict__ ws) {
    extern __shared__ __attribute__((aligned(16))) char smem[];
    {
        unsigned long long pc; asm volatile("s_getpc_b64 %0" : "=s"(pc));
        const unsigned wv = __builtin_amdgcn_readfirstlane(threadIdx.x >> 6), piece = ((blockIdx.x >> 3) * 8 + wv) * 1024;
        dma16<false>((const char*)(pc & ~1023ull), (piece < CODE_BYTES ? piece : CODE_BYTES - 1024) + (threadIdx.x & 63) * 16, (unsigned)(uintptr_t)smem + 4 * P1_SLOT + wv * 2048);
    }
    if ((blockIdx.x >> 3) == 31 - (threadIdx.x >> 6)) {
        const int l = threadIdx.x & 63;
        const char* a = l < 48 ? ws + ((size_t)l << 20) : l < 51 ? out + ((size_t)(l - 48) << 20) : l < 54 ? w1 + ((size_t)(l - 51) << 20) : l < 57 ? w3 + ((size_t)(l - 54) << 20)
                      : l < 60 ? w2 + ((size_t)(l - 57) << 20) : wo;
        asm volatile("s_mov_b32 m0, %1\n\ts_nop 0\n\tglobal_load_lds_dword %0, off" :: "v"(a), "s"((unsigned)(uintptr_t)smem + 4 * P1_SLOT + __builtin_amdgcn_readfirstlane(threadIdx.x >> 6) * 2048) : "memory");
    }
    Ctx c; c.x = x; c.wqkv = wqkv; c.wo = wo; c.w1 = w1; c.w3 = w3; c.w2 = w2; c.out = out; c.ws = ws;
    c.rs = __builtin_amdgcn_make_buffer_rsrc(ws, 0, WS_END, 0x00020000);
    c.lds = (lptr)smem; c.lds0 = (unsigned)(uintptr_t)smem; c.b = blockIdx.x; c.lane = threadIdx.x & 63; c.wid = __builtin_amdgcn_readfirstlane(threadIdx.x >> 6);
    const int lane = c.lane, g = c.b & 7, idx = c.b >> 3;
    c.tag = *(const __attribute__((address_space(4))) unsigned*)(ws + WS_E);
    u32x2 res[3] = {(u32x2){0u, 0u}, (u32x2){0u, 0u}, (u32x2){0u, 0u}};
    const int qb4 = 8 * (g & 1) + (idx & 7), fc = 4 * (g >> 1) + (idx >> 3), pp = pj_pp(c.b);

    phase1(c);
    WAIT_VM(0);
    publish(c, FL_KV);
    repack_store(c);

    { const int hh = 4 * g + (idx & 3), qb = idx >> 2;
      const int wg = lane < 16 ? wg_p1(lane, (32 + hh) / 6) : lane < 32 ? wg_p1(lane - 16, (64 + hh) / 6) : wg_p1(2 * qb + (lane & 1), hh / 6);
      const unsigned off[1] = {flag_off(FL_KV, wg)}; wait_flags(c, off); }
    phase2(c);
    WAIT_VM(0);
    publish(c, FL_O);
    pj_prefetch<20, 4>(c, WS_WOP);

    { const unsigned off[1] = {flag_off(FL_O, wg_p2(lane & 31, pp >> 2))}; wait_flags(c, off); }
    phase_proj<20, 4, true>(c, WS_O, WS_WOP, fc, res);
    WAIT_VM(0);
    publish(c, FL_H);
    p4_prefetch(c, fc);

    { const unsigned off[1] = {flag_off(FL_H, wg_p3(2 * qb4 + (lane & 1), (lane >> 1) & 7))}; wait_flags(c, off); }
    phase4(c, qb4, fc);
    WAIT_VM(0);
    publish(c, FL_M);
    pj_prefetch<54, 6>(c, WS_W2P);

    { const unsigned fm = flag_off(FL_M, wg_p4(pp >> 1, lane & 15));
      if (c.b == 0) {
          const unsigned off[5] = {fm, flag_off(FL_O, lane), flag_off(FL_O, 64 + lane), flag_off(FL_O, 128 + lane), flag_off(FL_O, 192 + lane)}; wait_flags(c, off);
      } else { const unsigned off[1] = {fm}; wait_flags(c, off); } }
    phase_proj<54, 6, false>(c, WS_HM, WS_W2P, fc, res);
    if (c.b == 0 && threadIdx.x == 0) st32(c.rs, WS_E, c.tag);
}

}

extern "C" void kernel_launch(void* const* d_in, const int* in_sizes, int n_in, void* d_out, int out_size, void* d_ws, size_t ws_size, hipStream_t stream) {
    using namespace blk;
    const char* x = (const char*)d_in[0]; const char* wqkv = (const char*)d_in[1]; const char* wo = (const char*)d_in[2];
    const char* w1 = (const char*)d_in[4]; const char* w3 = (const char*)d_in[5]; const char* w2 = (const char*)d_in[6];
    char* ws = (char*)d_ws; char* out = (char*)d_out;
    hipFuncSetAttribute((const void*)block_fwd, hipFuncAttributeMaxDynamicSharedMemorySize, LDS_BYTES);
    block_fwd<<<256, 512, LDS_BYTES, stream>>>(x, wqkv, wo, w1, w3, w2, out, ws);
}
```

```cpp
#include <hip/hip_runtime.h>
#include <hip/hip_bf16.h>
#include <stdint.h>

namespace blk {

constexpr int S = 2048, D = 640, H = 32, F = 1728;
constexpr float LN_EPS = 1e-5f;
constexpr float QSCALE = 0.22360679774997896f * 1.4426950408889634f;

#define LAS __attribute__((address_space(3)))
#define GAS __attribute__((address_space(1)))
#define DI __device__ __forceinline__
typedef short s16x8 __attribute__((ext_vector_type(8)));
typedef short s16x4 __attribute__((ext_vector_type(4)));
typedef float f32x16 __attribute__((ext_vector_type(16)));
typedef float f32x4 __attribute__((ext_vector_type(4)));
typedef float f32x2 __attribute__((ext_vector_type(2)));
typedef unsigned u32x4 __attribute__((ext_vector_type(4)));
typedef unsigned u32x2 __attribute__((ext_vector_type(2)));
typedef __bf16 bf16x2 __attribute__((ext_vector_type(2)));
typedef LAS char* lptr;
typedef LAS const char* lcptr;
typedef __amdgpu_buffer_rsrc_t rsrc_t;

constexpr unsigned WS_E = 0;
constexpr unsigned WS_FLAG = 4096;
constexpr unsigned WS_ROWP = 256u << 10;
constexpr unsigned WS_COLP = 512u << 10;
constexpr unsigned WS_Q = 1u << 20;
constexpr unsigned WS_KV = 4u << 20;
constexpr unsigned WS_O = 12u << 20;
constexpr unsigned WS_H = 16u << 20;
constexpr unsigned WS_HST = 20u << 20;
constexpr unsigned WS_CSP = 21u << 20;
constexpr unsigned WS_HM = 24u << 20;
constexpr unsigned WS_W13P = 32u << 20;
constexpr unsigned WS_W2P = 40u << 20;
constexpr unsigned WS_WOP = 44u << 20;
constexpr unsigned WS_END = 48u << 20;
enum { FL_KV = 0, FL_PK = 1, FL_O = 2, FL_H = 3, FL_M = 4 };
constexpr int LDS_BYTES = 163840;
constexpr unsigned SPIN_CAP = 1u << 16;
constexpr unsigned CODE_BYTES = 64 * 1024;

#define SBAR() __builtin_amdgcn_sched_barrier(0)
#define PIN(x) asm volatile("" : "+v"(x))
#define MFMA32(a, b, c) __builtin_amdgcn_mfma_f32_32x32x16_bf16(a, b, c, 0, 0, 0)
#define MFMA32K8(a, b, c) __builtin_amdgcn_mfma_f32_32x32x8bf16_1k(a, b, c, 0, 0, 0)
#define MFMA16(a, b, c) __builtin_amdgcn_mfma_f32_16x16x32_bf16(a, b, c, 0, 0, 0)
#define WAIT_BAR(N) asm volatile("s_waitcnt vmcnt(%0) lgkmcnt(0)\n\ts_barrier" :: "i"(N) : "memory")
#define WAIT_BARV(N) asm volatile("s_waitcnt vmcnt(%0)\n\ts_barrier" :: "i"(N) : "memory")
#define WAIT_VM(N) asm volatile("s_waitcnt vmcnt(%0)" :: "i"(N) : "memory")
#define LDS_BAR() asm volatile("s_waitcnt lgkmcnt(0)\n\ts_barrier" ::: "memory")

template <bool SC> DI void dma16(const void* sbase, unsigned voff, unsigned ldsaddr) {
    if (SC) asm volatile("s_mov_b32 m0, %2\n\ts_nop 0\n\tglobal_load_lds_dwordx4 %0, %1 sc1" :: "v"(voff), "s"(sbase), "s"(ldsaddr) : "memory");
    else asm volatile("s_mov_b32 m0, %2\n\ts_nop 0\n\tglobal_load_lds_dwordx4 %0, %1" :: "v"(voff), "s"(sbase), "s"(ldsaddr) : "memory");
}
template <bool SC> DI void dma16m(const void* sbase, unsigned voff, unsigned ldsaddr, unsigned long long mask) {
    if (SC) asm volatile("s_mov_b32 m0, %2\n\ts_mov_b64 exec, %3\n\tglobal_load_lds_dwordx4 %0, %1 sc1\n\ts_mov_b64 exec, -1" :: "v"(voff), "s"(sbase), "s"(ldsaddr), "s"(mask) : "memory");
    else asm volatile("s_mov_b32 m0, %2\n\ts_mov_b64 exec, %3\n\tglobal_load_lds_dwordx4 %0, %1\n\ts_mov_b64 exec, -1" :: "v"(voff), "s"(sbase), "s"(ldsaddr), "s"(mask) : "memory");
}
constexpr int imin(int a, int b) { return a < b ? a : b; }
constexpr int imax(int a, int b) { return a > b ? a : b; }
constexpr int fly(int s, int R, int NS) { return imax(0, imin(R - 3, NS - 2 - s)); }
constexpr int fly2(int s, int R, int NS, int na, int nw) {
    const int need = imin(s + 1, NS - 1);
    const int pro = imin(R - 1, NS);
    const int issued = pro * na + imax(0, imin(s, NS - R + 1)) * (na + nw);
    const int endpos = need < pro ? (need + 1) * na : pro * na + (need - pro + 1) * (na + nw);
    return imax(0, issued - endpos);
}
DI int rot(int i, int rho, int ns) { const int t = i + rho; return t >= ns ? t - ns : t; }
DI unsigned cvtpk(float lo, float hi) { f32x2 v = {lo, hi}; return __builtin_bit_cast(unsigned, __builtin_convertvector(v, bf16x2)); }
DI float bflo(unsigned w) { return __uint_as_float(w << 16); }
DI float bfhi(unsigned w) { return __uint_as_float(w & 0xffff0000u); }
DI s16x8 ld128(lcptr p) { return *(const LAS s16x8*)p; }
DI s16x4 ld64(lcptr p) { return *(const LAS s16x4*)p; }
DI s16x4 ldtr(lcptr p) { return __builtin_bit_cast(s16x4, __builtin_amdgcn_ds_read_tr16_b64_v4i16((LAS s16x4*)p)); }
DI s16x8 cat(s16x4 a, s16x4 b) { return __builtin_shufflevector(a, b, 0, 1, 2, 3, 4, 5, 6, 7); }
DI f32x4 sel4(unsigned m, f32x4 a, f32x4 b) { const u32x4 x = __builtin_bit_cast(u32x4, a), y = __builtin_bit_cast(u32x4, b); return __builtin_bit_cast(f32x4, (x & m) | (y & ~m)); }
DI float swap32_add(float v) { auto r = __builtin_amdgcn_permlane32_swap(__float_as_uint(v), __float_as_uint(v), false, false); return __uint_as_float(r[0]) + __uint_as_float(r[1]); }
DI float swap32_max(float v) { auto r = __builtin_amdgcn_permlane32_swap(__float_as_uint(v), __float_as_uint(v), false, false); return fmaxf(__uint_as_float(r[0]), __uint_as_float(r[1])); }
DI float upper_half(float v) { auto r = __builtin_amdgcn_permlane32_swap(__float_as_uint(v), __float_as_uint(v), false, false); return __uint_as_float(r[1]); }
DI void st128(rsrc_t rs, unsigned off, u32x4 v) { __builtin_amdgcn_raw_buffer_store_b128(v, rs, off, 0, 16); }
DI void st64(rsrc_t rs, unsigned off, u32x2 v) { __builtin_amdgcn_raw_buffer_store_b64(v, rs, off, 0, 16); }
DI void st32(rsrc_t rs, unsigned off, unsigned v) { __builtin_amdgcn_raw_buffer_store_b32(v, rs, off, 0, 16); }
DI u32x4 lg128(rsrc_t rs, unsigned off) { return __builtin_amdgcn_raw_buffer_load_b128(rs, off, 0, 16); }
DI u32x2 lg64(rsrc_t rs, unsigned off) { return __builtin_amdgcn_raw_buffer_load_b64(rs, off, 0, 16); }
DI unsigned lg32(rsrc_t rs, unsigned off) { return __builtin_amdgcn_raw_buffer_load_b32(rs, off, 0, 16); }

DI void ldg_async(s16x8& dst, const void* sbase, unsigned voff) { asm volatile("global_load_dwordx4 %0, %1, %2 sc1" : "=v"(dst) : "v"(voff), "s"(sbase) : "memory"); }
#define WAIT_USE(N, x) asm volatile("s_waitcnt vmcnt(%1)" : "+v"(x) : "i"(N) : "memory")
struct Ctx { const char *x, *wqkv, *wo, *w1, *w3, *w2; char *out, *ws; rsrc_t rs; lptr lds; unsigned lds0, tag; int b, lane, wid; };

DI unsigned flag_off(int kind, int wg) { return WS_FLAG + (unsigned)(kind * 256 + wg) * 128u; }
DI void publish(const Ctx& c, int kind) {
    asm volatile("s_barrier" ::: "memory");
    if (threadIdx.x == 0) st32(c.rs, flag_off(kind, c.b), c.tag);
}
template <int N> DI void wait_flags(const Ctx& c, const unsigned (&off)[N]) {
    if (c.wid == 0) {
        for (unsigned spins = 0; spins < SPIN_CAP; ++spins) {
            unsigned v[N];
#pragma unroll
            for (int i = 0; i < N; ++i) v[i] = lg32(c.rs, off[i]);
            bool ok = true;
#pragma unroll
            for (int i = 0; i < N; ++i) ok &= v[i] == c.tag;
            if (__all(ok)) break;
            __builtin_amdgcn_s_sleep(1);
        }
    }
    asm volatile("s_waitcnt lgkmcnt(0)\n\ts_barrier" ::: "memory");
}
DI int wg_p1(int pb, int nb) { return ((pb >> 3) | ((nb >> 2) << 1)) + 8 * ((pb & 7) + 8 * (nb & 3)); }
DI int wg_p2(int hh, int qb) { return (hh >> 2) + 8 * ((hh & 3) + 4 * qb); }
DI int wg_p3(int pp, int cc) { return (((pp >> 3) << 1) | (cc >> 2)) + 8 * ((pp & 7) + 8 * (cc & 3)); }
DI int wg_p4(int qb, int fc) { return ((qb >> 3) | ((fc >> 2) << 1)) + 8 * ((qb & 7) + 8 * (fc & 3)); }

constexpr int P1_R = 5, P1_SLOT = 32768, P1_NS = 10;
constexpr int P1_STG = 264;
constexpr int P1_OUT = 65536;
constexpr int P1_ST = P1_OUT + 40960;
constexpr int P1_CS = P1_ST + 1024;

DI void repack_issue_one(const Ctx& c, int i);
template <int UP>
DI void p1_loop(Ctx& c, unsigned xoff, unsigned woff, int tt, int rho, unsigned put, unsigned get, f32x16& acc0, f32x16& acc1, u32x2 (&part)[10]) {
    f32x16 accx = {};
    const int lane = c.lane, wid = c.wid, r = lane & 31, h = lane >> 5, q = (lane >> 2) & 3, p = lane & 3, cg = (lane >> 4) & 1;
    constexpr unsigned long long WMASK = ~((1ull << 15) | (1ull << 27) | (1ull << 39) | (1ull << 51));
    const unsigned xdst = c.lds0 + wid * 2048, wdst = c.lds0 + 16384 + wid * 2048;
    const int xrow = 32 * tt + r;
    const unsigned xa = xrow * 128 + ((h ^ ((xrow >> 1) & 7)) * 16);
    const unsigned wk = 16384 + (8 * h + q) * 256 + (p & 1) * 8;
    const unsigned wa0 = wk + (((4 * (2 * UP) + 2 * cg + (p >> 1)) ^ (4 * q)) * 16);
    const unsigned wa1 = wk + (((4 * (2 * UP + 1) + 2 * cg + (p >> 1)) ^ (4 * q)) * 16);
    const unsigned waz = wk + (((4 * tt + 2 * cg + (p >> 1)) ^ (4 * q)) * 16);
    const s16x8 ones = {0x3f80, 0x3f80, 0x3f80, 0x3f80, 0x3f80, 0x3f80, 0x3f80, 0x3f80};
#define P1_PIECE(st, slot, k) do { const int p_ = rot(st, rho, P1_NS); \
        if ((k) == 0) dma16<false>(c.x + p_ * 128, xoff, xdst + (slot)); \
        if ((k) == 1) dma16<false>(c.x + p_ * 128 + 8 * 1280, xoff ^ 64, xdst + (slot) + 1024); \
        if ((k) == 2) dma16m<false>(c.wqkv + (size_t)p_ * (64 * 3840), woff, wdst + (slot), WMASK); \
        if ((k) == 3) dma16m<false>(c.wqkv + (size_t)p_ * (64 * 3840) + 4 * 3840, woff, wdst + (slot) + 1024, WMASK); } while (0)
#define P1_ISSUE(st, slot) do { P1_PIECE(st, slot, 0); P1_PIECE(st, slot, 1); P1_PIECE(st, slot, 2); P1_PIECE(st, slot, 3); } while (0)
#define P1_READ(n) do { lcptr b_ = (lcptr)c.lds + (((n) >> 2) % P1_R) * P1_SLOT; \
        X[(n) % 3] = ld128(b_ + (xa ^ (((n) & 3) * 32))); \
        W0[(n) % 3] = cat(ldtr(b_ + wa0 + ((n) & 3) * 4096), ldtr(b_ + wa0 + ((n) & 3) * 4096 + 1024)); \
        W1[(n) % 3] = cat(ldtr(b_ + wa1 + ((n) & 3) * 4096), ldtr(b_ + wa1 + ((n) & 3) * 4096 + 1024)); \
        if (UP && (n) < 4) WZ[(n) & 3] = cat(ldtr(b_ + waz + ((n) & 3) * 4096), ldtr(b_ + waz + ((n) & 3) * 4096 + 1024)); } while (0)
#define P1_MMA(n) do { acc0 = MFMA32(W0[(n) % 3], X[(n) % 3], acc0); acc1 = MFMA32(W1[(n) % 3], X[(n) % 3], acc1); \
        if ((n) < 4) { if (UP) accx = MFMA32(WZ[(n) & 3], ones, accx); else accx = MFMA32(X[(n) % 3], X[(n) % 3], accx); } } while (0)
    P1_ISSUE(0, 0); P1_ISSUE(1, P1_SLOT); P1_ISSUE(2, 2 * P1_SLOT);
    WAIT_BAR(8);
    asm volatile("" : "+s"(c.tag));
    c.tag += 1u;
    P1_ISSUE(3, 3 * P1_SLOT);
    s16x8 X[3] = {}, W0[3] = {}, W1[3] = {}, WZ[4] = {};
    P1_READ(0); P1_READ(1);
#pragma unroll
    for (int s = 0; s < P1_NS; ++s) {
        constexpr int NX = P1_R - 1, GS = P1_NS - P1_R;
        WAIT_BARV(4 * fly(s, P1_R, P1_NS) + (s >= 1 && s + 1 <= P1_R - 1 ? 1 : 0) + (s > GS && !UP ? 10 : 0) + imax(0, s - GS - 1));
        SBAR();
#pragma unroll
        for (int ks = 0; ks < 4; ++ks) {
            const int n = 4 * s + ks;
            if (n + 2 < 4 * P1_NS) P1_READ(n + 2);
            SBAR(); P1_MMA(n); SBAR();
            if (s + NX < P1_NS) P1_PIECE(s + NX, ((s + NX) % P1_R) * P1_SLOT, ks);
            if (s > GS && ks == 1) repack_issue_one(c, s - GS - 1);
            SBAR();
        }
        if (s == 0) {
            const int tr = (r & 3) + 4 * (r >> 3); float v = 0.f;
#pragma unroll
            for (int i = 0; i < 16; ++i) v = (tr == i) ? accx[i] : v;
            st64(c.rs, put + (h == ((r >> 2) & 1) ? (tt * 32 + r) * 8 : 0x7fffff00u), (u32x2){__float_as_uint(v), c.tag});
            SBAR();
        }
        if (s == GS && !UP) {
#pragma unroll
            for (int i = 0; i < 10; ++i) part[i] = lg64(c.rs, get + i * 1024);
            SBAR();
        }
    }
#undef P1_PIECE
#undef P1_ISSUE
#undef P1_READ
#undef P1_MMA
}

DI void phase1(Ctx& c) {
    const int lane = c.lane, wid = c.wid, tid = threadIdx.x, g = c.b & 7, idx = c.b >> 3;
    const int pb = 8 * (g & 1) + (idx & 7), nb = 4 * (g >> 1) + (idx >> 3);
    const int tt = wid & 3, up = wid >> 2, r = lane & 31, h = lane >> 5;
    lptr lds = c.lds;
    if (tid < P1_R * 64) { const int sl = tid >> 6, k = tid & 63;
        *(LAS u32x4*)(lds + sl * P1_SLOT + 16384 + k * 256 + ((15 ^ (4 * (k & 3))) * 16)) = (u32x4){0x3f803f80u, 0x3f803f80u, 0x3f803f80u, 0x3f803f80u}; }
    const int xr = 16 * wid + (lane >> 3), xc = (lane & 7) ^ ((xr >> 1) & 7);
    const unsigned xoff = (pb * 128 + xr) * 1280 + xc * 16;
    const int wkk = 8 * wid + (lane >> 4), wp = (lane & 15) ^ (4 * (wkk & 3)), wpc = wp < 15 ? wp : 14;
    const unsigned woff = wkk * 3840 + nb * 240 + wpc * 16;
    f32x16 acc0 = {}, acc1 = {};
    const int rho = (pb + 3 * nb) % P1_NS;
    const unsigned put = up ? WS_COLP + (unsigned)(nb * 16 + pb) * 1024 : WS_ROWP + (unsigned)(pb * 16 + nb) * 1024;
    const int gi = (wid & 1) * 64 + lane;
    const unsigned get = ((wid & 2) ? WS_COLP + (unsigned)nb * 16384 : WS_ROWP + (unsigned)pb * 16384) + gi * 8;
    u32x2 part[10];
    if (up == 0) p1_loop<0>(c, xoff, woff, tt, rho, put, get, acc0, acc1, part);
    else p1_loop<1>(c, xoff, woff, tt, rho, put, get, acc0, acc1, part);
    float psum = 0.f;
    if (up == 0) {
        for (unsigned spins = 0; spins < SPIN_CAP; ++spins) {
            bool ok = true;
#pragma unroll
            for (int i = 0; i < 10; ++i) ok &= part[i].y == c.tag;
            if (__all(ok)) break;
            __builtin_amdgcn_s_sleep(1);
#pragma unroll
            for (int i = 0; i < 10; ++i) part[i] = lg64(c.rs, get + i * 1024);
        }
#pragma unroll
        for (int i = 0; i < 10; ++i) psum += __uint_as_float(part[i].x);
    }
    LDS_BAR();
    LAS float* st = (LAS float*)(lds + P1_ST); LAS float* cs = (LAS float*)(lds + P1_CS);
    if (wid < 2) st[gi * 2 + 1] = psum; else if (wid < 4) cs[gi] = psum;
    if (up == 1 && h == 0) st[(tt * 32 + r) * 2] = acc1[12];
    LDS_BAR();
    { const f32x2 sv = *(const LAS f32x2*)(st + (tt * 32 + r) * 2);
      const float mean = sv.x * (1.0f / D), var = fmaxf(sv.y * (1.0f / D) - mean * mean, 0.f), rstd = __builtin_amdgcn_rsqf(var + LN_EPS), rm = rstd * mean;
      lptr stg = lds + P1_OUT + (32 * tt + r) * P1_STG;
#pragma unroll
      for (int u = 0; u < 2; ++u)
#pragma unroll
        for (int gq = 0; gq < 4; ++gq) {
            const int col = (2 * up + u) * 32 + 8 * gq + 4 * h;
            const f32x4 c4 = *(const LAS f32x4*)(cs + col);
            const float sc = (nb * 120 + col < D) ? QSCALE : 1.0f, a = rstd * sc, bb = rm * sc;
            float v[4];
#pragma unroll
            for (int e = 0; e < 4; ++e) v[e] = a * (u ? acc1[4 * gq + e] : acc0[4 * gq + e]) - bb * c4[e];
            *(LAS u32x2*)(stg + col * 2) = (u32x2){cvtpk(v[0], v[1]), cvtpk(v[2], v[3])};
        }
    }
    LDS_BAR();
#pragma unroll
    for (int j = 0; j < 3; ++j) {
        const int slot = up + 2 * j, gidx = 6 * nb + slot, type = gidx >> 5, head = gidx & 31, tile = 4 * pb + tt;
        if (type < 2) {
            lcptr row = (lcptr)lds + P1_OUT + (32 * tt + r) * P1_STG + slot * 40;
            const u32x2 a0 = *(const LAS u32x2*)(row + 16 * h), a1 = *(const LAS u32x2*)(row + 16 * h + 8), a2 = *(const LAS u32x2*)(row + 32);
            const unsigned dst = type ? WS_KV + (unsigned)(head * 64 + tile) * 3072 : WS_Q + (unsigned)(head * 64 + tile) * 1536;
            st128(c.rs, dst + lane * 16, (u32x4){a0.x, a0.y, a1.x, a1.y});
            st64(c.rs, dst + 1024 + lane * 8, h ? (u32x2){type ? 0x00003f80u : 0u, 0u} : a2);
        } else {
            const int q = (lane >> 2) & 3, p = lane & 3, cg = (lane >> 4) & 1;
            lcptr vb = (lcptr)lds + P1_OUT + (32 * tt + 4 * h + q) * P1_STG + slot * 40 + (cg * 4 + p < 5 ? (cg * 4 + p) * 8 : 0);
            const unsigned dst = WS_KV + (unsigned)(head * 64 + tile) * 3072 + 1536;
#pragma unroll
            for (int s = 0; s < 2; ++s) {
                const u32x2 a = __builtin_bit_cast(u32x2, ldtr(vb + (16 * s) * P1_STG)), e = __builtin_bit_cast(u32x2, ldtr(vb + (16 * s + 8) * P1_STG));
                u32x4 v = {a.x, a.y, e.x, e.y};
                if (r == 20) v = (u32x4){0x3f803f80u, 0x3f803f80u, 0x3f803f80u, 0x3f803f80u};
                if (r > 20) v = (u32x4){0u, 0u, 0u, 0u};
                if (r < 24) st128(c.rs, dst + s * 768 + h * 384 + r * 16, v);
            }
        }
    }
}

constexpr int PK_OFF = 0;
DI int pk_job(int b, int i) { const int j = b + 256 * i; return j < 910 ? j : j - 256; }
DI void repack_issue_one(const Ctx& c, int i) {
    const int row = 8 * c.wid + (c.lane >> 3), pc = c.lane & 7;
    const int j = pk_job(c.b, i);
    const char* src; unsigned off;
    if (j < 540) { const int jj = j < 270 ? j : j - 270, kb = jj / 27, cb = jj % 27; src = j < 270 ? c.w1 : c.w3; off = (64 * kb + row) * 3456 + cb * 128 + pc * 16; }
    else if (j < 810) { const int jj = j - 540, kb = jj / 10, cb = jj % 10; src = c.w2; off = (64 * kb + row) * 1280 + cb * 128 + pc * 16; }
    else { const int jj = j - 810, kb = jj / 10, cb = jj % 10, kp = 64 * kb + row, G = kp >> 3, e = kp & 7;
        const int k = G < 64 ? 20 * (G >> 1) + 8 * (G & 1) + e : 40 * (G - 64) + 16 + (e < 4 ? e : e + 16);
        src = c.wo; off = k * 1280 + cb * 128 + pc * 16; }
    dma16<false>(src, off, c.lds0 + PK_OFF + i * 8192 + c.wid * 1024);
}
DI void repack_store(const Ctx& c) {
    const int lane = c.lane, w = c.wid, g = lane >> 4, i16 = lane & 15, q = i16 >> 2, p = i16 & 3;
#pragma unroll
    for (int i = 0; i < 4; ++i) {
        const int j = pk_job(c.b, i);
        lcptr blk = (lcptr)c.lds + PK_OFF + i * 8192 + (w >> 1) * 32 + p * 8;
        if (j < 540) {
            const int mat = j >= 270, jj = mat ? j - 270 : j, kb = jj / 27, cb = jj % 27, ks = 2 * (w & 1) + (g >> 1), hh = g & 1;
            lcptr a = blk + (16 * ks + 8 * hh + q) * 128;
            const u32x2 v0 = __builtin_bit_cast(u32x2, ldtr(a)), v1 = __builtin_bit_cast(u32x2, ldtr(a + 512));
            st128(c.rs, WS_W13P + (unsigned)((4 * cb + (w >> 1)) * 40 + 4 * kb + ks) * 1024 + (mat * 16 + i16 + 32 * hh) * 16, (u32x4){v0.x, v0.y, v1.x, v1.y});
            float s = ((bflo(v0.x) + bfhi(v0.x)) + (bflo(v0.y) + bfhi(v0.y))) + ((bflo(v1.x) + bfhi(v1.x)) + (bflo(v1.y) + bfhi(v1.y)));
            s += __shfl_xor(s, 16); s += __shfl_xor(s, 32);
            if (lane < 16) st32(c.rs, WS_CSP + (unsigned)((mat * 20 + 2 * kb + (w & 1)) * F + 64 * cb + 16 * (w >> 1) + i16) * 4, __float_as_uint(s));
        } else {
            const bool isw2 = j < 810; const int jj = isw2 ? j - 540 : j - 810, kb = jj / 10, cb = jj % 10, ks = w & 1;
            lcptr a = blk + (32 * ks + 8 * g + q) * 128;
            const u32x2 v0 = __builtin_bit_cast(u32x2, ldtr(a)), v1 = __builtin_bit_cast(u32x2, ldtr(a + 512));
            const unsigned dst = isw2 ? WS_W2P + (unsigned)((4 * cb + (w >> 1)) * 54 + 2 * kb + ks) * 1024 : WS_WOP + (unsigned)((4 * cb + (w >> 1)) * 20 + 2 * kb + ks) * 1024;
            st128(c.rs, dst + lane * 16, (u32x4){v0.x, v0.y, v1.x, v1.y});
        }
    }
}

constexpr int P2_R = 4, P2_SLOT = 24576, P2_NS = 8;
constexpr int P2_Q = 110592;
constexpr int XCH = 135168;
constexpr int P2_EX = XCH;
constexpr int P4_CS = XCH + 24576;

DI void attn_safe(const Ctx& c, unsigned kv, s16x8 q0, s16x4 q1z, f32x16& o) {
    const int lane = c.lane, r = lane & 31, h = lane >> 5, vr = r < 24 ? r : r - 16;
    float m = -INFINITY; o = (f32x16){};
    for (int t = 0; t < 64; ++t) {
        const unsigned p = kv + t * 3072;
        const u32x4 k0 = lg128(c.rs, p + lane * 16); const u32x2 k1 = lg64(c.rs, p + 1024 + lane * 8);
        const u32x4 v0 = lg128(c.rs, p + 1536 + h * 384 + vr * 16), v1 = lg128(c.rs, p + 1536 + 768 + h * 384 + vr * 16);
        f32x16 s = MFMA32(__builtin_bit_cast(s16x8, k0), q0, (f32x16){}); s = MFMA32K8(__builtin_bit_cast(s16x4, k1), q1z, s);
        float tm = s[0];
#pragma unroll
        for (int i = 1; i < 16; ++i) tm = fmaxf(tm, s[i]);
        tm = swap32_max(tm);
        const float mn = fmaxf(m, tm), al = __builtin_amdgcn_exp2f(m - mn); m = mn;
#pragma unroll
        for (int i = 0; i < 16; ++i) { o[i] *= al; s[i] = __builtin_amdgcn_exp2f(s[i] - mn); }
        const u32x4 p0 = {cvtpk(s[0], s[1]), cvtpk(s[2], s[3]), cvtpk(s[4], s[5]), cvtpk(s[6], s[7])};
        const u32x4 p1 = {cvtpk(s[8], s[9]), cvtpk(s[10], s[11]), cvtpk(s[12], s[13]), cvtpk(s[14], s[15])};
        o = MFMA32(__builtin_bit_cast(s16x8, v0), __builtin_bit_cast(s16x8, p0), o); o = MFMA32(__builtin_bit_cast(s16x8, v1), __builtin_bit_cast(s16x8, p1), o);
    }
}

DI void phase2(const Ctx& c) {
    const int lane = c.lane, wid = c.wid, g = c.b & 7, idx = c.b >> 3, hh = 4 * g + (idx & 3), qb = idx >> 2;
    const int qg = wid & 3, kh = wid >> 2, r = lane & 31, h = lane >> 5;
    lptr lds = c.lds;
    const char* kvsrc = c.ws + WS_KV + (size_t)hh * 64 * 3072 + wid * 3072;
    const unsigned dst0 = c.lds0 + wid * 3072, voff = lane * 16;
#define P2_ISSUE(st, slot) do { const char* s_ = kvsrc + (size_t)(st) * 24576; dma16<true>(s_, voff, dst0 + (slot)); dma16<true>(s_ + 1024, voff, dst0 + (slot) + 1024); dma16<true>(s_ + 2048, voff, dst0 + (slot) + 2048); } while (0)
    { const char* qsrc = c.ws + WS_Q + ((size_t)hh * 64 + 8 * qb + wid) * 1536;
      dma16<true>(qsrc, voff, c.lds0 + P2_Q + wid * 1536); dma16m<true>(qsrc + 1024, voff, c.lds0 + P2_Q + wid * 1536 + 1024, 0xffffffffull); }
    P2_ISSUE(0, 0); P2_ISSUE(1, P2_SLOT);
    const int vr = r < 24 ? r : 21 + (r & 1);
    const unsigned ka = 4 * kh * 3072 + lane * 16, kb = 4 * kh * 3072 + 1024 + lane * 8, va = 4 * kh * 3072 + 1536 + h * 384 + vr * 16;
#define LDK0(slot, t) ld128((lcptr)lds + (slot) + ka + (t) * 3072)
#define LDK1(slot, t) ld64((lcptr)lds + (slot) + kb + (t) * 3072)
#define LDV0(slot, t) ld128((lcptr)lds + (slot) + va + (t) * 3072)
#define LDV1(slot, t) ld128((lcptr)lds + (slot) + va + (t) * 3072 + 768)
    WAIT_BAR(3);
    if (threadIdx.x == 0) st32(c.rs, flag_off(FL_PK, c.b), c.tag);
    P2_ISSUE(2, 2 * P2_SLOT);
    s16x8 q0[2]; s16x4 q1z[2], q1[2];
#pragma unroll
    for (int i = 0; i < 2; ++i) { q0[i] = ld128((lcptr)lds + P2_Q + (2 * qg + i) * 1536 + lane * 16); q1z[i] = ld64((lcptr)lds + P2_Q + (2 * qg + i) * 1536 + 1024 + lane * 8); }
    { const s16x8 k0 = ld128((lcptr)lds + lane * 16); const s16x4 k1 = ld64((lcptr)lds + 1024 + lane * 8);
#pragma unroll
      for (int i = 0; i < 2; ++i) { f32x16 s = MFMA32(k0, q0[i], (f32x16){}); s = MFMA32K8(k1, q1z[i], s);
          float m = s[0];
#pragma unroll
          for (int e = 1; e < 16; ++e) m = fmaxf(m, s[e]);
          m = swap32_max(m);
          q1[i] = q1z[i]; if (h) q1[i][0] = (short)(cvtpk(-m, 0.f) & 0xffffu); } }
    s16x8 kA0 = LDK0(0, 0), kB0, vA0 = LDV0(0, 0), vA1 = LDV1(0, 0), vB0, vB1 = vA1; s16x4 kA1 = LDK1(0, 0), kB1;
    f32x16 sA, sB, o[2] = {(f32x16){}, (f32x16){}};
    sA = MFMA32(kA0, q0[0], (f32x16){}); sA = MFMA32K8(kA1, q1[0], sA);
    u32x4 p0, p1 = {0u, 0u, 0u, 0u};
    int cur = 0, nxt = P2_SLOT, fre = (P2_R - 1) * P2_SLOT;
#define EX(v) __builtin_amdgcn_exp2f(v)
#define P2_STEP(C, N, K0, K1, NQ, OC, V0, OP, VP, LOADS, LAST) do { \
        OP = MFMA32(VP, __builtin_bit_cast(s16x8, p1), OP); SBAR(); \
        LOADS; SBAR(); \
        C[0] = EX(C[0]); C[1] = EX(C[1]); C[2] = EX(C[2]); C[3] = EX(C[3]); p0[0] = cvtpk(C[0], C[1]); p0[1] = cvtpk(C[2], C[3]); PIN(p0); SBAR(); \
        if (!(LAST)) N = MFMA32(K0, q0[NQ], (f32x16){}); SBAR(); \
        C[4] = EX(C[4]); C[5] = EX(C[5]); C[6] = EX(C[6]); C[7] = EX(C[7]); p0[2] = cvtpk(C[4], C[5]); p0[3] = cvtpk(C[6], C[7]); PIN(p0); SBAR(); \
        if (!(LAST)) N = MFMA32K8(K1, q1[NQ], N); SBAR(); \
        C[8] = EX(C[8]); C[9] = EX(C[9]); C[10] = EX(C[10]); C[11] = EX(C[11]); p1[0] = cvtpk(C[8], C[9]); p1[1] = cvtpk(C[10], C[11]); PIN(p1); SBAR(); \
        OC = MFMA32(V0, __builtin_bit_cast(s16x8, p0), OC); SBAR(); \
        C[12] = EX(C[12]); C[13] = EX(C[13]); C[14] = EX(C[14]); C[15] = EX(C[15]); p1[2] = cvtpk(C[12], C[13]); p1[3] = cvtpk(C[14], C[15]); PIN(p1); SBAR(); \
    } while (0)
#define P2_PIECE(st, k) do { if ((st) >= 0) dma16<true>(kvsrc + (size_t)(st) * 24576 + (k) * 1024, voff, dst0 + fre + (k) * 1024); } while (0)
#define P2_PAIR(T, NS_, NT, LAST, ST, KA, KB) do { \
        if (((T) == 0) == (kh != 0)) __builtin_amdgcn_s_setprio(1); else __builtin_amdgcn_s_setprio(0); \
        P2_STEP(sA, sB, kA0, kA1, 1, o[0], vA0, o[1], vB1, { kB0 = LDK0(cur, (T) + 1); kB1 = LDK1(cur, (T) + 1); vB0 = LDV0(cur, (T) + 1); vB1 = LDV1(cur, (T) + 1); }, false); \
        P2_STEP(sB, sA, kB0, kB1, 0, o[1], vA0, o[0], vA1, { P2_PIECE(ST, KA); }, false); \
        P2_STEP(sA, sB, kB0, kB1, 1, o[0], vB0, o[1], vA1, { if (!(LAST)) { kA0 = LDK0(NS_, NT); kA1 = LDK1(NS_, NT); vA0 = LDV0(NS_, NT); vA1 = LDV1(NS_, NT); } }, false); \
        P2_STEP(sB, sA, kA0, kA1, 0, o[1], vB0, o[0], vB1, { if ((KB) >= 0) P2_PIECE(ST, KB); }, LAST); } while (0)
#define P2_STAGE(LAST, ST) do { P2_PAIR(0, cur, 2, false, ST, 0, 1); P2_PAIR(2, nxt, 0, LAST, ST, 2, -1); \
        fre = cur; cur = nxt; nxt = (nxt == (P2_R - 1) * P2_SLOT) ? 0 : nxt + P2_SLOT; } while (0)
#pragma unroll
    for (int s = 0; s < P2_NS - 3; ++s) { WAIT_BAR(3); P2_STAGE(false, s + P2_R - 1); }
    WAIT_BAR(3);
    unsigned pk[4];
    if (wid == 0) {
#pragma unroll
        for (int i = 0; i < 4; ++i) pk[i] = lg32(c.rs, flag_off(FL_PK, 64 * i + lane));
    }
    P2_STAGE(false, -1);
    if (wid == 0) {
        for (unsigned spins = 0; spins < SPIN_CAP; ++spins) {
            if (__all(pk[0] == c.tag && pk[1] == c.tag && pk[2] == c.tag && pk[3] == c.tag)) break;
            __builtin_amdgcn_s_sleep(1);
#pragma unroll
            for (int i = 0; i < 4; ++i) pk[i] = lg32(c.rs, flag_off(FL_PK, 64 * i + lane));
        }
    }
    WAIT_BAR(0);
    const unsigned wsc = c.lds0 + P2_Q + 12288 + wid * 1024;
    unsigned wu[6];
#pragma unroll
    for (int i = 0; i < 6; ++i) { const int L = imin((idx * 8 + wid) * 6 + i, 1519);
        wu[i] = (L < 400 ? WS_WOP + (g & 1) * 409600 + L * 1024 : WS_W13P + imin((g >> 1) * 1146880 + (L - 400) * 1024, 108 * 40960 - 1024)) + lane * 16; }
#define P2_WARM(i) dma16<true>(c.ws, wu[i], wsc)
    P2_PAIR(0, cur, 2, false, -1, 0, 1); P2_WARM(0); P2_WARM(1); P2_PAIR(2, nxt, 0, false, -1, 2, -1); P2_WARM(2);
    fre = cur; cur = nxt; nxt = (nxt == (P2_R - 1) * P2_SLOT) ? 0 : nxt + P2_SLOT;
    P2_WARM(3); P2_PAIR(0, cur, 2, false, -1, 0, 1); P2_WARM(4); P2_WARM(5); P2_PAIR(2, nxt, 0, true, -1, 2, -1);
#undef P2_WARM
    o[1] = MFMA32(vB1, __builtin_bit_cast(s16x8, p1), o[1]);
#undef P2_STEP
#undef P2_PAIR
#undef P2_PIECE
#undef P2_STAGE
#undef P2_ISSUE
    __builtin_amdgcn_s_setprio(0);
    LAS f32x4* ex = (LAS f32x4*)(lds + P2_EX);
    { const unsigned m = kh ? 0xffffffffu : 0u;
#pragma unroll
      for (int i = 0; i < 3; ++i) { const f32x4 a = {o[0][4 * i], o[0][4 * i + 1], o[0][4 * i + 2], o[0][4 * i + 3]}, e = {o[1][4 * i], o[1][4 * i + 1], o[1][4 * i + 2], o[1][4 * i + 3]};
          ex[(wid * 3 + i) * 64 + lane] = sel4(m, a, e); }
      LDS_BAR();
      f32x16 of = {};
#pragma unroll
      for (int i = 0; i < 3; ++i) { const f32x4 a = {o[0][4 * i], o[0][4 * i + 1], o[0][4 * i + 2], o[0][4 * i + 3]}, e = {o[1][4 * i], o[1][4 * i + 1], o[1][4 * i + 2], o[1][4 * i + 3]};
          const f32x4 t = sel4(m, e, a) + ex[((wid ^ 4) * 3 + i) * 64 + lane];
          of[4 * i] = t[0]; of[4 * i + 1] = t[1]; of[4 * i + 2] = t[2]; of[4 * i + 3] = t[3]; }
      float l = upper_half(of[8]);
      bool bad = !(l < 1.2676506e30f && l > 8.6736174e-19f);
#pragma unroll
      for (int i = 0; i < 12; ++i) bad |= !(fabsf(of[i]) < 3.0e38f);
      if (__any(bad)) {
          const s16x8 qq0 = ld128((lcptr)lds + P2_Q + (2 * qg + kh) * 1536 + lane * 16); const s16x4 qq1 = ld64((lcptr)lds + P2_Q + (2 * qg + kh) * 1536 + 1024 + lane * 8);
          attn_safe(c, WS_KV + (unsigned)hh * 64 * 3072, qq0, qq1, of); l = upper_half(of[8]); }
      const float il = __builtin_amdgcn_rcpf(l);
      const unsigned a0 = cvtpk(of[0] * il, of[1] * il), a1 = cvtpk(of[2] * il, of[3] * il), b0 = cvtpk(of[4] * il, of[5] * il), b1 = cvtpk(of[6] * il, of[7] * il);
      auto x0 = __builtin_amdgcn_permlane32_swap(a0, b0, false, false), x1 = __builtin_amdgcn_permlane32_swap(a1, b1, false, false);
      const unsigned t16 = 2 * (8 * qb + 2 * qg + kh) + (r >> 4);
      st128(c.rs, WS_O + (t16 * 80 + 2 * hh + h) * 256 + (r & 15) * 16, (u32x4){x0[0], x1[0], x0[1], x1[1]});
      if (h == 0) st64(c.rs, WS_O + (t16 * 80 + 64 + (hh >> 1)) * 256 + (r & 15) * 16 + (hh & 1) * 8, (u32x2){cvtpk(of[8] * il, of[9] * il), cvtpk(of[10] * il, of[11] * il)});
    }
#undef LDK0
#undef LDK1
#undef LDV0
#undef LDV1
#undef EX
}

constexpr int PJ_R = 5;
template <int NKS, int KSS> struct PJ {
    static constexpr int NS = NKS / KSS, SLOT = 5 * KSS * 1024, NWI = (5 * KSS + 7) / 8, NA = NKS / 2, PRE = imin(PJ_R, NS);
};
template <int NKS, int KSS> DI void pj_issue_w(const Ctx& c, const unsigned (&voff)[(5 * KSS + 7) / 8], int st) {
    using P = PJ<NKS, KSS>;
    const char* base = c.ws + st * (KSS * 1024);
#pragma unroll
    for (int i = 0; i < P::NWI; ++i) dma16<true>(base, voff[i], c.lds0 + (st % PJ_R) * P::SLOT + imin(c.wid + 8 * i, 5 * KSS - 1) * 1024);
}
template <int NKS, int KSS> DI void pj_voff(const Ctx& c, unsigned wbase, int cc, unsigned (&voff)[(5 * KSS + 7) / 8]) {
#pragma unroll
    for (int i = 0; i < PJ<NKS, KSS>::NWI; ++i) { const int f = imin(c.wid + 8 * i, 5 * KSS - 1); voff[i] = wbase + (unsigned)((5 * cc + f / KSS) * NKS + f % KSS) * 1024 + c.lane * 16; }
}
DI int pj_pp(int b) { return 8 * ((b & 7) >> 1) + ((b >> 3) & 7); }
DI int pj_cc(int b) { return 4 * (b & 1) + (b >> 6); }
template <int NKS, int KSS> DI void pj_prefetch(const Ctx& c, unsigned wbase) {
    unsigned voff[PJ<NKS, KSS>::NWI]; pj_voff<NKS, KSS>(c, wbase, pj_cc(c.b), voff);
#pragma unroll
    for (int s = 0; s < PJ<NKS, KSS>::PRE; ++s) pj_issue_w<NKS, KSS>(c, voff, s);
}

template <int NA, int WIN, int HK, int NS, int NWI, int NRES> constexpr int pj_younger(int j) {
    int n = imin(NA - 1, j + WIN) - j;
    if (j < WIN) n += NRES;
    for (int s = 1; s + PJ_R - 1 < NS; ++s)
        if (HK * s <= j && (j < WIN || HK * s > j - WIN)) n += NWI;
    return n;
}
template <int NA, int WIN, int HK, int NS, int NWI> constexpr int pj_bar_younger(int s) {
    const int s0 = s + 2 - PJ_R;
    int n = 0;
    for (int t = s0 + 1; t < s; ++t) if (t + PJ_R - 1 < NS) n += NWI;
    for (int j = HK * s0; j < HK * s; ++j) if (j + WIN < NA) ++n;
    return n;
}
template <int NKS, int KSS, bool FIRST, int KH>
DI void pj_body(const Ctx& c, unsigned abase, unsigned wbase, int fc, u32x2 (&res)[3]) {
    using P = PJ<NKS, KSS>;
    constexpr int NS = P::NS, NG = NKS * 4, NA = P::NA, HK = KSS / 2, NCT = KH ? 2 : 3, CT0 = KH ? 3 : 0;
    const int lane = c.lane, wid = c.wid, t16l = wid & 3, r = lane & 15, gq = lane >> 4, pp = pj_pp(c.b), cc = pj_cc(c.b);
    lptr lds = c.lds;
    const unsigned t16 = 4 * pp + t16l;
    const int tok = 16 * t16 + r;
    constexpr int WIN = imin(NA, FIRST ? 4 : 9);
    s16x8 a[NA];
    const unsigned aoff = abase + (t16 * NG + 4 * KH) * 256 + lane * 16;
#define PJ_ABASE(j) (c.ws + (j) * 2048)
#pragma unroll
    for (int j = 0; j < WIN; ++j) ldg_async(a[j], PJ_ABASE(j), aoff);
    if (FIRST) {
#pragma unroll
        for (int i = 0; i < NCT; ++i) res[i] = *(const GAS u32x2*)(c.x + (size_t)tok * 1280 + cc * 160 + (CT0 + i) * 32 + gq * 8);
    }
    constexpr int NCS = FIRST && KH ? 20 : 0;
    unsigned csv[NCS ? NCS : 1];
    const int cst = wid * 64 + lane - 256;
    if (NCS) { const int mat = cst >= 112, col = imin(fc * 112 + imin(cst, 223) - 112 * mat, F - 1);
#pragma unroll
        for (int i = 0; i < NCS; ++i) csv[i] = lg32(c.rs, WS_CSP + (unsigned)((mat * 20 + i) * F + col) * 4); }
    unsigned voff[P::NWI]; pj_voff<NKS, KSS>(c, wbase, cc, voff);
    constexpr int NRES = (FIRST ? NCT : 0) + NCS;
    WAIT_BARV(WIN + NRES);
    const unsigned wa = KH * 1024 + lane * 16;
#define PJ_READ(W, st, i) do { lcptr b_ = (lcptr)lds + ((st) % PJ_R) * P::SLOT + wa + (i) * 2048; \
        _Pragma("unroll") for (int ct = 0; ct < 5; ++ct) W[ct] = ld128(b_ + ct * KSS * 1024); } while (0)
    f32x4 acc[5];
#pragma unroll
    for (int ct = 0; ct < 5; ++ct) acc[ct] = (f32x4){};
    s16x8 w[2][5];
    PJ_READ(w[0], 0, 0);
#pragma unroll
    for (int s = 0; s < NS; ++s) {
        if (NS > PJ_R && s >= 1) {
            if (s + 1 >= PJ_R && s + 1 < NS) WAIT_BAR((pj_bar_younger<NA, WIN, HK, NS, P::NWI>(s))); else LDS_BAR();
            SBAR();
            if (s + PJ_R - 1 < NS) pj_issue_w<NKS, KSS>(c, voff, s + PJ_R - 1);
        }
#pragma unroll
        for (int i = 0; i < HK; ++i) {
            const int j = s * HK + i;
            if (i + 1 < HK) PJ_READ(w[(j + 1) & 1], s, i + 1); else if (s + 1 < NS) PJ_READ(w[(j + 1) & 1], s + 1, 0);
            if (j + WIN < NA) ldg_async(a[j + WIN], PJ_ABASE(j + WIN), aoff);
            WAIT_USE((pj_younger<NA, WIN, HK, NS, P::NWI, NRES>(j)), a[j]);
            SBAR();
#pragma unroll
            for (int ct = 0; ct < 5; ++ct) acc[ct] = MFMA16(w[j & 1][ct], a[j], acc[ct]);
            SBAR();
        }
    }
#undef PJ_READ
#undef PJ_ABASE
    if (NCS) { float s = 0.f;
#pragma unroll
        for (int i = 0; i < NCS; ++i) s += __uint_as_float(csv[i]);
        if (cst < 224) *(LAS float*)(lds + P4_CS + ((cst >= 112) * 128 + cst - 112 * (cst >= 112)) * 4) = s; }
    LDS_BAR();
    LAS f32x4* red = (LAS f32x4*)(lds + XCH);
#pragma unroll
    for (int i = 0; i < 5 - NCT; ++i) red[(wid * 3 + i) * 64 + lane] = acc[(KH ? 0 : 3) + i];
    LDS_BAR();
    float s1 = 0.f, s2 = 0.f;
#pragma unroll
    for (int i = 0; i < NCT; ++i) {
        const f32x4 o = red[((wid ^ 4) * 3 + i) * 64 + lane], m = acc[CT0 + i];
        const unsigned w0 = cvtpk(bflo(res[i].x) + m[0] + o[0], bfhi(res[i].x) + m[1] + o[1]), w1 = cvtpk(bflo(res[i].y) + m[2] + o[2], bfhi(res[i].y) + m[3] + o[3]);
        if (FIRST) {
            res[i] = (u32x2){w0, w1};
            st64(c.rs, WS_H + (t16 * 80 + 10 * cc + 2 * (CT0 + i) + (gq >> 1)) * 256 + r * 16 + (gq & 1) * 8, res[i]);
            const float a_ = bflo(w0), b_ = bfhi(w0), c_ = bflo(w1), d = bfhi(w1); s1 += (a_ + b_) + (c_ + d); s2 += (a_ * a_ + b_ * b_) + (c_ * c_ + d * d);
        } else {
            *(GAS u32x2*)(c.out + (size_t)tok * 1280 + cc * 160 + (CT0 + i) * 32 + gq * 8) = (u32x2){w0, w1};
        }
    }
    if (FIRST) {
        s1 += __shfl_xor(s1, 16); s2 += __shfl_xor(s2, 16); s1 = swap32_add(s1); s2 = swap32_add(s2);
        LAS f32x2* ex = (LAS f32x2*)(lds + XCH + 2048);
        if (KH == 1 && gq == 0) ex[t16l * 16 + r] = (f32x2){s1, s2};
        LDS_BAR();
        if (KH == 0 && gq == 0) { const f32x2 e = ex[t16l * 16 + r];
            st64(c.rs, WS_HST + ((t16 * 8 + cc) * 16 + r) * 8, (u32x2){__float_as_uint(s1 + e.x), __float_as_uint(s2 + e.y)}); }
    }
}
template <int NKS, int KSS, bool FIRST>
DI void phase_proj(const Ctx& c, unsigned abase, unsigned wbase, int fc, u32x2 (&res)[3]) {
    if (c.wid < 4) pj_body<NKS, KSS, FIRST, 0>(c, abase, wbase, fc, res);
    else pj_body<NKS, KSS, FIRST, 1>(c, abase, wbase, fc, res);
}

constexpr int P4_R = 7, P4_SLOT = 22528, P4_NS = 20;

DI void p4_wvoff(const Ctx& c, int fc, unsigned& woff0, unsigned& woff1) {
    const unsigned o0 = c.wid * 1792 + c.lane * 16, o1 = c.wid * 1792 + 1024 + (c.lane < 48 ? c.lane : 47) * 16;
    woff0 = WS_W13P + (unsigned)imin(7 * fc + (o0 >> 11), 107) * 40960 + (o0 & 2047); woff1 = WS_W13P + (unsigned)imin(7 * fc + (o1 >> 11), 107) * 40960 + (o1 & 2047);
}
DI void p4_issue_w(const Ctx& c, unsigned woff0, unsigned woff1, int st) {
    const char* w_ = c.ws + st * 2048; const unsigned wdst = c.lds0 + 8192 + c.wid * 1792 + (st % P4_R) * P4_SLOT;
    dma16<true>(w_, woff0, wdst); dma16m<true>(w_, woff1, wdst + 1024, 0xffffffffffffull);
}
DI void p4_prefetch(const Ctx& c, int fc) {
    unsigned woff0, woff1; p4_wvoff(c, fc, woff0, woff1);
#pragma unroll
    for (int s = 0; s < P4_R - 1; ++s) p4_issue_w(c, woff0, woff1, s);
}
constexpr int p4_extra(int s) { int n = 0; for (int t = s - 5 < 0 ? 0 : s - 5; t < s; ++t) if (false) ++n; return n; }
template <int UG>
DI void p4_body(const Ctx& c, int qb, int fc) {
    constexpr int NU = UG ? 3 : 4, U0 = UG ? 4 : 0;
    const int lane = c.lane, wid = c.wid, tt = wid & 3, r = lane & 31, h = lane >> 5;
    lptr lds = c.lds;
    const unsigned adst = c.lds0 + wid * 1024;
    const unsigned aoff = WS_H + (unsigned)(8 * qb + wid) * 80 * 256 + lane * 16;
    unsigned woff0, woff1; p4_wvoff(c, fc, woff0, woff1);
#pragma unroll
    for (int s = 0; s < P4_R - 1; ++s) dma16<true>(c.ws + s * 1024, aoff, adst + s * P4_SLOT);
    const unsigned xa = ((2 * tt + (r >> 4)) * 4 + h) * 256 + (r & 15) * 16, wa = 8192 + U0 * 2048 + lane * 16;
    const unsigned t16 = 8 * qb + 2 * tt + (r >> 4);
    u32x2 hs[4];
#pragma unroll
    for (int i = 0; i < 4; ++i) hs[i] = lg64(c.rs, WS_HST + ((t16 * 8 + 4 * h + i) * 16 + (r & 15)) * 8);
#define P4_PIECE(st, k) do { const unsigned sl_ = ((st) % P4_R) * P4_SLOT; \
        if ((k) == 0) dma16<true>(c.ws + (st) * 1024, aoff, adst + sl_); \
        if ((k) == 1) dma16<true>(c.ws + (st) * 2048, woff0, c.lds0 + 8192 + wid * 1792 + sl_); \
        if ((k) == 2) dma16m<true>(c.ws + (st) * 2048, woff1, c.lds0 + 8192 + wid * 1792 + sl_ + 1024, 0xffffffffffffull); } while (0)
#define P4_READ(n) do { lcptr b_ = (lcptr)lds + (((n) >> 1) % P4_R) * P4_SLOT; X[(n) % 3] = ld128(b_ + xa + ((n) & 1) * 512); \
        _Pragma("unroll") for (int u = 0; u < NU; ++u) W[(n) % 3][u] = ld128(b_ + wa + u * 2048 + ((n) & 1) * 1024); } while (0)
    f32x16 acc[NU];
#pragma unroll
    for (int u = 0; u < NU; ++u) acc[u] = (f32x16){};
    WAIT_BAR((P4_R - 2) + 4);
    s16x8 X[3], W[3][NU];
    P4_READ(0); P4_READ(1);
#pragma unroll
    for (int s = 0; s < P4_NS; ++s) {
        WAIT_BARV(fly2(s, P4_R, P4_NS, 1, 2) + (s + 1 < P4_R - 1 ? 4 : 0) + p4_extra(s));
        SBAR();
#define P4_NEXT(k) do { if (s + P4_R - 1 < P4_NS) P4_PIECE(s + P4_R - 1, k); SBAR(); } while (0)
#pragma unroll
        for (int ks = 0; ks < 2; ++ks) {
            const int n = 2 * s + ks;
            if (n + 2 < 2 * P4_NS) P4_READ(n + 2);
            SBAR();
            acc[0] = MFMA32(W[n % 3][0], X[n % 3], acc[0]); SBAR(); P4_NEXT(ks);
            acc[1] = MFMA32(W[n % 3][1], X[n % 3], acc[1]); acc[2] = MFMA32(W[n % 3][2], X[n % 3], acc[2]); SBAR();
            if (ks) P4_NEXT(2);
            if (NU > 3) acc[NU - 1] = MFMA32(W[n % 3][NU - 1], X[n % 3], acc[NU - 1]);
            SBAR();
        }
#undef P4_NEXT
        if (false) {
            const int L = imin(((c.b >> 3) * 8 + wid) * 5 + s / 3, 1079);
            dma16<true>(c.ws, WS_W2P + (c.b & 1) * 1105920 + L * 1024 + lane * 16, c.lds0 + P4_R * P4_SLOT + 1024); SBAR(); }
    }
#undef P4_READ
    LDS_BAR();
    float s1 = (__uint_as_float(hs[0].x) + __uint_as_float(hs[1].x)) + (__uint_as_float(hs[2].x) + __uint_as_float(hs[3].x));
    float s2 = (__uint_as_float(hs[0].y) + __uint_as_float(hs[1].y)) + (__uint_as_float(hs[2].y) + __uint_as_float(hs[3].y));
    s1 = swap32_add(s1); s2 = swap32_add(s2);
    const float mean = s1 * (1.0f / D), var = fmaxf(s2 * (1.0f / D) - mean * mean, 0.f), rstd = __builtin_amdgcn_rsqf(var + LN_EPS);
    const f32x2 nm = {-mean, -mean}, k1 = {-1.4426950408889634f * rstd, -1.4426950408889634f * rstd}, r2 = {rstd * rstd, rstd * rstd}, one = {1.0f, 1.0f};
    const LAS float* csl = (const LAS float*)(lds + P4_CS);
#pragma unroll
    for (int u = 0; u < NU; ++u) {
        if (7 * fc + U0 + u < 108) {
            u32x2 w[2];
#pragma unroll
            for (int gq = 0; gq < 2; ++gq) {
                const f32x4 cg_ = *(const LAS f32x4*)(csl + (U0 + u) * 16 + 8 * gq + 4 * h), cu = *(const LAS f32x4*)(csl + 128 + (U0 + u) * 16 + 8 * gq + 4 * h);
                f32x2 o[2];
#pragma unroll
                for (int e = 0; e < 2; ++e) {
                    const f32x2 a = __builtin_elementwise_fma(nm, (f32x2){cg_[2 * e], cg_[2 * e + 1]}, (f32x2){acc[u][4 * gq + 2 * e], acc[u][4 * gq + 2 * e + 1]});
                    const f32x2 b2 = __builtin_elementwise_fma(nm, (f32x2){cu[2 * e], cu[2 * e + 1]}, (f32x2){acc[u][8 + 4 * gq + 2 * e], acc[u][8 + 4 * gq + 2 * e + 1]});
                    const f32x2 t = a * k1, d = (f32x2){__builtin_amdgcn_exp2f(t.x), __builtin_amdgcn_exp2f(t.y)} + one;
                    const f32x2 rc = {__builtin_amdgcn_rcpf(d.x), __builtin_amdgcn_rcpf(d.y)};
                    o[e] = (a * b2) * (rc * r2);
                }
                w[gq] = (u32x2){cvtpk(o[0].x, o[0].y), cvtpk(o[1].x, o[1].y)};
            }
            auto x0 = __builtin_amdgcn_permlane32_swap(w[0].x, w[1].x, false, false), x1 = __builtin_amdgcn_permlane32_swap(w[0].y, w[1].y, false, false);
            st128(c.rs, WS_HM + (t16 * 216 + 14 * fc + 2 * (U0 + u) + h) * 256 + (r & 15) * 16, (u32x4){x0[0], x1[0], x0[1], x1[1]});
        }
    }
}
DI void phase4(const Ctx& c, int qb, int fc) {
    if (c.wid < 4) p4_body<0>(c, qb, fc); else p4_body<1>(c, qb, fc);
}
__global__ __launch_bounds__(512, 2) void block_fwd(const char* __restrict__ x, const char* __restrict__ wqkv, const char* __restrict__ wo, const char* __restrict__ w1,
                                                    const char* __restrict__ w3, const char* __restrict__ w2, char* __restrict__ out, char* __restrict__ ws) {
    extern __shared__ __attribute__((aligned(16))) char smem[];
    {
        unsigned long long pc; asm volatile("s_getpc_b64 %0" : "=s"(pc));
        const unsigned wv = __builtin_amdgcn_readfirstlane(threadIdx.x >> 6), piece = ((blockIdx.x >> 3) * 8 + wv) * 1024;
        dma16<false>((const char*)(pc & ~1023ull), (piece < CODE_BYTES ? piece : CODE_BYTES - 1024) + (threadIdx.x & 63) * 16, (unsigned)(uintptr_t)smem + 4 * P1_SLOT + wv * 2048);
    }
    Ctx c; c.x = x; c.wqkv = wqkv; c.wo = wo; c.w1 = w1; c.w3 = w3; c.w2 = w2; c.out = out; c.ws = ws;
    c.rs = __builtin_amdgcn_make_buffer_rsrc(ws, 0, WS_END, 0x00020000);
    c.lds = (lptr)smem; c.lds0 = (unsigned)(uintptr_t)smem; c.b = blockIdx.x; c.lane = threadIdx.x & 63; c.wid = __builtin_amdgcn_readfirstlane(threadIdx.x >> 6);
    const int lane = c.lane, g = c.b & 7, idx = c.b >> 3;
    c.tag = *(const __attribute__((address_space(4))) unsigned*)(ws + WS_E);
    u32x2 res[3] = {(u32x2){0u, 0u}, (u32x2){0u, 0u}, (u32x2){0u, 0u}};
    const int qb4 = 8 * (g & 1) + (idx & 7), fc = 4 * (g >> 1) + (idx >> 3), pp = pj_pp(c.b);

    phase1(c);
    WAIT_VM(0);
    publish(c, FL_KV);
    repack_store(c);

    { const int hh = 4 * g + (idx & 3), qb = idx >> 2;
      const int wg = lane < 16 ? wg_p1(lane, (32 + hh) / 6) : lane < 32 ? wg_p1(lane - 16, (64 + hh) / 6) : wg_p1(2 * qb + (lane & 1), hh / 6);
      const unsigned off[1] = {flag_off(FL_KV, wg)}; wait_flags(c, off); }
    phase2(c);
    WAIT_VM(0);
    publish(c, FL_O);
    pj_prefetch<20, 4>(c, WS_WOP);

    { const unsigned off[1] = {flag_off(FL_O, wg_p2(lane & 31, pp >> 2))}; wait_flags(c, off); }
    phase_proj<20, 4, true>(c, WS_O, WS_WOP, fc, res);
    WAIT_VM(0);
    publish(c, FL_H);
    p4_prefetch(c, fc);

    { const unsigned off[1] = {flag_off(FL_H, wg_p3(2 * qb4 + (lane & 1), (lane >> 1) & 7))}; wait_flags(c, off); }
    phase4(c, qb4, fc);
    WAIT_VM(0);
    publish(c, FL_M);
    pj_prefetch<54, 6>(c, WS_W2P);

    { const unsigned fm = flag_off(FL_M, wg_p4(pp >> 1, lane & 15));
      if (c.b == 0) {
          const unsigned off[5] = {fm, flag_off(FL_O, lane), flag_off(FL_O, 64 + lane), flag_off(FL_O, 128 + lane), flag_off(FL_O, 192 + lane)}; wait_flags(c, off);
      } else { const unsigned off[1] = {fm}; wait_flags(c, off); } }
    phase_proj<54, 6, false>(c, WS_HM, WS_W2P, fc, res);
    if (c.b == 0 && threadIdx.x == 0) st32(c.rs, WS_E, c.tag);
}

}

extern "C" void kernel_launch(void* const* d_in, const int* in_sizes, int n_in, void* d_out, int out_size, void* d_ws, size_t ws_size, hipStream_t stream) {
    using namespace blk;
    const char* x = (const char*)d_in[0]; const char* wqkv = (const char*)d_in[1]; const char* wo = (const char*)d_in[2];
    const char* w1 = (const char*)d_in[4]; const char* w3 = (const char*)d_in[5]; const char* w2 = (const char*)d_in[6];
    char* ws = (char*)d_ws; char* out = (char*)d_out;
    hipFuncSetAttribute((const void*)block_fwd, hipFuncAttributeMaxDynamicSharedMemorySize, LDS_BYTES);
    block_fwd<<<256, 512, LDS_BYTES, stream>>>(x, wqkv, wo, w1, w3, w2, out, ws);
}
```

```cpp
#include <hip/hip_runtime.h>
#include <hip/hip_bf16.h>
#include <stdint.h>

namespace blk {

constexpr int S = 2048, D = 640, H = 32, F = 1728;
constexpr float LN_EPS = 1e-5f;
constexpr float QSCALE = 0.22360679774997896f * 1.4426950408889634f;

#define LAS __attribute__((address_space(3)))
#define GAS __attribute__((address_space(1)))
#define DI __device__ __forceinline__
typedef short s16x8 __attribute__((ext_vector_type(8)));
typedef short s16x4 __attribute__((ext_vector_type(4)));
typedef float f32x16 __attribute__((ext_vector_type(16)));
typedef float f32x4 __attribute__((ext_vector_type(4)));
typedef float f32x2 __attribute__((ext_vector_type(2)));
typedef unsigned u32x4 __attribute__((ext_vector_type(4)));
typedef unsigned u32x2 __attribute__((ext_vector_type(2)));
typedef __bf16 bf16x2 __attribute__((ext_vector_type(2)));
typedef LAS char* lptr;
typedef LAS const char* lcptr;
typedef __amdgpu_buffer_rsrc_t rsrc_t;

constexpr unsigned WS_E = 0;
constexpr unsigned WS_FLAG = 4096;
constexpr unsigned WS_ROWP = 256u << 10;
constexpr unsigned WS_COLP = 512u << 10;
constexpr unsigned WS_Q = 1u << 20;
constexpr unsigned WS_KV = 4u << 20;
constexpr unsigned WS_O = 12u << 20;
constexpr unsigned WS_H = 16u << 20;
constexpr unsigned WS_HST = 20u << 20;
constexpr unsigned WS_CSP = 21u << 20;
constexpr unsigned WS_HM = 24u << 20;
constexpr unsigned WS_W13P = 32u << 20;
constexpr unsigned WS_W2P = 40u << 20;
constexpr unsigned WS_WOP = 44u << 20;
constexpr unsigned WS_END = 48u << 20;
enum { FL_KV = 0, FL_PK = 1, FL_O = 2, FL_H = 3, FL_M = 4 };
constexpr int LDS_BYTES = 163840;
constexpr unsigned SPIN_CAP = 1u << 16;
constexpr unsigned CODE_BYTES = 62 * 1024;

#define SBAR() __builtin_amdgcn_sched_barrier(0)
#define PIN(x) asm volatile("" : "+v"(x))
#define MFMA32(a, b, c) __builtin_amdgcn_mfma_f32_32x32x16_bf16(a, b, c, 0, 0, 0)
#define MFMA32K8(a, b, c) __builtin_amdgcn_mfma_f32_32x32x8bf16_1k(a, b, c, 0, 0, 0)
#define MFMA16(a, b, c) __builtin_amdgcn_mfma_f32_16x16x32_bf16(a, b, c, 0, 0, 0)
#define WAIT_BAR(N) asm volatile("s_waitcnt vmcnt(%0) lgkmcnt(0)\n\ts_barrier" :: "i"(N) : "memory")
#define WAIT_BARV(N) asm volatile("s_waitcnt vmcnt(%0)\n\ts_barrier" :: "i"(N) : "memory")
#define WAIT_VM(N) asm volatile("s_waitcnt vmcnt(%0)" :: "i"(N) : "memory")
#define LDS_BAR() asm volatile("s_waitcnt lgkmcnt(0)\n\ts_barrier" ::: "memory")
#define POLL_PAUSE() do { __builtin_amdgcn_s_sleep(1); asm volatile("" ::: "memory"); } while (0)

template <bool SC> DI void dma16(const void* sbase, unsigned voff, unsigned ldsaddr) {
    if (SC) asm volatile("s_mov_b32 m0, %2\n\ts_nop 0\n\tglobal_load_lds_dwordx4 %0, %1 sc1" :: "v"(voff), "s"(sbase), "s"(ldsaddr) : "memory");
    else asm volatile("s_mov_b32 m0, %2\n\ts_nop 0\n\tglobal_load_lds_dwordx4 %0, %1" :: "v"(voff), "s"(sbase), "s"(ldsaddr) : "memory");
}
template <bool SC> DI void dma16m(const void* sbase, unsigned voff, unsigned ldsaddr, unsigned long long mask) {
    if (SC) asm volatile("s_mov_b32 m0, %2\n\ts_mov_b64 exec, %3\n\tglobal_load_lds_dwordx4 %0, %1 sc1\n\ts_mov_b64 exec, -1" :: "v"(voff), "s"(sbase), "s"(ldsaddr), "s"(mask) : "memory");
    else asm volatile("s_mov_b32 m0, %2\n\ts_mov_b64 exec, %3\n\tglobal_load_lds_dwordx4 %0, %1\n\ts_mov_b64 exec, -1" :: "v"(voff), "s"(sbase), "s"(ldsaddr), "s"(mask) : "memory");
}
constexpr int imin(int a, int b) { return a < b ? a : b; }
constexpr int imax(int a, int b) { return a > b ? a : b; }
constexpr int fly(int s, int R, int NS) { return imax(0, imin(R - 3, NS - 2 - s)); }
constexpr int fly2(int s, int R, int NS, int na, int nw) {
    const int need = imin(s + 1, NS - 1);
    const int pro = imin(R - 1, NS);
    const int issued = pro * na + imax(0, imin(s, NS - R + 1)) * (na + nw);
    const int endpos = need < pro ? (need + 1) * na : pro * na + (need - pro + 1) * (na + nw);
    return imax(0, issued - endpos);
}
DI int rot(int i, int rho, int ns) { const int t = i + rho; return t >= ns ? t - ns : t; }
DI unsigned cvtpk(float lo, float hi) { f32x2 v = {lo, hi}; return __builtin_bit_cast(unsigned, __builtin_convertvector(v, bf16x2)); }
DI float bflo(unsigned w) { return __uint_as_float(w << 16); }
DI float bfhi(unsigned w) { return __uint_as_float(w & 0xffff0000u); }
DI s16x8 ld128(lcptr p) { return *(const LAS s16x8*)p; }
DI s16x4 ld64(lcptr p) { return *(const LAS s16x4*)p; }
DI s16x4 ldtr(lcptr p) { return __builtin_bit_cast(s16x4, __builtin_amdgcn_ds_read_tr16_b64_v4i16((LAS s16x4*)p)); }
DI s16x8 cat(s16x4 a, s16x4 b) { return __builtin_shufflevector(a, b, 0, 1, 2, 3, 4, 5, 6, 7); }
DI f32x4 sel4(unsigned m, f32x4 a, f32x4 b) { const u32x4 x = __builtin_bit_cast(u32x4, a), y = __builtin_bit_cast(u32x4, b); return __builtin_bit_cast(f32x4, (x & m) | (y & ~m)); }
DI float swap32_add(float v) { auto r = __builtin_amdgcn_permlane32_swap(__float_as_uint(v), __float_as_uint(v), false, false); return __uint_as_float(r[0]) + __uint_as_float(r[1]); }
DI float swap32_max(float v) { auto r = __builtin_amdgcn_permlane32_swap(__float_as_uint(v), __float_as_uint(v), false, false); return fmaxf(__uint_as_float(r[0]), __uint_as_float(r[1])); }
DI float upper_half(float v) { auto r = __builtin_amdgcn_permlane32_swap(__float_as_uint(v), __float_as_uint(v), false, false); return __uint_as_float(r[1]); }
DI void st128(rsrc_t rs, unsigned off, u32x4 v) { __builtin_amdgcn_raw_buffer_store_b128(v, rs, off, 0, 16); }
DI void st64(rsrc_t rs, unsigned off, u32x2 v) { __builtin_amdgcn_raw_buffer_store_b64(v, rs, off, 0, 16); }
DI void st32(rsrc_t rs, unsigned off, unsigned v) { __builtin_amdgcn_raw_buffer_store_b32(v, rs, off, 0, 16); }
DI u32x4 lg128(rsrc_t rs, unsigned off) { return __builtin_amdgcn_raw_buffer_load_b128(rs, off, 0, 16); }
DI u32x2 lg64(rsrc_t rs, unsigned off) { return __builtin_amdgcn_raw_buffer_load_b64(rs, off, 0, 16); }
DI unsigned lg32(rsrc_t rs, unsigned off) { return __builtin_amdgcn_raw_buffer_load_b32(rs, off, 0, 16); }

DI void ldg_async(s16x8& dst, const void* sbase, unsigned voff) { asm volatile("global_load_dwordx4 %0, %1, %2 sc1" : "=v"(dst) : "v"(voff), "s"(sbase) : "memory"); }
#define WAIT_USE(N, x) asm volatile("s_waitcnt vmcnt(%1)" : "+v"(x) : "i"(N) : "memory")
struct Ctx { const char *x, *wqkv, *wo, *w1, *w3, *w2; char *out, *ws; rsrc_t rs; lptr lds; unsigned lds0, tag; int b, lane, wid; };

DI unsigned flag_off(int kind, int wg) { return WS_FLAG + (unsigned)(kind * 256 + wg) * 128u; }
DI void publish(const Ctx& c, int kind) {
    asm volatile("s_barrier" ::: "memory");
    if (threadIdx.x == 0) st32(c.rs, flag_off(kind, c.b), c.tag);
}
template <int N> DI void wait_flags(const Ctx& c, const unsigned (&off)[N]) {
    if (c.wid == 0) {
        for (unsigned spins = 0; spins < SPIN_CAP; ++spins) {
            unsigned v[N];
#pragma unroll
            for (int i = 0; i < N; ++i) v[i] = lg32(c.rs, off[i]);
            bool ok = true;
#pragma unroll
            for (int i = 0; i < N; ++i) ok &= v[i] == c.tag;
            if (__all(ok)) break;
            POLL_PAUSE();
        }
    }
    asm volatile("s_waitcnt lgkmcnt(0)\n\ts_barrier" ::: "memory");
}
DI int wg_p1(int pb, int nb) { return ((pb >> 3) | ((nb >> 2) << 1)) + 8 * ((pb & 7) + 8 * (nb & 3)); }
DI int wg_p2(int hh, int qb) { return (hh >> 2) + 8 * ((hh & 3) + 4 * qb); }
DI int wg_p3(int pp, int cc) { return (((pp >> 3) << 1) | (cc >> 2)) + 8 * ((pp & 7) + 8 * (cc & 3)); }
DI int wg_p4(int qb, int fc) { return ((qb >> 3) | ((fc >> 2) << 1)) + 8 * ((qb & 7) + 8 * (fc & 3)); }

constexpr int P1_R = 5, P1_SLOT = 32768, P1_NS = 10;
constexpr int P1_STG = 264;
constexpr int P1_OUT = 65536;
constexpr int P1_ST = P1_OUT + 40960;
constexpr int P1_CS = P1_ST + 1024;

template <int UP>
DI void p1_loop(Ctx& c, unsigned xoff, unsigned woff, int tt, int rho, unsigned put, unsigned get, f32x16& acc0, f32x16& acc1, u32x2 (&part)[10]) {
    f32x16 accx = {};
    const int lane = c.lane, wid = c.wid, r = lane & 31, h = lane >> 5, q = (lane >> 2) & 3, p = lane & 3, cg = (lane >> 4) & 1;
    constexpr unsigned long long WMASK = ~((1ull << 15) | (1ull << 27) | (1ull << 39) | (1ull << 51));
    const unsigned xdst = c.lds0 + wid * 2048, wdst = c.lds0 + 16384 + wid * 2048;
    const int xrow = 32 * tt + r;
    const unsigned xa = xrow * 128 + ((h ^ ((xrow >> 1) & 7)) * 16);
    const unsigned wk = 16384 + (8 * h + q) * 256 + (p & 1) * 8;
    const unsigned wa0 = wk + (((4 * (2 * UP) + 2 * cg + (p >> 1)) ^ (4 * q)) * 16);
    const unsigned wa1 = wk + (((4 * (2 * UP + 1) + 2 * cg + (p >> 1)) ^ (4 * q)) * 16);
    const unsigned waz = wk + (((4 * tt + 2 * cg + (p >> 1)) ^ (4 * q)) * 16);
    const s16x8 ones = {0x3f80, 0x3f80, 0x3f80, 0x3f80, 0x3f80, 0x3f80, 0x3f80, 0x3f80};
#define P1_PIECE(st, slot, k) do { const int p_ = rot(st, rho, P1_NS); \
        if ((k) == 0) dma16<false>(c.x + p_ * 128, xoff, xdst + (slot)); \
        if ((k) == 1) dma16<false>(c.x + p_ * 128 + 8 * 1280, xoff ^ 64, xdst + (slot) + 1024); \
        if ((k) == 2) dma16m<false>(c.wqkv + (size_t)p_ * (64 * 3840), woff, wdst + (slot), WMASK); \
        if ((k) == 3) dma16m<false>(c.wqkv + (size_t)p_ * (64 * 3840) + 4 * 3840, woff, wdst + (slot) + 1024, WMASK); } while (0)
#define P1_ISSUE(st, slot) do { P1_PIECE(st, slot, 0); P1_PIECE(st, slot, 1); P1_PIECE(st, slot, 2); P1_PIECE(st, slot, 3); } while (0)
#define P1_READ(n) do { lcptr b_ = (lcptr)c.lds + (((n) >> 2) % P1_R) * P1_SLOT; \
        X[(n) % 3] = ld128(b_ + (xa ^ (((n) & 3) * 32))); \
        W0[(n) % 3] = cat(ldtr(b_ + wa0 + ((n) & 3) * 4096), ldtr(b_ + wa0 + ((n) & 3) * 4096 + 1024)); \
        W1[(n) % 3] = cat(ldtr(b_ + wa1 + ((n) & 3) * 4096), ldtr(b_ + wa1 + ((n) & 3) * 4096 + 1024)); \
        if (UP && (n) < 4) WZ[(n) & 3] = cat(ldtr(b_ + waz + ((n) & 3) * 4096), ldtr(b_ + waz + ((n) & 3) * 4096 + 1024)); } while (0)
#define P1_MMA(n) do { acc0 = MFMA32(W0[(n) % 3], X[(n) % 3], acc0); acc1 = MFMA32(W1[(n) % 3], X[(n) % 3], acc1); \
        if ((n) < 4) { if (UP) accx = MFMA32(WZ[(n) & 3], ones, accx); else accx = MFMA32(X[(n) % 3], X[(n) % 3], accx); } } while (0)
    P1_ISSUE(0, 0); P1_ISSUE(1, P1_SLOT); P1_ISSUE(2, 2 * P1_SLOT);
    WAIT_BAR(8);
    asm volatile("" : "+s"(c.tag));
    c.tag += 1u;
    P1_ISSUE(3, 3 * P1_SLOT);
    s16x8 X[3] = {}, W0[3] = {}, W1[3] = {}, WZ[4] = {};
    P1_READ(0); P1_READ(1);
#pragma unroll
    for (int s = 0; s < P1_NS; ++s) {
        constexpr int NX = P1_R - 1, GS = P1_NS - P1_R;
        WAIT_BARV(4 * fly(s, P1_R, P1_NS) + (s >= 1 && s + 1 <= P1_R - 1 ? 1 : 0) + (s > GS && !UP ? 10 : 0));
        SBAR();
#pragma unroll
        for (int ks = 0; ks < 4; ++ks) {
            const int n = 4 * s + ks;
            if (n + 2 < 4 * P1_NS) P1_READ(n + 2);
            SBAR(); P1_MMA(n); SBAR();
            if (s + NX < P1_NS) P1_PIECE(s + NX, ((s + NX) % P1_R) * P1_SLOT, ks);
            SBAR();
        }
        if (s == 0) {
            const int tr = (r & 3) + 4 * (r >> 3); float v = 0.f;
#pragma unroll
            for (int i = 0; i < 16; ++i) v = (tr == i) ? accx[i] : v;
            st64(c.rs, put + (h == ((r >> 2) & 1) ? (tt * 32 + r) * 8 : 0x7fffff00u), (u32x2){__float_as_uint(v), c.tag});
            SBAR();
        }
        if (s == GS && !UP) {
#pragma unroll
            for (int i = 0; i < 10; ++i) part[i] = lg64(c.rs, get + i * 1024);
            SBAR();
        }
    }
#undef P1_PIECE
#undef P1_ISSUE
#undef P1_READ
#undef P1_MMA
}

DI void phase1(Ctx& c) {
    const int lane = c.lane, wid = c.wid, tid = threadIdx.x, g = c.b & 7, idx = c.b >> 3;
    const int pb = 8 * (g & 1) + (idx & 7), nb = 4 * (g >> 1) + (idx >> 3);
    const int tt = wid & 3, up = wid >> 2, r = lane & 31, h = lane >> 5;
    lptr lds = c.lds;
    if (tid < P1_R * 64) { const int sl = tid >> 6, k = tid & 63;
        *(LAS u32x4*)(lds + sl * P1_SLOT + 16384 + k * 256 + ((15 ^ (4 * (k & 3))) * 16)) = (u32x4){0x3f803f80u, 0x3f803f80u, 0x3f803f80u, 0x3f803f80u}; }
    const int xr = 16 * wid + (lane >> 3), xc = (lane & 7) ^ ((xr >> 1) & 7);
    const unsigned xoff = (pb * 128 + xr) * 1280 + xc * 16;
    const int wkk = 8 * wid + (lane >> 4), wp = (lane & 15) ^ (4 * (wkk & 3)), wpc = wp < 15 ? wp : 14;
    const unsigned woff = wkk * 3840 + nb * 240 + wpc * 16;
    f32x16 acc0 = {}, acc1 = {};
    const int rho = (pb + 3 * nb) % P1_NS;
    const unsigned put = up ? WS_COLP + (unsigned)(nb * 16 + pb) * 1024 : WS_ROWP + (unsigned)(pb * 16 + nb) * 1024;
    const int gi = (wid & 1) * 64 + lane;
    const unsigned get = ((wid & 2) ? WS_COLP + (unsigned)nb * 16384 : WS_ROWP + (unsigned)pb * 16384) + gi * 8;
    u32x2 part[10];
    if (up == 0) p1_loop<0>(c, xoff, woff, tt, rho, put, get, acc0, acc1, part);
    else p1_loop<1>(c, xoff, woff, tt, rho, put, get, acc0, acc1, part);
    float psum = 0.f;
    if (up == 0) {
        for (unsigned spins = 0; spins < SPIN_CAP; ++spins) {
            bool ok = true;
#pragma unroll
            for (int i = 0; i < 10; ++i) ok &= part[i].y == c.tag;
            if (__all(ok)) break;
            POLL_PAUSE();
#pragma unroll
            for (int i = 0; i < 10; ++i) part[i] = lg64(c.rs, get + i * 1024);
        }
#pragma unroll
        for (int i = 0; i < 10; ++i) psum += __uint_as_float(part[i].x);
    }
    LDS_BAR();
    LAS float* st = (LAS float*)(lds + P1_ST); LAS float* cs = (LAS float*)(lds + P1_CS);
    if (wid < 2) st[gi * 2 + 1] = psum; else if (wid < 4) cs[gi] = psum;
    if (up == 1 && h == 0) st[(tt * 32 + r) * 2] = acc1[12];
    LDS_BAR();
    { const f32x2 sv = *(const LAS f32x2*)(st + (tt * 32 + r) * 2);
      const float mean = sv.x * (1.0f / D), var = fmaxf(sv.y * (1.0f / D) - mean * mean, 0.f), rstd = __builtin_amdgcn_rsqf(var + LN_EPS), rm = rstd * mean;
      lptr stg = lds + P1_OUT + (32 * tt + r) * P1_STG;
#pragma unroll
      for (int u = 0; u < 2; ++u)
#pragma unroll
        for (int gq = 0; gq < 4; ++gq) {
            const int col = (2 * up + u) * 32 + 8 * gq + 4 * h;
            const f32x4 c4 = *(const LAS f32x4*)(cs + col);
            const float sc = (nb * 120 + col < D) ? QSCALE : 1.0f, a = rstd * sc, bb = rm * sc;
            float v[4];
#pragma unroll
            for (int e = 0; e < 4; ++e) v[e] = a * (u ? acc1[4 * gq + e] : acc0[4 * gq + e]) - bb * c4[e];
            *(LAS u32x2*)(stg + col * 2) = (u32x2){cvtpk(v[0], v[1]), cvtpk(v[2], v[3])};
        }
    }
    LDS_BAR();
#pragma unroll
    for (int j = 0; j < 3; ++j) {
        const int slot = up + 2 * j, gidx = 6 * nb + slot, type = gidx >> 5, head = gidx & 31, tile = 4 * pb + tt;
        if (type < 2) {
            lcptr row = (lcptr)lds + P1_OUT + (32 * tt + r) * P1_STG + slot * 40;
            const u32x2 a0 = *(const LAS u32x2*)(row + 16 * h), a1 = *(const LAS u32x2*)(row + 16 * h + 8), a2 = *(const LAS u32x2*)(row + 32);
            const unsigned dst = type ? WS_KV + (unsigned)(head * 64 + tile) * 3072 : WS_Q + (unsigned)(head * 64 + tile) * 1536;
            st128(c.rs, dst + lane * 16, (u32x4){a0.x, a0.y, a1.x, a1.y});
            st64(c.rs, dst + 1024 + lane * 8, h ? (u32x2){type ? 0x00003f80u : 0u, 0u} : a2);
        } else {
            const int q = (lane >> 2) & 3, p = lane & 3, cg = (lane >> 4) & 1;
            lcptr vb = (lcptr)lds + P1_OUT + (32 * tt + 4 * h + q) * P1_STG + slot * 40 + (cg * 4 + p < 5 ? (cg * 4 + p) * 8 : 0);
            const unsigned dst = WS_KV + (unsigned)(head * 64 + tile) * 3072 + 1536;
#pragma unroll
            for (int s = 0; s < 2; ++s) {
                const u32x2 a = __builtin_bit_cast(u32x2, ldtr(vb + (16 * s) * P1_STG)), e = __builtin_bit_cast(u32x2, ldtr(vb + (16 * s + 8) * P1_STG));
                u32x4 v = {a.x, a.y, e.x, e.y};
                if (r == 20) v = (u32x4){0x3f803f80u, 0x3f803f80u, 0x3f803f80u, 0x3f803f80u};
                if (r > 20) v = (u32x4){0u, 0u, 0u, 0u};
                if (r < 24) st128(c.rs, dst + s * 768 + h * 384 + r * 16, v);
            }
        }
    }
}

constexpr int PK_OFF = 102400;
struct PkPlan { const char* src[4]; unsigned soff[4], dst[4], cs[4]; };
DI void pk_plan(const Ctx& c, PkPlan& p) {
    const int lane = c.lane, w = c.wid, g = lane >> 4, i16 = lane & 15, row = 8 * w + (lane >> 3), pc = lane & 7;
    const char *p1 = c.w1, *p3 = c.w3, *p2 = c.w2, *po = c.wo;
    asm("" : "+s"(p1), "+s"(p3), "+s"(p2), "+s"(po));
#pragma unroll
    for (int i = 0; i < 4; ++i) {
        int j = c.b + 256 * i; j = __builtin_amdgcn_readfirstlane(j < 910 ? j : j - 256);
        const bool w13 = j < 540, isw2 = j < 810; const int mat = j >= 270;
        const int jj = j - (w13 ? 270 * mat : isw2 ? 540 : 810), nb = w13 ? 27 : 10, kb = w13 ? jj / 27 : jj / 10, cb = jj - nb * kb;
        const int kp = 64 * kb + row, G = kp >> 3, e = kp & 7;
        const int k = isw2 ? kp : G < 64 ? 20 * (G >> 1) + 8 * (G & 1) + e : 40 * (G - 64) + 16 + (e < 4 ? e : e + 16);
        p.src[i] = j < 270 ? p1 : j < 540 ? p3 : isw2 ? p2 : po;
        p.soff[i] = (unsigned)k * (w13 ? 3456 : 1280) + cb * 128 + pc * 16;
        const unsigned ct = 4 * cb + (w >> 1);
        p.dst[i] = w13 ? WS_W13P + (ct * 40 + 4 * kb + 2 * (w & 1) + (g >> 1)) * 1024 + mat * 256 + ((g & 1) * 32 + i16) * 16
                       : (isw2 ? WS_W2P : WS_WOP) + (ct * (isw2 ? 54 : 20) + 2 * kb + (w & 1)) * 1024 + lane * 16;
        p.cs[i] = w13 && i16 == 0 ? WS_CSP + (unsigned)((mat * 20 + 2 * kb + (w & 1)) * F + 16 * ct) * 4 + g * 16 : 0x7fffff00u;
    }
}
template <int I> DI void repack_issue_one(const Ctx& c, const PkPlan& p) { dma16<false>(p.src[I], p.soff[I], c.lds0 + PK_OFF + I * 8192 + c.wid * 1024); }
template <int I> DI void repack_store_one(const Ctx& c, const PkPlan& p, unsigned a) {
    const s16x8 v = cat(ldtr((lcptr)c.lds + a + I * 8192), ldtr((lcptr)c.lds + a + I * 8192 + 512));
    st128(c.rs, p.dst[I], __builtin_bit_cast(u32x4, v));
    const s16x8 ones = {0x3f80, 0x3f80, 0x3f80, 0x3f80, 0x3f80, 0x3f80, 0x3f80, 0x3f80};
    st128(c.rs, p.cs[I], __builtin_bit_cast(u32x4, MFMA16(v, ones, (f32x4){})));
}

constexpr int P2_R = 4, P2_SLOT = 24576, P2_NS = 8;
constexpr int P2_Q = PK_OFF;
constexpr int XCH = 135168;
constexpr int P2_EX = XCH;
constexpr int P4_CS = XCH + 24576;

DI void attn_safe(const Ctx& c, unsigned kv, s16x8 q0, s16x4 q1z, f32x16& o) {
    const int lane = c.lane, r = lane & 31, h = lane >> 5, vr = r < 24 ? r : r - 16;
    float m = -INFINITY; o = (f32x16){};
    for (int t = 0; t < 64; ++t) {
        const unsigned p = kv + t * 3072;
        const u32x4 k0 = lg128(c.rs, p + lane * 16); const u32x2 k1 = lg64(c.rs, p + 1024 + lane * 8);
        const u32x4 v0 = lg128(c.rs, p + 1536 + h * 384 + vr * 16), v1 = lg128(c.rs, p + 1536 + 768 + h * 384 + vr * 16);
        f32x16 s = MFMA32(__builtin_bit_cast(s16x8, k0), q0, (f32x16){}); s = MFMA32K8(__builtin_bit_cast(s16x4, k1), q1z, s);
        float tm = s[0];
#pragma unroll
        for (int i = 1; i < 16; ++i) tm = fmaxf(tm, s[i]);
        tm = swap32_max(tm);
        const float mn = fmaxf(m, tm), al = __builtin_amdgcn_exp2f(m - mn); m = mn;
#pragma unroll
        for (int i = 0; i < 16; ++i) { o[i] *= al; s[i] = __builtin_amdgcn_exp2f(s[i] - mn); }
        const u32x4 p0 = {cvtpk(s[0], s[1]), cvtpk(s[2], s[3]), cvtpk(s[4], s[5]), cvtpk(s[6], s[7])};
        const u32x4 p1 = {cvtpk(s[8], s[9]), cvtpk(s[10], s[11]), cvtpk(s[12], s[13]), cvtpk(s[14], s[15])};
        o = MFMA32(__builtin_bit_cast(s16x8, v0), __builtin_bit_cast(s16x8, p0), o); o = MFMA32(__builtin_bit_cast(s16x8, v1), __builtin_bit_cast(s16x8, p1), o);
    }
}

DI void phase2(const Ctx& c, const PkPlan& pk_) {
    const int lane = c.lane, wid = c.wid, g = c.b & 7, idx = c.b >> 3, hh = 4 * g + (idx & 3), qb = idx >> 2;
    const int qg = wid & 3, kh = wid >> 2, r = lane & 31, h = lane >> 5;
    lptr lds = c.lds;
    const char* kvsrc = c.ws + WS_KV + (size_t)hh * 64 * 3072 + wid * 3072;
    const unsigned dst0 = c.lds0 + wid * 3072, voff = lane * 16;
#define P2_ISSUE(st, slot) do { const char* s_ = kvsrc + (size_t)(st) * 24576; dma16<true>(s_, voff, dst0 + (slot)); dma16<true>(s_ + 1024, voff, dst0 + (slot) + 1024); dma16<true>(s_ + 2048, voff, dst0 + (slot) + 2048); } while (0)
    { const char* qsrc = c.ws + WS_Q + ((size_t)hh * 64 + 8 * qb + wid) * 1536;
      dma16<true>(qsrc, voff, c.lds0 + P2_Q + wid * 1536); dma16m<true>(qsrc + 1024, voff, c.lds0 + P2_Q + wid * 1536 + 1024, 0xffffffffull); }
    P2_ISSUE(0, 0); P2_ISSUE(1, P2_SLOT);
    const int vr = r < 24 ? r : 21 + (r & 1);
    const unsigned ka = 4 * kh * 3072 + lane * 16, kb = 4 * kh * 3072 + 1024 + lane * 8, va = 4 * kh * 3072 + 1536 + h * 384 + vr * 16;
#define LDK0(slot, t) ld128((lcptr)lds + (slot) + ka + (t) * 3072)
#define LDK1(slot, t) ld64((lcptr)lds + (slot) + kb + (t) * 3072)
#define LDV0(slot, t) ld128((lcptr)lds + (slot) + va + (t) * 3072)
#define LDV1(slot, t) ld128((lcptr)lds + (slot) + va + (t) * 3072 + 768)
    WAIT_BAR(3);
    P2_ISSUE(2, 2 * P2_SLOT);
    s16x8 q0[2]; s16x4 q1z[2], q1[2];
#pragma unroll
    for (int i = 0; i < 2; ++i) { q0[i] = ld128((lcptr)lds + P2_Q + (2 * qg + i) * 1536 + lane * 16); q1z[i] = ld64((lcptr)lds + P2_Q + (2 * qg + i) * 1536 + 1024 + lane * 8); }
    { const s16x8 k0 = ld128((lcptr)lds + lane * 16); const s16x4 k1 = ld64((lcptr)lds + 1024 + lane * 8);
#pragma unroll
      for (int i = 0; i < 2; ++i) { f32x16 s = MFMA32(k0, q0[i], (f32x16){}); s = MFMA32K8(k1, q1z[i], s);
          float m = s[0];
#pragma unroll
          for (int e = 1; e < 16; ++e) m = fmaxf(m, s[e]);
          m = swap32_max(m);
          q1[i] = q1z[i]; if (h) q1[i][0] = (short)(cvtpk(-m, 0.f) & 0xffffu); } }
    s16x8 kA0 = LDK0(0, 0), kB0, vA0 = LDV0(0, 0), vA1 = LDV1(0, 0), vB0, vB1 = vA1; s16x4 kA1 = LDK1(0, 0), kB1;
    f32x16 sA, sB, o[2] = {(f32x16){}, (f32x16){}};
    sA = MFMA32(kA0, q0[0], (f32x16){}); sA = MFMA32K8(kA1, q1[0], sA);
    u32x4 p0, p1 = {0u, 0u, 0u, 0u};
    int cur = 0, nxt = P2_SLOT, fre = (P2_R - 1) * P2_SLOT;
#define EX(v) __builtin_amdgcn_exp2f(v)
#define P2_STEP(C, N, K0, K1, NQ, OC, V0, OP, VP, LOADS, LAST) do { \
        OP = MFMA32(VP, __builtin_bit_cast(s16x8, p1), OP); SBAR(); \
        LOADS; SBAR(); \
        C[0] = EX(C[0]); C[1] = EX(C[1]); C[2] = EX(C[2]); C[3] = EX(C[3]); p0[0] = cvtpk(C[0], C[1]); p0[1] = cvtpk(C[2], C[3]); PIN(p0); SBAR(); \
        if (!(LAST)) N = MFMA32(K0, q0[NQ], (f32x16){}); SBAR(); \
        C[4] = EX(C[4]); C[5] = EX(C[5]); C[6] = EX(C[6]); C[7] = EX(C[7]); p0[2] = cvtpk(C[4], C[5]); p0[3] = cvtpk(C[6], C[7]); PIN(p0); SBAR(); \
        if (!(LAST)) N = MFMA32K8(K1, q1[NQ], N); SBAR(); \
        C[8] = EX(C[8]); C[9] = EX(C[9]); C[10] = EX(C[10]); C[11] = EX(C[11]); p1[0] = cvtpk(C[8], C[9]); p1[1] = cvtpk(C[10], C[11]); PIN(p1); SBAR(); \
        OC = MFMA32(V0, __builtin_bit_cast(s16x8, p0), OC); SBAR(); \
        C[12] = EX(C[12]); C[13] = EX(C[13]); C[14] = EX(C[14]); C[15] = EX(C[15]); p1[2] = cvtpk(C[12], C[13]); p1[3] = cvtpk(C[14], C[15]); PIN(p1); SBAR(); \
    } while (0)
#define P2_PIECE(st, k) do { if ((st) >= 0) dma16<true>(kvsrc + (size_t)(st) * 24576 + (k) * 1024, voff, dst0 + fre + (k) * 1024); } while (0)
#define P2_PAIR(T, NS_, NT, LAST, ST, KA, KB) do { \
        if (((T) == 0) == (kh != 0)) __builtin_amdgcn_s_setprio(1); else __builtin_amdgcn_s_setprio(0); \
        P2_STEP(sA, sB, kA0, kA1, 1, o[0], vA0, o[1], vB1, { kB0 = LDK0(cur, (T) + 1); kB1 = LDK1(cur, (T) + 1); vB0 = LDV0(cur, (T) + 1); vB1 = LDV1(cur, (T) + 1); }, false); \
        P2_STEP(sB, sA, kB0, kB1, 0, o[1], vA0, o[0], vA1, { P2_PIECE(ST, KA); }, false); \
        P2_STEP(sA, sB, kB0, kB1, 1, o[0], vB0, o[1], vA1, { if (!(LAST)) { kA0 = LDK0(NS_, NT); kA1 = LDK1(NS_, NT); vA0 = LDV0(NS_, NT); vA1 = LDV1(NS_, NT); } }, false); \
        P2_STEP(sB, sA, kA0, kA1, 0, o[1], vB0, o[0], vB1, { if ((KB) >= 0) P2_PIECE(ST, KB); }, LAST); } while (0)
#define P2_STAGE(LAST, ST) do { P2_PAIR(0, cur, 2, false, ST, 0, 1); P2_PAIR(2, nxt, 0, LAST, ST, 2, -1); \
        fre = cur; cur = nxt; nxt = (nxt == (P2_R - 1) * P2_SLOT) ? 0 : nxt + P2_SLOT; } while (0)
    const unsigned pka = PK_OFF + (wid >> 1) * 32 + (lane & 3) * 8 + (32 * (wid & 1) + 8 * (lane >> 4) + ((lane & 15) >> 2)) * 128;
#pragma unroll
    for (int s = 0; s < P2_NS - 3; ++s) {
        WAIT_BAR(s == 1 || s == 2 ? 3 + 2 : s == 3 ? 4 + 3 : 3);
        if (s == 2) { repack_store_one<0>(c, pk_, pka); repack_store_one<1>(c, pk_, pka); }
        if (s == 3) { repack_store_one<2>(c, pk_, pka); repack_store_one<3>(c, pk_, pka); }
        if (s == 4 && threadIdx.x == 0) st32(c.rs, flag_off(FL_PK, c.b), c.tag);
        P2_STAGE(false, s + P2_R - 1);
        if (s == 0) { repack_issue_one<0>(c, pk_); repack_issue_one<1>(c, pk_); }
        if (s == 1) { repack_issue_one<2>(c, pk_); repack_issue_one<3>(c, pk_); }
    }
    WAIT_BAR(3);
    unsigned pk[4];
    if (wid == 0) {
#pragma unroll
        for (int i = 0; i < 4; ++i) pk[i] = lg32(c.rs, flag_off(FL_PK, 64 * i + lane));
    }
    P2_STAGE(false, -1);
    if (wid == 0) {
        for (unsigned spins = 0; spins < SPIN_CAP; ++spins) {
            if (__all(pk[0] == c.tag && pk[1] == c.tag && pk[2] == c.tag && pk[3] == c.tag)) break;
            POLL_PAUSE();
#pragma unroll
            for (int i = 0; i < 4; ++i) pk[i] = lg32(c.rs, flag_off(FL_PK, 64 * i + lane));
        }
    }
    WAIT_BAR(0);
    const unsigned wsc = c.lds0 + PK_OFF + wid * 1024;
    unsigned wu[6];
#pragma unroll
    for (int i = 0; i < 6; ++i) { const int L = imin((idx * 8 + wid) * 6 + i, 1519);
        wu[i] = (L < 400 ? WS_WOP + (g & 1) * 409600 + L * 1024 : WS_W13P + imin((g >> 1) * 1146880 + (L - 400) * 1024, 108 * 40960 - 1024)) + lane * 16; }
#define P2_WARM(i) dma16<true>(c.ws, wu[i], wsc)
    P2_PAIR(0, cur, 2, false, -1, 0, 1); P2_WARM(0); P2_WARM(1); P2_PAIR(2, nxt, 0, false, -1, 2, -1); P2_WARM(2);
    fre = cur; cur = nxt; nxt = (nxt == (P2_R - 1) * P2_SLOT) ? 0 : nxt + P2_SLOT;
    P2_WARM(3); P2_PAIR(0, cur, 2, false, -1, 0, 1); P2_WARM(4); P2_WARM(5); P2_PAIR(2, nxt, 0, true, -1, 2, -1);
#undef P2_WARM
    o[1] = MFMA32(vB1, __builtin_bit_cast(s16x8, p1), o[1]);
#undef P2_STEP
#undef P2_PAIR
#undef P2_PIECE
#undef P2_STAGE
#undef P2_ISSUE
    __builtin_amdgcn_s_setprio(0);
    LAS f32x4* ex = (LAS f32x4*)(lds + P2_EX);
    { const unsigned m = kh ? 0xffffffffu : 0u;
#pragma unroll
      for (int i = 0; i < 3; ++i) { const f32x4 a = {o[0][4 * i], o[0][4 * i + 1], o[0][4 * i + 2], o[0][4 * i + 3]}, e = {o[1][4 * i], o[1][4 * i + 1], o[1][4 * i + 2], o[1][4 * i + 3]};
          ex[(wid * 3 + i) * 64 + lane] = sel4(m, a, e); }
      LDS_BAR();
      f32x16 of = {};
#pragma unroll
      for (int i = 0; i < 3; ++i) { const f32x4 a = {o[0][4 * i], o[0][4 * i + 1], o[0][4 * i + 2], o[0][4 * i + 3]}, e = {o[1][4 * i], o[1][4 * i + 1], o[1][4 * i + 2], o[1][4 * i + 3]};
          const f32x4 t = sel4(m, e, a) + ex[((wid ^ 4) * 3 + i) * 64 + lane];
          of[4 * i] = t[0]; of[4 * i + 1] = t[1]; of[4 * i + 2] = t[2]; of[4 * i + 3] = t[3]; }
      float l = upper_half(of[8]);
      bool bad = !(l < 1.2676506e30f && l > 8.6736174e-19f);
#pragma unroll
      for (int i = 0; i < 12; ++i) bad |= !(fabsf(of[i]) < 3.0e38f);
      if (__any(bad)) {
          const unsigned qo = WS_Q + (unsigned)(hh * 64 + 8 * qb + 2 * qg + kh) * 1536;
          const s16x8 qq0 = __builtin_bit_cast(s16x8, lg128(c.rs, qo + lane * 16)); const s16x4 qq1 = __builtin_bit_cast(s16x4, lg64(c.rs, qo + 1024 + lane * 8));
          attn_safe(c, WS_KV + (unsigned)hh * 64 * 3072, qq0, qq1, of); l = upper_half(of[8]); }
      const float il = __builtin_amdgcn_rcpf(l);
      const unsigned a0 = cvtpk(of[0] * il, of[1] * il), a1 = cvtpk(of[2] * il, of[3] * il), b0 = cvtpk(of[4] * il, of[5] * il), b1 = cvtpk(of[6] * il, of[7] * il);
      auto x0 = __builtin_amdgcn_permlane32_swap(a0, b0, false, false), x1 = __builtin_amdgcn_permlane32_swap(a1, b1, false, false);
      const unsigned t16 = 2 * (8 * qb + 2 * qg + kh) + (r >> 4);
      st128(c.rs, WS_O + (t16 * 80 + 2 * hh + h) * 256 + (r & 15) * 16, (u32x4){x0[0], x1[0], x0[1], x1[1]});
      if (h == 0) st64(c.rs, WS_O + (t16 * 80 + 64 + (hh >> 1)) * 256 + (r & 15) * 16 + (hh & 1) * 8, (u32x2){cvtpk(of[8] * il, of[9] * il), cvtpk(of[10] * il, of[11] * il)});
    }
#undef LDK0
#undef LDK1
#undef LDV0
#undef LDV1
#undef EX
}

constexpr int PJ_R = 5;
template <int NKS, int KSS> struct PJ {
    static constexpr int NS = NKS / KSS, SLOT = 5 * KSS * 1024, NWI = (5 * KSS + 7) / 8, NA = NKS / 2, PRE = imin(PJ_R, NS);
};
template <int NKS, int KSS> DI void pj_issue_w(const Ctx& c, const unsigned (&voff)[(5 * KSS + 7) / 8], int st) {
    using P = PJ<NKS, KSS>;
    const char* base = c.ws + st * (KSS * 1024);
#pragma unroll
    for (int i = 0; i < P::NWI; ++i) dma16<true>(base, voff[i], c.lds0 + (st % PJ_R) * P::SLOT + imin(c.wid + 8 * i, 5 * KSS - 1) * 1024);
}
template <int NKS, int KSS> DI void pj_voff(const Ctx& c, unsigned wbase, int cc, unsigned (&voff)[(5 * KSS + 7) / 8]) {
#pragma unroll
    for (int i = 0; i < PJ<NKS, KSS>::NWI; ++i) { const int f = imin(c.wid + 8 * i, 5 * KSS - 1); voff[i] = wbase + (unsigned)((5 * cc + f / KSS) * NKS + f % KSS) * 1024 + c.lane * 16; }
}
DI int pj_pp(int b) { return 8 * ((b & 7) >> 1) + ((b >> 3) & 7); }
DI int pj_cc(int b) { return 4 * (b & 1) + (b >> 6); }
template <int NKS, int KSS> DI void pj_prefetch(const Ctx& c, unsigned wbase) {
    unsigned voff[PJ<NKS, KSS>::NWI]; pj_voff<NKS, KSS>(c, wbase, pj_cc(c.b), voff);
#pragma unroll
    for (int s = 0; s < PJ<NKS, KSS>::PRE; ++s) pj_issue_w<NKS, KSS>(c, voff, s);
}

template <int NA, int WIN, int HK, int NS, int NWI, int NRES> constexpr int pj_younger(int j) {
    int n = imin(NA - 1, j + WIN) - j;
    if (j < WIN) n += NRES;
    for (int s = 1; s + PJ_R - 1 < NS; ++s)
        if (HK * s <= j && (j < WIN || HK * s > j - WIN)) n += NWI;
    return n;
}
template <int NA, int WIN, int HK, int NS, int NWI> constexpr int pj_bar_younger(int s) {
    const int s0 = s + 2 - PJ_R;
    int n = 0;
    for (int t = s0 + 1; t < s; ++t) if (t + PJ_R - 1 < NS) n += NWI;
    for (int j = HK * s0; j < HK * s; ++j) if (j + WIN < NA) ++n;
    return n;
}
template <int NKS, int KSS, bool FIRST, int KH>
DI void pj_body(const Ctx& c, unsigned abase, unsigned wbase, int fc, u32x2 (&res)[3]) {
    using P = PJ<NKS, KSS>;
    constexpr int NS = P::NS, NG = NKS * 4, NA = P::NA, HK = KSS / 2, NCT = KH ? 2 : 3, CT0 = KH ? 3 : 0;
    const int lane = c.lane, wid = c.wid, t16l = wid & 3, r = lane & 15, gq = lane >> 4, pp = pj_pp(c.b), cc = pj_cc(c.b);
    lptr lds = c.lds;
    const unsigned t16 = 4 * pp + t16l;
    const int tok = 16 * t16 + r;
    constexpr int WIN = imin(NA, FIRST ? 4 : 9);
    s16x8 a[NA];
    const unsigned aoff = abase + (t16 * NG + 4 * KH) * 256 + lane * 16;
#define PJ_ABASE(j) (c.ws + (j) * 2048)
#pragma unroll
    for (int j = 0; j < WIN; ++j) ldg_async(a[j], PJ_ABASE(j), aoff);
    if (FIRST) {
#pragma unroll
        for (int i = 0; i < NCT; ++i) res[i] = *(const GAS u32x2*)(c.x + (size_t)tok * 1280 + cc * 160 + (CT0 + i) * 32 + gq * 8);
    }
    constexpr int NCS = FIRST && KH ? 20 : 0;
    unsigned csv[NCS ? NCS : 1];
    const int cst = wid * 64 + lane - 256;
    if (NCS) { const int mat = cst >= 112, col = imin(fc * 112 + imin(cst, 223) - 112 * mat, F - 1);
#pragma unroll
        for (int i = 0; i < NCS; ++i) csv[i] = lg32(c.rs, WS_CSP + (unsigned)((mat * 20 + i) * F + col) * 4); }
    unsigned voff[P::NWI]; pj_voff<NKS, KSS>(c, wbase, cc, voff);
    constexpr int NRES = (FIRST ? NCT : 0) + NCS;
    WAIT_BARV(WIN + NRES);
    const unsigned wa = KH * 1024 + lane * 16;
#define PJ_READ(W, st, i) do { lcptr b_ = (lcptr)lds + ((st) % PJ_R) * P::SLOT + wa + (i) * 2048; \
        _Pragma("unroll") for (int ct = 0; ct < 5; ++ct) W[ct] = ld128(b_ + ct * KSS * 1024); } while (0)
    f32x4 acc[5];
#pragma unroll
    for (int ct = 0; ct < 5; ++ct) acc[ct] = (f32x4){};
    s16x8 w[2][5];
    PJ_READ(w[0], 0, 0);
#pragma unroll
    for (int s = 0; s < NS; ++s) {
        if (NS > PJ_R && s >= 1) {
            if (s + 1 >= PJ_R && s + 1 < NS) WAIT_BAR((pj_bar_younger<NA, WIN, HK, NS, P::NWI>(s))); else LDS_BAR();
            SBAR();
            if (s + PJ_R - 1 < NS) pj_issue_w<NKS, KSS>(c, voff, s + PJ_R - 1);
        }
#pragma unroll
        for (int i = 0; i < HK; ++i) {
            const int j = s * HK + i;
            if (i + 1 < HK) PJ_READ(w[(j + 1) & 1], s, i + 1); else if (s + 1 < NS) PJ_READ(w[(j + 1) & 1], s + 1, 0);
            if (j + WIN < NA) ldg_async(a[j + WIN], PJ_ABASE(j + WIN), aoff);
            WAIT_USE((pj_younger<NA, WIN, HK, NS, P::NWI, NRES>(j)), a[j]);
            SBAR();
#pragma unroll
            for (int ct = 0; ct < 5; ++ct) acc[ct] = MFMA16(w[j & 1][ct], a[j], acc[ct]);
            SBAR();
        }
    }
#undef PJ_READ
#undef PJ_ABASE
    if (NCS) { float s = 0.f;
#pragma unroll
        for (int i = 0; i < NCS; ++i) s += __uint_as_float(csv[i]);
        if (cst < 224) *(LAS float*)(lds + P4_CS + ((cst >= 112) * 128 + cst - 112 * (cst >= 112)) * 4) = s; }
    LDS_BAR();
    LAS f32x4* red = (LAS f32x4*)(lds + XCH);
#pragma unroll
    for (int i = 0; i < 5 - NCT; ++i) red[(wid * 3 + i) * 64 + lane] = acc[(KH ? 0 : 3) + i];
    LDS_BAR();
    float s1 = 0.f, s2 = 0.f;
#pragma unroll
    for (int i = 0; i < NCT; ++i) {
        const f32x4 o = red[((wid ^ 4) * 3 + i) * 64 + lane], m = acc[CT0 + i];
        const unsigned w0 = cvtpk(bflo(res[i].x) + m[0] + o[0], bfhi(res[i].x) + m[1] + o[1]), w1 = cvtpk(bflo(res[i].y) + m[2] + o[2], bfhi(res[i].y) + m[3] + o[3]);
        if (FIRST) {
            res[i] = (u32x2){w0, w1};
            st64(c.rs, WS_H + (t16 * 80 + 10 * cc + 2 * (CT0 + i) + (gq >> 1)) * 256 + r * 16 + (gq & 1) * 8, res[i]);
            const float a_ = bflo(w0), b_ = bfhi(w0), c_ = bflo(w1), d = bfhi(w1); s1 += (a_ + b_) + (c_ + d); s2 += (a_ * a_ + b_ * b_) + (c_ * c_ + d * d);
        } else {
            *(GAS u32x2*)(c.out + (size_t)tok * 1280 + cc * 160 + (CT0 + i) * 32 + gq * 8) = (u32x2){w0, w1};
        }
    }
    if (FIRST) {
        s1 += __shfl_xor(s1, 16); s2 += __shfl_xor(s2, 16); s1 = swap32_add(s1); s2 = swap32_add(s2);
        LAS f32x2* ex = (LAS f32x2*)(lds + XCH + 2048);
        if (KH == 1 && gq == 0) ex[t16l * 16 + r] = (f32x2){s1, s2};
        LDS_BAR();
        if (KH == 0 && gq == 0) { const f32x2 e = ex[t16l * 16 + r];
            st64(c.rs, WS_HST + ((t16 * 8 + cc) * 16 + r) * 8, (u32x2){__float_as_uint(s1 + e.x), __float_as_uint(s2 + e.y)}); }
    }
}
template <int NKS, int KSS, bool FIRST>
DI void phase_proj(const Ctx& c, unsigned abase, unsigned wbase, int fc, u32x2 (&res)[3]) {
    if (c.wid < 4) pj_body<NKS, KSS, FIRST, 0>(c, abase, wbase, fc, res);
    else pj_body<NKS, KSS, FIRST, 1>(c, abase, wbase, fc, res);
}

constexpr int P4_R = 7, P4_SLOT = 22528, P4_NS = 20;

DI void p4_wvoff(const Ctx& c, int fc, unsigned& woff0, unsigned& woff1) {
    const unsigned o0 = c.wid * 1792 + c.lane * 16, o1 = c.wid * 1792 + 1024 + (c.lane < 48 ? c.lane : 47) * 16;
    woff0 = WS_W13P + (unsigned)imin(7 * fc + (o0 >> 11), 107) * 40960 + (o0 & 2047); woff1 = WS_W13P + (unsigned)imin(7 * fc + (o1 >> 11), 107) * 40960 + (o1 & 2047);
}
DI void p4_issue_w(const Ctx& c, unsigned woff0, unsigned woff1, int st) {
    const char* w_ = c.ws + st * 2048; const unsigned wdst = c.lds0 + 8192 + c.wid * 1792 + (st % P4_R) * P4_SLOT;
    dma16<true>(w_, woff0, wdst); dma16m<true>(w_, woff1, wdst + 1024, 0xffffffffffffull);
}
DI void p4_prefetch(const Ctx& c, int fc) {
    unsigned woff0, woff1; p4_wvoff(c, fc, woff0, woff1);
#pragma unroll
    for (int s = 0; s < P4_R - 1; ++s) p4_issue_w(c, woff0, woff1, s);
}
template <int UG>
DI void p4_body(const Ctx& c, int qb, int fc) {
    constexpr int NU = UG ? 3 : 4, U0 = UG ? 4 : 0;
    const int lane = c.lane, wid = c.wid, tt = wid & 3, r = lane & 31, h = lane >> 5;
    lptr lds = c.lds;
    const unsigned adst = c.lds0 + wid * 1024;
    const unsigned aoff = WS_H + (unsigned)(8 * qb + wid) * 80 * 256 + lane * 16;
    unsigned woff0, woff1; p4_wvoff(c, fc, woff0, woff1);
#pragma unroll
    for (int s = 0; s < P4_R - 1; ++s) dma16<true>(c.ws + s * 1024, aoff, adst + s * P4_SLOT);
    const unsigned xa = ((2 * tt + (r >> 4)) * 4 + h) * 256 + (r & 15) * 16, wa = 8192 + U0 * 2048 + lane * 16;
    const unsigned t16 = 8 * qb + 2 * tt + (r >> 4);
    u32x2 hs[4];
#pragma unroll
    for (int i = 0; i < 4; ++i) hs[i] = lg64(c.rs, WS_HST + ((t16 * 8 + 4 * h + i) * 16 + (r & 15)) * 8);
#define P4_PIECE(st, k) do { const unsigned sl_ = ((st) % P4_R) * P4_SLOT; \
        if ((k) == 0) dma16<true>(c.ws + (st) * 1024, aoff, adst + sl_); \
        if ((k) == 1) dma16<true>(c.ws + (st) * 2048, woff0, c.lds0 + 8192 + wid * 1792 + sl_); \
        if ((k) == 2) dma16m<true>(c.ws + (st) * 2048, woff1, c.lds0 + 8192 + wid * 1792 + sl_ + 1024, 0xffffffffffffull); } while (0)
#define P4_READ(n) do { lcptr b_ = (lcptr)lds + (((n) >> 1) % P4_R) * P4_SLOT; X[(n) % 3] = ld128(b_ + xa + ((n) & 1) * 512); \
        _Pragma("unroll") for (int u = 0; u < NU; ++u) W[(n) % 3][u] = ld128(b_ + wa + u * 2048 + ((n) & 1) * 1024); } while (0)
    f32x16 acc[NU];
#pragma unroll
    for (int u = 0; u < NU; ++u) acc[u] = (f32x16){};
    WAIT_BAR((P4_R - 2) + 4);
    s16x8 X[3], W[3][NU];
    P4_READ(0); P4_READ(1);
#pragma unroll
    for (int s = 0; s < P4_NS; ++s) {
        WAIT_BARV(fly2(s, P4_R, P4_NS, 1, 2) + (s + 1 < P4_R - 1 ? 4 : 0));
        SBAR();
#define P4_NEXT(k) do { if (s + P4_R - 1 < P4_NS) P4_PIECE(s + P4_R - 1, k); SBAR(); } while (0)
#pragma unroll
        for (int ks = 0; ks < 2; ++ks) {
            const int n = 2 * s + ks;
            if (n + 2 < 2 * P4_NS) P4_READ(n + 2);
            SBAR();
            acc[0] = MFMA32(W[n % 3][0], X[n % 3], acc[0]); SBAR(); P4_NEXT(ks);
            acc[1] = MFMA32(W[n % 3][1], X[n % 3], acc[1]); acc[2] = MFMA32(W[n % 3][2], X[n % 3], acc[2]); SBAR();
            if (ks) P4_NEXT(2);
            if (NU > 3) acc[NU - 1] = MFMA32(W[n % 3][NU - 1], X[n % 3], acc[NU - 1]);
            SBAR();
        }
#undef P4_NEXT
    }
#undef P4_READ
    LDS_BAR();
    float s1 = (__uint_as_float(hs[0].x) + __uint_as_float(hs[1].x)) + (__uint_as_float(hs[2].x) + __uint_as_float(hs[3].x));
    float s2 = (__uint_as_float(hs[0].y) + __uint_as_float(hs[1].y)) + (__uint_as_float(hs[2].y) + __uint_as_float(hs[3].y));
    s1 = swap32_add(s1); s2 = swap32_add(s2);
    const float mean = s1 * (1.0f / D), var = fmaxf(s2 * (1.0f / D) - mean * mean, 0.f), rstd = __builtin_amdgcn_rsqf(var + LN_EPS);
    const f32x2 nm = {-mean, -mean}, k1 = {-1.4426950408889634f * rstd, -1.4426950408889634f * rstd}, r2 = {rstd * rstd, rstd * rstd}, one = {1.0f, 1.0f};
    const LAS float* csl = (const LAS float*)(lds + P4_CS);
#pragma unroll
    for (int u = 0; u < NU; ++u) {
        if (7 * fc + U0 + u < 108) {
            u32x2 w[2];
#pragma unroll
            for (int gq = 0; gq < 2; ++gq) {
                const f32x4 cg_ = *(const LAS f32x4*)(csl + (U0 + u) * 16 + 8 * gq + 4 * h), cu = *(const LAS f32x4*)(csl + 128 + (U0 + u) * 16 + 8 * gq + 4 * h);
                f32x2 o[2];
#pragma unroll
                for (int e = 0; e < 2; ++e) {
                    const f32x2 a = __builtin_elementwise_fma(nm, (f32x2){cg_[2 * e], cg_[2 * e + 1]}, (f32x2){acc[u][4 * gq + 2 * e], acc[u][4 * gq + 2 * e + 1]});
                    const f32x2 b2 = __builtin_elementwise_fma(nm, (f32x2){cu[2 * e], cu[2 * e + 1]}, (f32x2){acc[u][8 + 4 * gq + 2 * e], acc[u][8 + 4 * gq + 2 * e + 1]});
                    const f32x2 t = a * k1, d = (f32x2){__builtin_amdgcn_exp2f(t.x), __builtin_amdgcn_exp2f(t.y)} + one;
                    const f32x2 rc = {__builtin_amdgcn_rcpf(d.x), __builtin_amdgcn_rcpf(d.y)};
                    o[e] = (a * b2) * (rc * r2);
                }
                w[gq] = (u32x2){cvtpk(o[0].x, o[0].y), cvtpk(o[1].x, o[1].y)};
            }
            auto x0 = __builtin_amdgcn_permlane32_swap(w[0].x, w[1].x, false, false), x1 = __builtin_amdgcn_permlane32_swap(w[0].y, w[1].y, false, false);
            st128(c.rs, WS_HM + (t16 * 216 + 14 * fc + 2 * (U0 + u) + h) * 256 + (r & 15) * 16, (u32x4){x0[0], x1[0], x0[1], x1[1]});
        }
    }
}
DI void phase4(const Ctx& c, int qb, int fc) {
    if (c.wid < 4) p4_body<0>(c, qb, fc); else p4_body<1>(c, qb, fc);
}
__global__ __launch_bounds__(512, 2) void block_fwd(const char* __restrict__ x, const char* __restrict__ wqkv, const char* __restrict__ wo, const char* __restrict__ w1,
                                                    const char* __restrict__ w3, const char* __restrict__ w2, char* __restrict__ out, char* __restrict__ ws) {
    extern __shared__ __attribute__((aligned(16))) char smem[];
    {
        unsigned long long pc; asm volatile("s_getpc_b64 %0" : "=s"(pc));
        const unsigned wv = __builtin_amdgcn_readfirstlane(threadIdx.x >> 6), piece = ((blockIdx.x >> 3) * 8 + wv) * 1024;
        dma16<false>((const char*)(pc & ~1023ull), (piece < CODE_BYTES ? piece : CODE_BYTES - 1024) + (threadIdx.x & 63) * 16, (unsigned)(uintptr_t)smem + 4 * P1_SLOT + wv * 2048);
    }
    Ctx c; c.x = x; c.wqkv = wqkv; c.wo = wo; c.w1 = w1; c.w3 = w3; c.w2 = w2; c.out = out; c.ws = ws;
    c.rs = __builtin_amdgcn_make_buffer_rsrc(ws, 0, WS_END, 0x00020000);
    c.lds = (lptr)smem; c.lds0 = (unsigned)(uintptr_t)smem; c.b = blockIdx.x; c.lane = threadIdx.x & 63; c.wid = __builtin_amdgcn_readfirstlane(threadIdx.x >> 6);
    const int lane = c.lane, g = c.b & 7, idx = c.b >> 3;
    c.tag = *(const __attribute__((address_space(4))) unsigned*)(ws + WS_E);
    u32x2 res[3] = {(u32x2){0u, 0u}, (u32x2){0u, 0u}, (u32x2){0u, 0u}};
    const int qb4 = 8 * (g & 1) + (idx & 7), fc = 4 * (g >> 1) + (idx >> 3), pp = pj_pp(c.b);

    phase1(c);
    WAIT_VM(0);
    publish(c, FL_KV);

    { const int hh = 4 * g + (idx & 3), qb = idx >> 2;
      const int wg = lane < 16 ? wg_p1(lane, (32 + hh) / 6) : lane < 32 ? wg_p1(lane - 16, (64 + hh) / 6) : wg_p1(2 * qb + (lane & 1), hh / 6);
      const unsigned off[1] = {flag_off(FL_KV, wg)};
      PkPlan plan; pk_plan(c, plan);
#pragma unroll
      for (int i = 0; i < 4; ++i) { PIN(plan.soff[i]); PIN(plan.dst[i]); PIN(plan.cs[i]); asm volatile("" : "+s"(plan.src[i])); }
      wait_flags(c, off);
      phase2(c, plan); }
    WAIT_VM(0);
    publish(c, FL_O);
    pj_prefetch<20, 4>(c, WS_WOP);

    { const unsigned off[1] = {flag_off(FL_O, wg_p2(lane & 31, pp >> 2))}; wait_flags(c, off); }
    phase_proj<20, 4, true>(c, WS_O, WS_WOP, fc, res);
    WAIT_VM(0);
    publish(c, FL_H);
    p4_prefetch(c, fc);

    { const unsigned off[1] = {flag_off(FL_H, wg_p3(2 * qb4 + (lane & 1), (lane >> 1) & 7))}; wait_flags(c, off); }
    phase4(c, qb4, fc);
    WAIT_VM(0);
    publish(c, FL_M);
    pj_prefetch<54, 6>(c, WS_W2P);

    { const unsigned fm = flag_off(FL_M, wg_p4(pp >> 1, lane & 15));
      if (c.b == 0) {
          const unsigned off[5] = {fm, flag_off(FL_O, lane), flag_off(FL_O, 64 + lane), flag_off(FL_O, 128 + lane), flag_off(FL_O, 192 + lane)}; wait_flags(c, off);
      } else { const unsigned off[1] = {fm}; wait_flags(c, off); } }
    phase_proj<54, 6, false>(c, WS_HM, WS_W2P, fc, res);
    if (c.b == 0 && threadIdx.x == 0) st32(c.rs, WS_E, c.tag);
}

}

extern "C" void kernel_launch(void* const* d_in, const int* in_sizes, int n_in, void* d_out, int out_size, void* d_ws, size_t ws_size, hipStream_t stream) {
    using namespace blk;
    const char* x = (const char*)d_in[0]; const char* wqkv = (const char*)d_in[1]; const char* wo = (const char*)d_in[2];
    const char* w1 = (const char*)d_in[4]; const char* w3 = (const char*)d_in[5]; const char* w2 = (const char*)d_in[6];
    char* ws = (char*)d_ws; char* out = (char*)d_out;
    hipFuncSetAttribute((const void*)block_fwd, hipFuncAttributeMaxDynamicSharedMemorySize, LDS_BYTES);
    block_fwd<<<256, 512, LDS_BYTES, stream>>>(x, wqkv, wo, w1, w3, w2, out, ws);
}
```
